```python
import torch

def _softmax(x, axis=-1):
    return torch.softmax(x, dim=axis)

def _gelu(x):
    return torch.nn.functional.gelu(x, approximate='tanh')

def _silu(x):
    return torch.nn.functional.silu(x)

def _sigmoid(x):
    return torch.sigmoid(x)

def _layernorm(x, axis=-1, eps=1e-05):
    m = x.mean(dim=axis, keepdim=True)
    v = x.var(dim=axis, keepdim=True, unbiased=False)
    return (x - m) / torch.sqrt(v + eps)

def _scatter_add(tgt, idx, src, axis=0):
    return tgt.clone().index_add_(axis, idx, src)

def _segment_sum(src, seg, n):
    out = torch.zeros((n,) + src.shape[1:], dtype=src.dtype)
    return out.index_add_(0, seg, src)

def _pad(x, pw):
    flat = tuple((v for pair in reversed(pw) for v in pair))
    return torch.nn.functional.pad(x, flat)
kernel_name = 'adaLN_cond__compressed_kv_cla__glu_gated_reglu__prompt'

def setup_inputs():

    _w = lambda a: (lambda t: t.to(torch.bfloat16) if t.is_floating_point() else t)(torch.as_tensor(a))

    def _A_setup():
        B, H, Sq, Sk, D, M = (1, 32, 2048, 2048, 128, 512)
        Dc = 4 * D
        return {'x': _w((lambda *s: torch.randn(*s, dtype=torch.bfloat16) * 0.05)(B, Sq, M)), 'c_kv': _w((lambda *s: torch.randn(*s, dtype=torch.bfloat16) * 0.8)(B, Sk, Dc)), 'w_q': _w((lambda *s: torch.randn(*s, dtype=torch.bfloat16) * 1.6)(M, H, D) * M ** (-0.5)), 'w_uk': _w((lambda *s: torch.randn(*s, dtype=torch.bfloat16) * 1.6)(Dc, H, D) * Dc ** (-0.5)), 'w_uv': _w((lambda *s: torch.randn(*s, dtype=torch.bfloat16) * 0.8)(Dc, H, D) * Dc ** (-0.5)), 'w_o': _w((lambda *s: torch.randn(*s, dtype=torch.bfloat16) * 0.8)(H, D, M) * (H * D) ** (-0.5))}
    _A_in = _A_setup()

    def _B_setup():
        B, S, D, F = (1, 2048, 512, 1792)
        return {'x': _w((lambda *s: torch.randn(*s, dtype=torch.bfloat16) * 0.05)(B, S, D)), 'W_gate': _w((lambda *s: torch.randn(*s, dtype=torch.bfloat16) * 0.4)(D, F) * (2.0 / D) ** 0.5), 'W_up': _w((lambda *s: torch.randn(*s, dtype=torch.bfloat16) * 0.4)(D, F) * (2.0 / D) ** 0.5), 'W_down': _w((lambda *s: torch.randn(*s, dtype=torch.bfloat16) * 0.4)(F, D) * (1.0 / F) ** 0.5)}
    _B_in = _B_setup()
    return {'A_x': _A_in['x'], 'A_c_kv': _A_in['c_kv'], 'A_w_q': _A_in['w_q'], 'A_w_uk': _A_in['w_uk'], 'A_w_uv': _A_in['w_uv'], 'A_w_o': _A_in['w_o'], 'B_x': _B_in['x'], 'B_W_gate': _B_in['W_gate'], 'B_W_up': _B_in['W_up'], 'B_W_down': _B_in['W_down'], 'c': _w((lambda *s: torch.randn(*s, dtype=torch.bfloat16) * 0.1)(1, 512) * 8.0), 'w_mod': _w((lambda *s: torch.randn(*s, dtype=torch.bfloat16) * 0.1)(512, 6 * 512) * 512 ** (-0.5) * 8.0)}

def reference(A_x, A_c_kv, A_w_q, A_w_uk, A_w_uv, A_w_o, B_x, B_W_gate, B_W_up, B_W_down, c, w_mod):

    def _A_ref(x, c_kv, w_q, w_uk, w_uv, w_o):
        B, Sq, M = (x.shape[0], x.shape[1], x.shape[2])
        Sk = c_kv.shape[1]
        H, D = (w_q.shape[1], w_q.shape[2])
        q = torch.einsum('bqm,mhd->bhqd', x, w_q)
        k = torch.einsum('bkc,chd->bhkd', c_kv, w_uk)
        v = torch.einsum('bkc,chd->bhkd', c_kv, w_uv)
        scores = torch.einsum('bhqd,bhkd->bhqk', q, k) * D ** (-0.5)
        off = Sk - Sq
        qi = torch.arange(Sq)[:, None]
        kj = torch.arange(Sk)[None, :]
        cmask = kj <= qi + off
        scores = torch.where(cmask[None, None], scores, -1000000000.0)
        p = _softmax(scores, axis=-1)
        ctx = torch.einsum('bhqk,bhkd->bhqd', p, v)
        return torch.einsum('bhqd,hdm->bqm', ctx, w_o)

    def _B_ref(x, W_gate, W_up, W_down):
        gate = torch.einsum('bsd,df->bsf', x, W_gate)
        up = torch.einsum('bsd,df->bsf', x, W_up)
        gate = gate * (lambda x, dt: x.to(dt))(gate > 0, gate.dtype)
        h = gate * up
        return torch.einsum('bsf,fd->bsd', h, W_down)
    x = A_x
    mod = (_silu(c) @ w_mod).reshape(c.shape[0], 6, 1, -1)
    sa, ca, ga, sb, cb, gb = (mod[:, 0].clone(), mod[:, 1].clone(), mod[:, 2].clone(), mod[:, 3].clone(), mod[:, 4].clone(), mod[:, 5].clone())
    h = _layernorm(x, axis=-1) * (1 + ca) + sa
    x = x + ga * _A_ref(h, A_c_kv, A_w_q, A_w_uk, A_w_uv, A_w_o)
    h = _layernorm(x, axis=-1) * (1 + cb) + sb
    x = x + gb * _B_ref(h, B_W_gate, B_W_up, B_W_down)
    return x
```

```cpp
#include <hip/hip_runtime.h>
#include <hip/hip_bf16.h>
typedef unsigned short u16;
__device__ __forceinline__ float bf2f(u16 b){ return __uint_as_float(((unsigned)b)<<16); }
__device__ __forceinline__ u16 f2bf(float f){ unsigned u=__float_as_uint(f); u += 0x7FFFu + ((u>>16)&1u); return (u16)(u>>16); }
__device__ __forceinline__ float rbf(float f){ return bf2f(f2bf(f)); }

__global__ void n_mod(const u16* c, const u16* wmod, float* mod){
  int n = blockIdx.x*blockDim.x+threadIdx.x; if(n>=3072) return;
  float acc=0; for(int k=0;k<512;++k){ float x=bf2f(c[k]); float s = rbf(x/(1.f+__expf(-x))); acc += s*bf2f(wmod[k*3072+n]); }
  mod[n]=rbf(acc);
}
__global__ void n_ln(const float* x, const float* sh, const float* cs, u16* h){
  int row=blockIdx.x; int t=threadIdx.x; __shared__ float red[512];
  float v=x[row*512+t]; red[t]=v; __syncthreads();
  for(int s=256;s>0;s>>=1){ if(t<s) red[t]+=red[t+s]; __syncthreads(); }
  float m=red[0]/512.f; __syncthreads();
  float d=v-m; red[t]=d*d; __syncthreads();
  for(int s=256;s>0;s>>=1){ if(t<s) red[t]+=red[t+s]; __syncthreads(); }
  float var=red[0]/512.f;
  float xn=d/sqrtf(var+1e-5f);
  h[row*512+t]=f2bf(xn*rbf(1.f+cs[t])+sh[t]);
}
__global__ void n_cvt(const u16* a, float* o, int n){ int i=blockIdx.x*blockDim.x+threadIdx.x; if(i<n) o[i]=bf2f(a[i]); }
__global__ void n_gemm(const u16* A, const u16* B, float* C, int M, int N, int K){
  __shared__ float As[16][17], Bs[16][17];
  int tx=threadIdx.x, ty=threadIdx.y; int row=blockIdx.y*16+ty, col=blockIdx.x*16+tx; float acc=0;
  for(int k0=0;k0<K;k0+=16){ As[ty][tx]=bf2f(A[(long)row*K+k0+tx]); Bs[ty][tx]=bf2f(B[(long)(k0+ty)*N+col]); __syncthreads();
    for(int k=0;k<16;++k) acc+=As[ty][k]*Bs[k][tx]; __syncthreads(); }
  C[(long)row*N+col]=acc;
}
__global__ void n_round(const float* a, u16* o, long n){ long i=(long)blockIdx.x*blockDim.x+threadIdx.x; if(i<n) o[i]=f2bf(a[i]); }
__global__ void n_attn(const u16* q, const u16* k, const u16* v, u16* ctx){
  int i=blockIdx.x, h=blockIdx.y, t=threadIdx.x; __shared__ float s[2048]; __shared__ float qs[128]; __shared__ float red[128];
  qs[t]=bf2f(q[(long)i*4096+h*128+t]); __syncthreads();
  float mx=-1e30f;
  for(int j=t;j<=i;j+=128){ float a=0; for(int d=0;d<128;++d) a+=qs[d]*bf2f(k[(long)j*4096+h*128+d]); a*=0.08838834764831845f; s[j]=a; mx=fmaxf(mx,a);}
  red[t]=mx; __syncthreads(); for(int st=64;st>0;st>>=1){ if(t<st) red[t]=fmaxf(red[t],red[t+st]); __syncthreads(); } mx=red[0]; __syncthreads();
  float sum=0; for(int j=t;j<=i;j+=128){ float e=expf(s[j]-mx); s[j]=e; sum+=e; }
  red[t]=sum; __syncthreads(); for(int st=64;st>0;st>>=1){ if(t<st) red[t]+=red[t+st]; __syncthreads(); } sum=red[0];
  float o=0; for(int j=0;j<=i;++j) o+=s[j]*bf2f(v[(long)j*4096+h*128+t]);
  ctx[(long)i*4096+h*128+t]=f2bf(o/sum);
}
__global__ void n_resid(const float* x, const float* g, const float* y, float* o){ int i=blockIdx.x*blockDim.x+threadIdx.x; o[i]=x[i]+g[i&511]*y[i]; }
__global__ void n_glu(const float* g, const float* u, u16* a, int n){ int i=blockIdx.x*blockDim.x+threadIdx.x; if(i<n){ float gg=g[i]; a[i]=f2bf((gg>0?gg:0.f)*u[i]); } }

extern "C" void kernel_launch(void* const* d_in, const int* in_sizes, int n_in, void* d_out, int out_size, void* d_ws, size_t ws_size, hipStream_t stream) {
  const u16* x=(const u16*)d_in[0]; const u16* ckv=(const u16*)d_in[1]; const u16* wq=(const u16*)d_in[2]; const u16* wuk=(const u16*)d_in[3]; const u16* wuv=(const u16*)d_in[4]; const u16* wo=(const u16*)d_in[5];
  const u16* wg=(const u16*)d_in[7]; const u16* wu=(const u16*)d_in[8]; const u16* wd=(const u16*)d_in[9]; const u16* c=(const u16*)d_in[10]; const u16* wmod=(const u16*)d_in[11];
  char* ws=(char*)d_ws; size_t off=0; auto alloc=[&](size_t b){ void* p=ws+off; off+=(b+255)&~(size_t)255; return p; };
  float* mod=(float*)alloc(3072*4); float* xf=(float*)alloc(2048*512*4); u16* h=(u16*)alloc(2048*512*2);
  float* tmp=(float*)alloc((size_t)2048*4096*4); u16* q=(u16*)alloc((size_t)2048*4096*2); u16* k=(u16*)alloc((size_t)2048*4096*2); u16* v=(u16*)alloc((size_t)2048*4096*2); u16* ctx=(u16*)alloc((size_t)2048*4096*2);
  float* y=(float*)alloc(2048*512*4); float* x2=(float*)alloc(2048*512*4); float* g=(float*)alloc(2048*1792*4); float* u=(float*)alloc(2048*1792*4); u16* act=(u16*)alloc(2048*1792*2); float* x3=(float*)alloc(2048*512*4);
  n_mod<<<12,256,0,stream>>>(c,wmod,mod);
  n_cvt<<<4096,256,0,stream>>>(x,xf,2048*512);
  n_ln<<<2048,512,0,stream>>>(xf,mod,mod+512,h);
  dim3 b(16,16);
  n_gemm<<<dim3(256,128),b,0,stream>>>(h,wq,tmp,2048,4096,512); n_round<<<32768,256,0,stream>>>(tmp,q,2048L*4096);
  n_gemm<<<dim3(256,128),b,0,stream>>>(ckv,wuk,tmp,2048,4096,512); n_round<<<32768,256,0,stream>>>(tmp,k,2048L*4096);
  n_gemm<<<dim3(256,128),b,0,stream>>>(ckv,wuv,tmp,2048,4096,512); n_round<<<32768,256,0,stream>>>(tmp,v,2048L*4096);
  n_attn<<<dim3(2048,32),128,0,stream>>>(q,k,v,ctx);
  n_gemm<<<dim3(32,128),b,0,stream>>>(ctx,wo,y,2048,512,4096);
  n_resid<<<4096,256,0,stream>>>(xf,mod+1024,y,x2);
  n_ln<<<2048,512,0,stream>>>(x2,mod+1536,mod+2048,h);
  n_gemm<<<dim3(112,128),b,0,stream>>>(h,wg,g,2048,1792,512);
  n_gemm<<<dim3(112,128),b,0,stream>>>(h,wu,u,2048,1792,512);
  n_glu<<<2048*1792/256,256,0,stream>>>(g,u,act,2048*1792);
  n_gemm<<<dim3(32,128),b,0,stream>>>(act,wd,y,2048,512,1792);
  n_resid<<<4096,256,0,stream>>>(x2,mod+2560,y,x3);
  n_round<<<4096,256,0,stream>>>(x3,(u16*)d_out,2048L*512);
}
```

```cpp
#include <hip/hip_runtime.h>

__global__ __launch_bounds__(512, 2) void kf_attn(const void* C, const void* WUK, const void* WUV, const void* WQ, void* WS, const void* WO, const void* WG, const void* WU, const void* WD, const void* X, const void* CV, const void* WM, void* OUT) {
  asm volatile(
    "s_load_dwordx8 s[88:95], %0, 0\n"
    "s_load_dwordx2 s[96:97], %0, 32\n"
    "s_load_dwordx2 s[98:99], %0, 64\n"
    "s_load_dwordx2 s[14:15], %0, 72\n"
    "s_load_dwordx4 s[16:19], %0, 80\n"
    "s_waitcnt lgkmcnt(0)\n"
    "s_mov_b64 s[74:75], s[96:97]\n"
    "s_add_u32 s74, s74, 67108864\n"
    "s_addc_u32 s75, s75, 0\n"
    "s_mov_b64 s[76:77], s[96:97]\n"
    "s_add_u32 s76, s76, 1048576\n"
    "s_addc_u32 s77, s77, 0\n"
    "s_mov_b64 s[78:79], s[96:97]\n"
    "s_add_u32 s78, s78, 33554432\n"
    "s_addc_u32 s79, s79, 0\n"
    "s_mov_b64 s[80:81], s[96:97]\n"
    "s_add_u32 s80, s80, 117440512\n"
    "s_addc_u32 s81, s81, 0\n"
    "v_and_b32 v3, 63, %2\n"
    "v_lshrrev_b32 v15, 6, %2\n"
    "v_lshlrev_b32 v2, 4, v3\n"
    "v_readfirstlane_b32 s60, v15\n"
    "s_and_b32 s66, %1, 7\n"
    "s_lshr_b32 s67, %1, 3\n"
    "s_and_b32 s62, s67, 7\n"
    "s_lshr_b32 s67, s67, 3\n"
    "s_lshl_b32 s66, s66, 2\n"
    "s_add_u32 s61, s66, s67\n"
    "s_lshl_b32 s66, s61, 8\n"
    "s_lshr_b32 s68, s60, 1\n"
    "s_and_b32 s67, s60, 1\n"
    "s_lshl_b32 s71, %1, 1\n"
    "s_add_u32 s71, s71, s67\n"
    "s_cmp_eq_u32 s68, 0\n"
    "s_cselect_b64 s[56:57], s[90:91], s[92:93]\n"
    "s_mov_b32 s58, 0x400000\n"
    "s_cmp_eq_u32 s68, 3\n"
    "s_cselect_b64 s[56:57], s[18:19], s[56:57]\n"
    "s_cselect_b32 s58, 0x300000, s58\n"
    "s_cmp_eq_u32 s68, 2\n"
    "s_cselect_b64 s[56:57], s[88:89], s[56:57]\n"
    "s_cselect_b32 s58, 0x200000, s58\n"
    "s_cselect_b32 s71, %1, s71\n"
    "s_cmp_eq_u32 s60, 5\n"
    "s_cselect_b32 s58, 0, s58\n"
    "s_and_b32 s57, s57, 0xffff\n"
    "s_mov_b32 s59, 0x20000\n"
    "s_lshl_b32 s71, s71, 13\n"
    "v_lshlrev_b32 v14, 3, v2\n"
    "s_bitcmp1_b32 s60, 1\n"
    "s_cbranch_scc1 L_not1_%=\n"
    "buffer_load_dword v80, v14, s[56:59], s71 offen\n"
    "L_not1_%=:\n"
    "s_getpc_b64 s[40:41]\n"
    "L_code0:\n"
    "s_and_b32 s41, s41, 0xffff\n"
    "s_mov_b32 s42, L_code_end-L_code0\n"
    "s_mov_b32 s43, 0x20000\n"
    "s_lshr_b32 s67, %1, 3\n"
    "s_lshl_b32 s67, s67, 13\n"
    "s_cmp_eq_u32 s60, 7\n"
    "s_cbranch_scc0 L_not2_%=\n"
    "buffer_load_dword v80, v14, s[40:43], s67 offen\n"
    "L_not2_%=:\n"
    "s_sub_u32 s68, 0x400000, s66\n"
    "s_lshl_b32 s67, s60, 7\n"
    "s_mov_b64 s[48:49], s[16:17]\n"
    "s_add_u32 s48, s48, s67\n"
    "s_addc_u32 s49, s49, 0\n"
    "s_and_b32 s49, s49, 0xffff\n"
    "s_mov_b32 s50, 128\n"
    "s_mov_b32 s51, 0x20000\n"
    "v_and_b32 v121, 15, v3\n"
    "v_lshlrev_b32 v121, 8, v121\n"
    "v_and_b32 v122, 48, v3\n"
    "v_add_u32 v121, v121, v122\n"
    "buffer_load_dwordx4 v[112:115], v121, s[48:51], 0 offen offset:0\n"
    "buffer_load_dwordx4 v[116:119], v121, s[48:51], 0 offen offset:64\n"
    "s_mov_b64 s[28:29], s[90:91]\n"
    "s_add_u32 s28, s28, s66\n"
    "s_addc_u32 s29, s29, 0\n"
    "s_and_b32 s29, s29, 0xffff\n"
    "s_mov_b32 s30, s68\n"
    "s_mov_b32 s31, 0x20000\n"
    "v_bfe_u32 v14, v3, 2, 3\n"
    "v_lshl_add_u32 v15, s60, 3, v14\n"
    "v_lshlrev_b32 v7, 13, v15\n"
    "v_lshl_add_u32 v15, s60, 4, v14\n"
    "v_lshlrev_b32 v5, 13, v15\n"
    "v_lshrrev_b32 v15, 5, v3\n"
    "v_lshlrev_b32 v15, 6, v15\n"
    "v_and_b32 v14, 3, v3\n"
    "v_lshl_add_u32 v15, v14, 4, v15\n"
    "v_add_u32 v7, v7, v15\n"
    "v_add_u32 v5, v5, v15\n"
    "s_and_b32 s67, s60, 1\n"
    "s_lshl_b32 s67, s67, 5\n"
    "v_xor_b32 v7, s67, v7\n"
    "v_xor_b32 v6, 32, v5\n"
    "s_lshl_b32 s63, s60, 11\n"
    "s_add_u32 m0, s63, 0\n"
    "s_mov_b32 s67, 0\n"
    "buffer_load_dwordx4 v5, s[28:31], s67 offen lds\n"
    "s_add_u32 m0, s63, 1024\n"
    "s_mov_b32 s67, 65536\n"
    "buffer_load_dwordx4 v6, s[28:31], s67 offen lds\n"
    "s_mov_b64 s[32:33], s[92:93]\n"
    "s_add_u32 s32, s32, s66\n"
    "s_addc_u32 s33, s33, 0\n"
    "s_and_b32 s33, s33, 0xffff\n"
    "s_mov_b32 s34, s68\n"
    "s_mov_b32 s35, 0x20000\n"
    "s_lshl_b32 s66, s62, 18\n"
    "s_lshl_b32 s67, s60, 15\n"
    "s_add_u32 s66, s66, s67\n"
    "s_mov_b64 s[24:25], s[88:89]\n"
    "s_add_u32 s24, s24, s66\n"
    "s_addc_u32 s25, s25, 0\n"
    "s_and_b32 s25, s25, 0xffff\n"
    "s_mov_b32 s26, 0x8000\n"
    "s_mov_b32 s27, 0x20000\n"
    "v_lshrrev_b32 v15, 3, v3\n"
    "v_lshlrev_b32 v4, 10, v15\n"
    "v_and_b32 v14, 7, v3\n"
    "v_lshrrev_b32 v15, 4, v3\n"
    "v_xor_b32 v14, v14, v15\n"
    "v_lshl_add_u32 v4, v14, 4, v4\n"
    "v_xor_b32 v1, 64, v4\n"
    "s_lshl_b32 s22, s60, 12\n"
    "s_add_u32 s22, s22, 65536\n"
    "s_add_u32 m0, s22, 0\n"
    "s_mov_b32 s65, 0\n"
    "buffer_load_dwordx4 v4, s[24:27], s65 offen lds\n"
    "s_add_u32 m0, s22, 1024\n"
    "s_mov_b32 s65, 8192\n"
    "buffer_load_dwordx4 v1, s[24:27], s65 offen lds\n"
    "s_add_u32 m0, s22, 2048\n"
    "s_mov_b32 s65, 16384\n"
    "buffer_load_dwordx4 v4, s[24:27], s65 offen lds\n"
    "s_add_u32 m0, s22, 3072\n"
    "s_mov_b32 s65, 24576\n"
    "buffer_load_dwordx4 v1, s[24:27], s65 offen lds\n"
    "s_add_u32 m0, s22, 32768\n"
    "s_mov_b32 s65, 128\n"
    "buffer_load_dwordx4 v4, s[24:27], s65 offen lds\n"
    "s_add_u32 m0, s22, 33792\n"
    "s_mov_b32 s65, 8320\n"
    "buffer_load_dwordx4 v1, s[24:27], s65 offen lds\n"
    "s_add_u32 m0, s22, 34816\n"
    "s_mov_b32 s65, 16512\n"
    "buffer_load_dwordx4 v4, s[24:27], s65 offen lds\n"
    "s_add_u32 m0, s22, 35840\n"
    "s_mov_b32 s65, 24704\n"
    "buffer_load_dwordx4 v1, s[24:27], s65 offen lds\n"
    "s_add_u32 m0, s22, 65536\n"
    "s_mov_b32 s65, 256\n"
    "buffer_load_dwordx4 v4, s[24:27], s65 offen lds\n"
    "s_add_u32 m0, s22, 66560\n"
    "s_mov_b32 s65, 8448\n"
    "buffer_load_dwordx4 v1, s[24:27], s65 offen lds\n"
    "s_add_u32 m0, s22, 67584\n"
    "s_mov_b32 s65, 16640\n"
    "buffer_load_dwordx4 v4, s[24:27], s65 offen lds\n"
    "s_add_u32 m0, s22, 68608\n"
    "s_mov_b32 s65, 24832\n"
    "buffer_load_dwordx4 v1, s[24:27], s65 offen lds\n"
    "s_add_u32 m0, s63, 16384\n"
    "s_mov_b32 s67, 1048576\n"
    "buffer_load_dwordx4 v5, s[28:31], s67 offen lds\n"
    "s_add_u32 m0, s63, 17408\n"
    "s_mov_b32 s67, 1114112\n"
    "buffer_load_dwordx4 v6, s[28:31], s67 offen lds\n"
    "v_and_b32 v14, 15, v3\n"
    "v_lshrrev_b32 v15, 4, v3\n"
    "v_bfe_u32 v9, v3, 1, 3\n"
    "v_xor_b32 v9, v15, v9\n"
    "v_lshlrev_b32 v8, 4, v9\n"
    "v_lshl_add_u32 v8, v14, 7, v8\n"
    "v_add_u32 v8, s22, v8\n"
    "v_xor_b32 v9, 64, v8\n"
    "v_bfe_u32 v14, v3, 2, 2\n"
    "v_lshlrev_b32 v11, 6, v14\n"
    "v_and_b32 v14, 3, v3\n"
    "v_lshl_add_u32 v11, v14, 3, v11\n"
    "v_and_b32 v14, 1, v15\n"
    "v_lshl_add_u32 v11, v14, 5, v11\n"
    "v_lshl_add_u32 v10, v15, 10, v11\n"
    "v_xor_b32 v11, 32, v10\n"
    "v_add_u32 v12, 0x10000, v10\n"
    "v_add_u32 v13, 0x10000, v11\n"
    "s_lshl_b32 s66, s61, 20\n"
    "s_lshl_b32 s67, s62, 17\n"
    "s_add_u32 s66, s66, s67\n"
    "s_lshl_b32 s67, s60, 14\n"
    "s_add_u32 s66, s66, s67\n"
    "s_mov_b32 s64, 0xffff0000\n"
    "s_mov_b64 s[36:37], s[74:75]\n"
    "s_add_u32 s36, s36, s66\n"
    "s_addc_u32 s37, s37, 0\n"
    "s_and_b32 s37, s37, 0xffff\n"
    "s_mov_b32 s38, 0x4000\n"
    "s_mov_b32 s39, 0x20000\n"
    "s_waitcnt vmcnt(16)\n"
    "v_lshlrev_b32 v122, 16, v112\n"
    "v_and_b32 v123, s64, v112\n"
    "v_mul_f32 v124, 0xbfb8aa3b, v122\n"
    "v_mul_f32 v125, 0xbfb8aa3b, v123\n"
    "v_exp_f32 v124, v124\n"
    "v_exp_f32 v125, v125\n"
    "v_add_f32 v124, 1.0, v124\n"
    "v_add_f32 v125, 1.0, v125\n"
    "v_rcp_f32 v124, v124\n"
    "v_rcp_f32 v125, v125\n"
    "v_mul_f32 v122, v122, v124\n"
    "v_mul_f32 v123, v123, v125\n"
    "v_cvt_pk_bf16_f32 v112, v122, v123\n"
    "v_lshlrev_b32 v122, 16, v113\n"
    "v_and_b32 v123, s64, v113\n"
    "v_mul_f32 v124, 0xbfb8aa3b, v122\n"
    "v_mul_f32 v125, 0xbfb8aa3b, v123\n"
    "v_exp_f32 v124, v124\n"
    "v_exp_f32 v125, v125\n"
    "v_add_f32 v124, 1.0, v124\n"
    "v_add_f32 v125, 1.0, v125\n"
    "v_rcp_f32 v124, v124\n"
    "v_rcp_f32 v125, v125\n"
    "v_mul_f32 v122, v122, v124\n"
    "v_mul_f32 v123, v123, v125\n"
    "v_cvt_pk_bf16_f32 v113, v122, v123\n"
    "v_lshlrev_b32 v122, 16, v114\n"
    "v_and_b32 v123, s64, v114\n"
    "v_mul_f32 v124, 0xbfb8aa3b, v122\n"
    "v_mul_f32 v125, 0xbfb8aa3b, v123\n"
    "v_exp_f32 v124, v124\n"
    "v_exp_f32 v125, v125\n"
    "v_add_f32 v124, 1.0, v124\n"
    "v_add_f32 v125, 1.0, v125\n"
    "v_rcp_f32 v124, v124\n"
    "v_rcp_f32 v125, v125\n"
    "v_mul_f32 v122, v122, v124\n"
    "v_mul_f32 v123, v123, v125\n"
    "v_cvt_pk_bf16_f32 v114, v122, v123\n"
    "v_lshlrev_b32 v122, 16, v115\n"
    "v_and_b32 v123, s64, v115\n"
    "v_mul_f32 v124, 0xbfb8aa3b, v122\n"
    "v_mul_f32 v125, 0xbfb8aa3b, v123\n"
    "v_exp_f32 v124, v124\n"
    "v_exp_f32 v125, v125\n"
    "v_add_f32 v124, 1.0, v124\n"
    "v_add_f32 v125, 1.0, v125\n"
    "v_rcp_f32 v124, v124\n"
    "v_rcp_f32 v125, v125\n"
    "v_mul_f32 v122, v122, v124\n"
    "v_mul_f32 v123, v123, v125\n"
    "v_cvt_pk_bf16_f32 v115, v122, v123\n"
    "v_lshlrev_b32 v122, 16, v116\n"
    "v_and_b32 v123, s64, v116\n"
    "v_mul_f32 v124, 0xbfb8aa3b, v122\n"
    "v_mul_f32 v125, 0xbfb8aa3b, v123\n"
    "v_exp_f32 v124, v124\n"
    "v_exp_f32 v125, v125\n"
    "v_add_f32 v124, 1.0, v124\n"
    "v_add_f32 v125, 1.0, v125\n"
    "v_rcp_f32 v124, v124\n"
    "v_rcp_f32 v125, v125\n"
    "v_mul_f32 v122, v122, v124\n"
    "v_mul_f32 v123, v123, v125\n"
    "v_cvt_pk_bf16_f32 v116, v122, v123\n"
    "v_lshlrev_b32 v122, 16, v117\n"
    "v_and_b32 v123, s64, v117\n"
    "v_mul_f32 v124, 0xbfb8aa3b, v122\n"
    "v_mul_f32 v125, 0xbfb8aa3b, v123\n"
    "v_exp_f32 v124, v124\n"
    "v_exp_f32 v125, v125\n"
    "v_add_f32 v124, 1.0, v124\n"
    "v_add_f32 v125, 1.0, v125\n"
    "v_rcp_f32 v124, v124\n"
    "v_rcp_f32 v125, v125\n"
    "v_mul_f32 v122, v122, v124\n"
    "v_mul_f32 v123, v123, v125\n"
    "v_cvt_pk_bf16_f32 v117, v122, v123\n"
    "v_lshlrev_b32 v122, 16, v118\n"
    "v_and_b32 v123, s64, v118\n"
    "v_mul_f32 v124, 0xbfb8aa3b, v122\n"
    "v_mul_f32 v125, 0xbfb8aa3b, v123\n"
    "v_exp_f32 v124, v124\n"
    "v_exp_f32 v125, v125\n"
    "v_add_f32 v124, 1.0, v124\n"
    "v_add_f32 v125, 1.0, v125\n"
    "v_rcp_f32 v124, v124\n"
    "v_rcp_f32 v125, v125\n"
    "v_mul_f32 v122, v122, v124\n"
    "v_mul_f32 v123, v123, v125\n"
    "v_cvt_pk_bf16_f32 v118, v122, v123\n"
    "v_lshlrev_b32 v122, 16, v119\n"
    "v_and_b32 v123, s64, v119\n"
    "v_mul_f32 v124, 0xbfb8aa3b, v122\n"
    "v_mul_f32 v125, 0xbfb8aa3b, v123\n"
    "v_exp_f32 v124, v124\n"
    "v_exp_f32 v125, v125\n"
    "v_add_f32 v124, 1.0, v124\n"
    "v_add_f32 v125, 1.0, v125\n"
    "v_rcp_f32 v124, v124\n"
    "v_rcp_f32 v125, v125\n"
    "v_mul_f32 v122, v122, v124\n"
    "v_mul_f32 v123, v123, v125\n"
    "v_cvt_pk_bf16_f32 v119, v122, v123\n"
    "v_mov_b32 v64, v112\n"
    "v_mov_b32 v65, v113\n"
    "v_mov_b32 v66, v114\n"
    "v_mov_b32 v67, v115\n"
    "v_mov_b32 v68, v116\n"
    "v_mov_b32 v69, v117\n"
    "v_mov_b32 v70, v118\n"
    "v_mov_b32 v71, v119\n"
    "s_lshl_b32 s66, %1, 3\n"
    "s_add_u32 s69, s66, s60\n"
    "s_lshl_b32 s66, s69, 10\n"
    "s_mov_b64 s[40:41], s[14:15]\n"
    "s_add_u32 s40, s40, s66\n"
    "s_addc_u32 s41, s41, 0\n"
    "s_and_b32 s41, s41, 0xffff\n"
    "s_mov_b32 s42, 0x400\n"
    "s_mov_b32 s43, 0x20000\n"
    "s_lshr_b32 s66, %1, 1\n"
    "s_lshl_b32 s66, s66, 14\n"
    "s_and_b32 s23, %1, 1\n"
    "s_lshl_b32 s23, s23, 7\n"
    "s_add_u32 s66, s66, s23\n"
    "s_lshl_b32 s23, s60, 11\n"
    "s_add_u32 s66, s66, s23\n"
    "s_mov_b64 s[44:45], s[76:77]\n"
    "s_add_u32 s44, s44, s66\n"
    "s_addc_u32 s45, s45, 0\n"
    "s_and_b32 s45, s45, 0xffff\n"
    "s_mov_b32 s46, 0x4000\n"
    "s_mov_b32 s47, 0x20000\n"
    "v_lshrrev_b32 v127, 2, v3\n"
    "v_and_b32 v125, 3, v3\n"
    "v_lshlrev_b32 v126, 10, v127\n"
    "v_lshl_add_u32 v126, v125, 8, v126\n"
    "v_lshlrev_b32 v127, 6, v127\n"
    "v_lshl_add_u32 v127, v125, 3, v127\n"
    "buffer_load_dwordx2 v[112:113], v127, s[40:43], 0 offen\n"
    "buffer_load_dwordx2 v[114:115], v127, s[40:43], 0 offen offset:32\n"
    "s_waitcnt vmcnt(12)\n"
    "ds_read_b128 a[0:3], v8 offset:0\n"
    "ds_read_b128 a[4:7], v9 offset:0\n"
    "ds_read_b128 a[64:67], v8 offset:2048\n"
    "ds_read_b128 a[68:71], v9 offset:2048\n"
    "s_waitcnt vmcnt(8)\n"
    "ds_read_b128 a[8:11], v8 offset:32768\n"
    "ds_read_b128 a[12:15], v9 offset:32768\n"
    "ds_read_b128 a[72:75], v8 offset:34816\n"
    "ds_read_b128 a[76:79], v9 offset:34816\n"
    "s_barrier\n"
    "ds_read_b64_tr_b16 v[80:81], v10 offset:0\n"
    "ds_read_b64_tr_b16 v[82:83], v10 offset:256\n"
    "ds_read_b64_tr_b16 v[84:85], v11 offset:0\n"
    "ds_read_b64_tr_b16 v[86:87], v11 offset:256\n"
    "ds_read_b64_tr_b16 v[88:89], v10 offset:512\n"
    "ds_read_b64_tr_b16 v[90:91], v10 offset:768\n"
    "ds_read_b64_tr_b16 v[92:93], v11 offset:512\n"
    "ds_read_b64_tr_b16 v[94:95], v11 offset:768\n"
    "ds_read_b64_tr_b16 v[96:97], v10 offset:4096\n"
    "ds_read_b64_tr_b16 v[98:99], v10 offset:4352\n"
    "ds_read_b64_tr_b16 v[100:101], v11 offset:4096\n"
    "ds_read_b64_tr_b16 v[102:103], v11 offset:4352\n"
    "ds_read_b64_tr_b16 v[104:105], v10 offset:4608\n"
    "ds_read_b64_tr_b16 v[106:107], v10 offset:4864\n"
    "ds_read_b64_tr_b16 v[108:109], v11 offset:4608\n"
    "ds_read_b64_tr_b16 v[110:111], v11 offset:4864\n"
    "s_waitcnt lgkmcnt(15)\n"
    "s_add_u32 m0, s22, 0\n"
    "s_mov_b32 s65, 384\n"
    "buffer_load_dwordx4 v4, s[24:27], s65 offen lds\n"
    "s_add_u32 m0, s22, 1024\n"
    "s_mov_b32 s65, 8576\n"
    "buffer_load_dwordx4 v1, s[24:27], s65 offen lds\n"
    "s_add_u32 m0, s22, 2048\n"
    "s_mov_b32 s65, 16768\n"
    "buffer_load_dwordx4 v4, s[24:27], s65 offen lds\n"
    "s_add_u32 m0, s22, 3072\n"
    "s_mov_b32 s65, 24960\n"
    "buffer_load_dwordx4 v1, s[24:27], s65 offen lds\n"
    "s_add_u32 m0, s22, 32768\n"
    "s_mov_b32 s65, 512\n"
    "buffer_load_dwordx4 v4, s[24:27], s65 offen lds\n"
    "s_add_u32 m0, s22, 33792\n"
    "s_mov_b32 s65, 8704\n"
    "buffer_load_dwordx4 v1, s[24:27], s65 offen lds\n"
    "s_add_u32 m0, s22, 34816\n"
    "s_mov_b32 s65, 16896\n"
    "buffer_load_dwordx4 v4, s[24:27], s65 offen lds\n"
    "s_add_u32 m0, s22, 35840\n"
    "s_mov_b32 s65, 25088\n"
    "buffer_load_dwordx4 v1, s[24:27], s65 offen lds\n"
    "s_add_u32 m0, s63, 32768\n"
    "s_mov_b32 s67, 2097152\n"
    "buffer_load_dwordx4 v5, s[28:31], s67 offen lds\n"
    "s_add_u32 m0, s63, 33792\n"
    "s_mov_b32 s67, 2162688\n"
    "buffer_load_dwordx4 v6, s[28:31], s67 offen lds\n"
    "s_add_u32 m0, s63, 49152\n"
    "s_mov_b32 s67, 3145728\n"
    "buffer_load_dwordx4 v5, s[28:31], s67 offen lds\n"
    "s_add_u32 m0, s63, 50176\n"
    "s_mov_b32 s67, 3211264\n"
    "buffer_load_dwordx4 v6, s[28:31], s67 offen lds\n"
    "s_cmp_eq_u32 s60, 0\n"
    "s_cbranch_scc0 L_hsA_%=\n"
    "s_lshl_b32 s20, %1, 2\n"
    "s_add_u32 s66, s20, 393216\n"
    "v_lshlrev_b32 v14, 10, v3\n"
    "v_add_u32 v14, s66, v14\n"
    "global_load_dword v48, v14, s[96:97] sc1\n"
    "v_add_u32 v14, 0x10000, v14\n"
    "global_load_dword v49, v14, s[96:97] sc1\n"
    "v_add_u32 v14, 0x10000, v14\n"
    "global_load_dword v50, v14, s[96:97] sc1\n"
    "v_add_u32 v14, 0x10000, v14\n"
    "global_load_dword v51, v14, s[96:97] sc1\n"
    "s_add_u32 s66, s20, 51200\n"
    "v_mov_b32 v14, s66\n"
    "global_load_dword v52, v14, s[96:97] sc1\n"
    "s_lshl_b32 s66, s61, 5\n"
    "s_lshl_b32 s67, s62, 2\n"
    "s_add_u32 s66, s66, s67\n"
    "s_add_u32 s66, s66, 49152\n"
    "v_mov_b32 v15, 0\n"
    "s_cmp_lt_u32 %1, 20\n"
    "s_cbranch_scc0 L_hsA0_%=\n"
    "v_mov_b32 v56, 0\n"
    "v_mov_b32 v57, 0\n"
    "v_mov_b32 v58, 0\n"
    "v_mov_b32 v59, 0\n"
    "s_lshl_b32 s67, %1, 10\n"
    "s_add_u32 s67, s67, 655360\n"
    "v_add_u32 v14, s67, v2\n"
    "global_store_dwordx4 v14, v[56:59], s[96:97] sc1\n"
    "L_hsA0_%=:\n"
    "v_mov_b32 v14, s66\n"
    "global_store_dword v14, v15, s[96:97] sc1\n"
    "L_hsA_%=:\n"
    "s_waitcnt lgkmcnt(12)\n"
    "v_mfma_f32_16x16x32_bf16 v[16:19], v[80:83], a[0:3], 0\n"
    "s_waitcnt vmcnt(16)\n"
    "v_add_u32 v14, 0x10000, v8\n"
    "v_add_u32 v15, 0x10000, v9\n"
    "ds_read_b128 a[16:19], v14 offset:0\n"
    "v_mfma_f32_16x16x32_bf16 v[32:35], v[80:83], a[64:67], 0\n"
    "ds_read_b64_tr_b16 v[80:81], v10 offset:8192\n"
    "ds_read_b64_tr_b16 v[82:83], v10 offset:8448\n"
    "v_mfma_f32_16x16x32_bf16 v[20:23], v[84:87], a[0:3], 0\n"
    "ds_read_b128 a[20:23], v15 offset:0\n"
    "ds_read_b128 a[80:83], v14 offset:2048\n"
    "ds_read_b128 a[84:87], v15 offset:2048\n"
    "v_mfma_f32_16x16x32_bf16 v[36:39], v[84:87], a[64:67], 0\n"
    "ds_read_b64_tr_b16 v[84:85], v11 offset:8192\n"
    "ds_read_b64_tr_b16 v[86:87], v11 offset:8448\n"
    "s_waitcnt lgkmcnt(15)\n"
    "v_mfma_f32_16x16x32_bf16 v[24:27], v[88:91], a[0:3], 0\n"
    "s_waitcnt vmcnt(12)\n"
    "v_lshlrev_b32 v116, 16, v112\n"
    "v_and_b32 v117, s64, v112\n"
    "v_lshlrev_b32 v118, 16, v113\n"
    "v_mfma_f32_16x16x32_bf16 v[40:43], v[88:91], a[64:67], 0\n"
    "ds_read_b64_tr_b16 v[88:89], v10 offset:8704\n"
    "ds_read_b64_tr_b16 v[90:91], v10 offset:8960\n"
    "v_mfma_f32_16x16x32_bf16 v[28:31], v[92:95], a[0:3], 0\n"
    "v_and_b32 v119, s64, v113\n"
    "v_lshlrev_b32 v120, 16, v114\n"
    "v_and_b32 v121, s64, v114\n"
    "v_mfma_f32_16x16x32_bf16 v[44:47], v[92:95], a[64:67], 0\n"
    "ds_read_b64_tr_b16 v[92:93], v11 offset:8704\n"
    "ds_read_b64_tr_b16 v[94:95], v11 offset:8960\n"
    "s_waitcnt lgkmcnt(15)\n"
    "v_mfma_f32_16x16x32_bf16 v[16:19], v[96:99], a[4:7], v[16:19]\n"
    "v_lshlrev_b32 v122, 16, v115\n"
    "v_and_b32 v123, s64, v115\n"
    "v_add_f32 v124, v116, v117\n"
    "v_mfma_f32_16x16x32_bf16 v[32:35], v[96:99], a[68:71], v[32:35]\n"
    "ds_read_b64_tr_b16 v[96:97], v10 offset:12288\n"
    "ds_read_b64_tr_b16 v[98:99], v10 offset:12544\n"
    "v_mfma_f32_16x16x32_bf16 v[20:23], v[100:103], a[4:7], v[20:23]\n"
    "v_add_f32 v124, v124, v118\n"
    "v_add_f32 v124, v124, v119\n"
    "v_add_f32 v124, v124, v120\n"
    "v_mfma_f32_16x16x32_bf16 v[36:39], v[100:103], a[68:71], v[36:39]\n"
    "ds_read_b64_tr_b16 v[100:101], v11 offset:12288\n"
    "ds_read_b64_tr_b16 v[102:103], v11 offset:12544\n"
    "s_waitcnt lgkmcnt(15)\n"
    "v_mfma_f32_16x16x32_bf16 v[24:27], v[104:107], a[4:7], v[24:27]\n"
    "s_waitcnt lgkmcnt(10)\n"
    "s_add_u32 m0, s22, 65536\n"
    "s_mov_b32 s65, 640\n"
    "buffer_load_dwordx4 v4, s[24:27], s65 offen lds\n"
    "v_mfma_f32_16x16x32_bf16 v[40:43], v[104:107], a[68:71], v[40:43]\n"
    "ds_read_b64_tr_b16 v[104:105], v10 offset:12800\n"
    "ds_read_b64_tr_b16 v[106:107], v10 offset:13056\n"
    "v_mfma_f32_16x16x32_bf16 v[28:31], v[108:111], a[4:7], v[28:31]\n"
    "s_add_u32 m0, s22, 66560\n"
    "s_mov_b32 s65, 8832\n"
    "buffer_load_dwordx4 v1, s[24:27], s65 offen lds\n"
    "v_mfma_f32_16x16x32_bf16 v[44:47], v[108:111], a[68:71], v[44:47]\n"
    "ds_read_b64_tr_b16 v[108:109], v11 offset:12800\n"
    "ds_read_b64_tr_b16 v[110:111], v11 offset:13056\n"
    "s_barrier\n"
    "s_waitcnt lgkmcnt(12)\n"
    "v_mfma_f32_16x16x32_bf16 v[16:19], v[80:83], a[8:11], v[16:19]\n"
    "s_add_u32 m0, s22, 67584\n"
    "s_mov_b32 s65, 17024\n"
    "buffer_load_dwordx4 v4, s[24:27], s65 offen lds\n"
    "v_mfma_f32_16x16x32_bf16 v[32:35], v[80:83], a[72:75], v[32:35]\n"
    "ds_read_b64_tr_b16 v[80:81], v10 offset:16384\n"
    "ds_read_b64_tr_b16 v[82:83], v10 offset:16640\n"
    "v_mfma_f32_16x16x32_bf16 v[20:23], v[84:87], a[8:11], v[20:23]\n"
    "s_add_u32 m0, s22, 68608\n"
    "s_mov_b32 s65, 25216\n"
    "buffer_load_dwordx4 v1, s[24:27], s65 offen lds\n"
    "v_mfma_f32_16x16x32_bf16 v[36:39], v[84:87], a[72:75], v[36:39]\n"
    "ds_read_b64_tr_b16 v[84:85], v11 offset:16384\n"
    "ds_read_b64_tr_b16 v[86:87], v11 offset:16640\n"
    "s_waitcnt lgkmcnt(12)\n"
    "v_mfma_f32_16x16x32_bf16 v[24:27], v[88:91], a[8:11], v[24:27]\n"
    "s_waitcnt vmcnt(12)\n"
    "ds_read_b128 a[24:27], v8 offset:0\n"
    "ds_read_b128 a[28:31], v9 offset:0\n"
    "ds_read_b128 a[88:91], v8 offset:2048\n"
    "v_mfma_f32_16x16x32_bf16 v[40:43], v[88:91], a[72:75], v[40:43]\n"
    "ds_read_b64_tr_b16 v[88:89], v10 offset:16896\n"
    "ds_read_b64_tr_b16 v[90:91], v10 offset:17152\n"
    "v_mfma_f32_16x16x32_bf16 v[28:31], v[92:95], a[8:11], v[28:31]\n"
    "ds_read_b128 a[92:95], v9 offset:2048\n"
    "v_add_f32 v124, v124, v121\n"
    "v_add_f32 v124, v124, v122\n"
    "v_mfma_f32_16x16x32_bf16 v[44:47], v[92:95], a[72:75], v[44:47]\n"
    "ds_read_b64_tr_b16 v[92:93], v11 offset:16896\n"
    "ds_read_b64_tr_b16 v[94:95], v11 offset:17152\n"
    "s_waitcnt lgkmcnt(15)\n"
    "v_mfma_f32_16x16x32_bf16 v[16:19], v[96:99], a[12:15], v[16:19]\n"
    "v_add_f32 v124, v124, v123\n"
    "s_nop 1\n"
    "v_add_f32_dpp v124, v124, v124 quad_perm:[1,0,3,2] row_mask:0xf bank_mask:0xf\n"
    "s_nop 1\n"
    "v_add_f32_dpp v124, v124, v124 quad_perm:[2,3,0,1] row_mask:0xf bank_mask:0xf\n"
    "v_mfma_f32_16x16x32_bf16 v[32:35], v[96:99], a[76:79], v[32:35]\n"
    "ds_read_b64_tr_b16 v[96:97], v10 offset:20480\n"
    "ds_read_b64_tr_b16 v[98:99], v10 offset:20736\n"
    "v_mfma_f32_16x16x32_bf16 v[20:23], v[100:103], a[12:15], v[20:23]\n"
    "v_add_f32_dpp v124, v124, v124 row_half_mirror row_mask:0xf bank_mask:0xf\n"
    "s_nop 1\n"
    "v_add_f32_dpp v124, v124, v124 row_mirror row_mask:0xf bank_mask:0xf\n"
    "v_mov_b32 v125, v124\n"
    "v_mfma_f32_16x16x32_bf16 v[36:39], v[100:103], a[76:79], v[36:39]\n"
    "ds_read_b64_tr_b16 v[100:101], v11 offset:20480\n"
    "ds_read_b64_tr_b16 v[102:103], v11 offset:20736\n"
    "s_waitcnt lgkmcnt(15)\n"
    "v_mfma_f32_16x16x32_bf16 v[24:27], v[104:107], a[12:15], v[24:27]\n"
    "s_waitcnt lgkmcnt(6)\n"
    "s_add_u32 m0, s22, 0\n"
    "s_mov_b32 s65, 768\n"
    "buffer_load_dwordx4 v4, s[24:27], s65 offen lds\n"
    "v_mfma_f32_16x16x32_bf16 v[40:43], v[104:107], a[76:79], v[40:43]\n"
    "ds_read_b64_tr_b16 v[104:105], v10 offset:20992\n"
    "ds_read_b64_tr_b16 v[106:107], v10 offset:21248\n"
    "v_mfma_f32_16x16x32_bf16 v[28:31], v[108:111], a[12:15], v[28:31]\n"
    "s_add_u32 m0, s22, 1024\n"
    "s_mov_b32 s65, 8960\n"
    "buffer_load_dwordx4 v1, s[24:27], s65 offen lds\n"
    "v_mfma_f32_16x16x32_bf16 v[44:47], v[108:111], a[76:79], v[44:47]\n"
    "ds_read_b64_tr_b16 v[108:109], v11 offset:20992\n"
    "ds_read_b64_tr_b16 v[110:111], v11 offset:21248\n"
    "s_cmp_eq_u32 s60, 0\n"
    "s_cbranch_scc0 L_hsB_%=\n"
    "s_waitcnt vmcnt(6)\n"
    "v_mov_b32 v14, v52\n"
    "s_nop 0\n"
    "v_readfirstlane_b32 s21, v14\n"
    "L_hsT_%=:\n"
    "s_nop 0\n"
    "s_add_u32 s21, s21, 1\n"
    "v_mov_b32 v14, v48\n"
    "v_mov_b32 v15, v49\n"
    "v_cmp_eq_u32 vcc, s21, v14\n"
    "s_cbranch_vccnz L_hsT_%=\n"
    "v_cmp_eq_u32 vcc, s21, v15\n"
    "s_cbranch_vccnz L_hsT_%=\n"
    "v_mov_b32 v14, v50\n"
    "v_mov_b32 v15, v51\n"
    "v_cmp_eq_u32 vcc, s21, v14\n"
    "s_cbranch_vccnz L_hsT_%=\n"
    "v_cmp_eq_u32 vcc, s21, v15\n"
    "s_cbranch_vccnz L_hsT_%=\n"
    "s_add_u32 s66, s20, 51200\n"
    "v_mov_b32 v14, s66\n"
    "v_mov_b32 v15, s21\n"
    "global_store_dword v14, v15, s[96:97] sc1\n"
    "L_hsB_%=:\n"
    "v_lshlrev_b32 v14, 3, v2\n"
    "s_bitcmp1_b32 s60, 1\n"
    "s_cbranch_scc0 L_not3_%=\n"
    "buffer_load_dword v76, v14, s[56:59], s71 offen\n"
    "L_not3_%=:\n"
    "v_mfma_f32_16x16x32_bf16 v[16:19], v[80:83], a[16:19], v[16:19]\n"
    "s_add_u32 m0, s22, 2048\n"
    "s_mov_b32 s65, 17152\n"
    "buffer_load_dwordx4 v4, s[24:27], s65 offen lds\n"
    "v_mfma_f32_16x16x32_bf16 v[32:35], v[80:83], a[80:83], v[32:35]\n"
    "ds_read_b64_tr_b16 v[80:81], v10 offset:24576\n"
    "ds_read_b64_tr_b16 v[82:83], v10 offset:24832\n"
    "v_mfma_f32_16x16x32_bf16 v[20:23], v[84:87], a[16:19], v[20:23]\n"
    "s_add_u32 m0, s22, 3072\n"
    "s_mov_b32 s65, 25344\n"
    "buffer_load_dwordx4 v1, s[24:27], s65 offen lds\n"
    "v_mfma_f32_16x16x32_bf16 v[36:39], v[84:87], a[80:83], v[36:39]\n"
    "ds_read_b64_tr_b16 v[84:85], v11 offset:24576\n"
    "ds_read_b64_tr_b16 v[86:87], v11 offset:24832\n"
    "s_waitcnt lgkmcnt(12)\n"
    "v_mfma_f32_16x16x32_bf16 v[24:27], v[88:91], a[16:19], v[24:27]\n"
    "s_waitcnt vmcnt(12)\n"
    "ds_read_b128 a[32:35], v8 offset:32768\n"
    "ds_read_b128 a[36:39], v9 offset:32768\n"
    "ds_read_b128 a[96:99], v8 offset:34816\n"
    "v_mfma_f32_16x16x32_bf16 v[40:43], v[88:91], a[80:83], v[40:43]\n"
    "ds_read_b64_tr_b16 v[88:89], v10 offset:25088\n"
    "ds_read_b64_tr_b16 v[90:91], v10 offset:25344\n"
    "v_mfma_f32_16x16x32_bf16 v[28:31], v[92:95], a[16:19], v[28:31]\n"
    "ds_read_b128 a[100:103], v9 offset:34816\n"
    "v_permlane16_swap_b32 v124, v125\n"
    "v_add_f32 v124, v124, v125\n"
    "v_mfma_f32_16x16x32_bf16 v[44:47], v[92:95], a[80:83], v[44:47]\n"
    "ds_read_b64_tr_b16 v[92:93], v11 offset:25088\n"
    "ds_read_b64_tr_b16 v[94:95], v11 offset:25344\n"
    "s_waitcnt lgkmcnt(15)\n"
    "v_mfma_f32_16x16x32_bf16 v[16:19], v[96:99], a[20:23], v[16:19]\n"
    "v_mov_b32 v125, v124\n"
    "s_nop 1\n"
    "v_permlane32_swap_b32 v124, v125\n"
    "v_add_f32 v124, v124, v125\n"
    "v_mfma_f32_16x16x32_bf16 v[32:35], v[96:99], a[84:87], v[32:35]\n"
    "ds_read_b64_tr_b16 v[96:97], v10 offset:28672\n"
    "ds_read_b64_tr_b16 v[98:99], v10 offset:28928\n"
    "v_mfma_f32_16x16x32_bf16 v[20:23], v[100:103], a[20:23], v[20:23]\n"
    "v_mul_f32 v124, 0x3b000000, v124\n"
    "v_sub_f32 v116, v116, v124\n"
    "v_sub_f32 v117, v117, v124\n"
    "v_mfma_f32_16x16x32_bf16 v[36:39], v[100:103], a[84:87], v[36:39]\n"
    "ds_read_b64_tr_b16 v[100:101], v11 offset:28672\n"
    "ds_read_b64_tr_b16 v[102:103], v11 offset:28928\n"
    "s_waitcnt lgkmcnt(15)\n"
    "v_mfma_f32_16x16x32_bf16 v[24:27], v[104:107], a[20:23], v[24:27]\n"
    "s_waitcnt lgkmcnt(6)\n"
    "s_add_u32 m0, s22, 32768\n"
    "s_mov_b32 s65, 896\n"
    "buffer_load_dwordx4 v4, s[24:27], s65 offen lds\n"
    "v_mfma_f32_16x16x32_bf16 v[40:43], v[104:107], a[84:87], v[40:43]\n"
    "ds_read_b64_tr_b16 v[104:105], v10 offset:29184\n"
    "ds_read_b64_tr_b16 v[106:107], v10 offset:29440\n"
    "v_mfma_f32_16x16x32_bf16 v[28:31], v[108:111], a[20:23], v[28:31]\n"
    "s_add_u32 m0, s22, 33792\n"
    "s_mov_b32 s65, 9088\n"
    "buffer_load_dwordx4 v1, s[24:27], s65 offen lds\n"
    "v_mfma_f32_16x16x32_bf16 v[44:47], v[108:111], a[84:87], v[44:47]\n"
    "ds_read_b64_tr_b16 v[108:109], v11 offset:29184\n"
    "ds_read_b64_tr_b16 v[110:111], v11 offset:29440\n"
    "s_waitcnt vmcnt(12)\n"
    "s_barrier\n"
    "v_mfma_f32_16x16x32_bf16 v[16:19], v[80:83], a[24:27], v[16:19]\n"
    "s_add_u32 m0, s63, 0\n"
    "s_mov_b32 s67, 128\n"
    "buffer_load_dwordx4 v5, s[28:31], s67 offen lds\n"
    "v_mfma_f32_16x16x32_bf16 v[32:35], v[80:83], a[88:91], v[32:35]\n"
    "ds_read_b64_tr_b16 v[80:81], v10 offset:32768\n"
    "ds_read_b64_tr_b16 v[82:83], v10 offset:33024\n"
    "v_mfma_f32_16x16x32_bf16 v[20:23], v[84:87], a[24:27], v[20:23]\n"
    "s_add_u32 m0, s63, 1024\n"
    "s_mov_b32 s67, 65664\n"
    "buffer_load_dwordx4 v6, s[28:31], s67 offen lds\n"
    "v_mfma_f32_16x16x32_bf16 v[36:39], v[84:87], a[88:91], v[36:39]\n"
    "ds_read_b64_tr_b16 v[84:85], v11 offset:32768\n"
    "ds_read_b64_tr_b16 v[86:87], v11 offset:33024\n"
    "s_waitcnt lgkmcnt(12)\n"
    "v_mfma_f32_16x16x32_bf16 v[24:27], v[88:91], a[24:27], v[24:27]\n"
    "s_add_u32 m0, s22, 34816\n"
    "s_mov_b32 s65, 17280\n"
    "buffer_load_dwordx4 v4, s[24:27], s65 offen lds\n"
    "v_mfma_f32_16x16x32_bf16 v[40:43], v[88:91], a[88:91], v[40:43]\n"
    "ds_read_b64_tr_b16 v[88:89], v10 offset:33280\n"
    "ds_read_b64_tr_b16 v[90:91], v10 offset:33536\n"
    "v_mfma_f32_16x16x32_bf16 v[28:31], v[92:95], a[24:27], v[28:31]\n"
    "s_add_u32 m0, s22, 35840\n"
    "s_mov_b32 s65, 25472\n"
    "buffer_load_dwordx4 v1, s[24:27], s65 offen lds\n"
    "v_mfma_f32_16x16x32_bf16 v[44:47], v[92:95], a[88:91], v[44:47]\n"
    "ds_read_b64_tr_b16 v[92:93], v11 offset:33280\n"
    "ds_read_b64_tr_b16 v[94:95], v11 offset:33536\n"
    "s_waitcnt lgkmcnt(12)\n"
    "v_mfma_f32_16x16x32_bf16 v[16:19], v[96:99], a[28:31], v[16:19]\n"
    "s_waitcnt vmcnt(10)\n"
    "v_add_u32 v14, 0x10000, v8\n"
    "v_add_u32 v15, 0x10000, v9\n"
    "ds_read_b128 a[40:43], v14 offset:0\n"
    "v_mfma_f32_16x16x32_bf16 v[32:35], v[96:99], a[92:95], v[32:35]\n"
    "ds_read_b64_tr_b16 v[96:97], v10 offset:36864\n"
    "ds_read_b64_tr_b16 v[98:99], v10 offset:37120\n"
    "v_mfma_f32_16x16x32_bf16 v[20:23], v[100:103], a[28:31], v[20:23]\n"
    "ds_read_b128 a[44:47], v15 offset:0\n"
    "ds_read_b128 a[104:107], v14 offset:2048\n"
    "ds_read_b128 a[108:111], v15 offset:2048\n"
    "v_mfma_f32_16x16x32_bf16 v[36:39], v[100:103], a[92:95], v[36:39]\n"
    "ds_read_b64_tr_b16 v[100:101], v11 offset:36864\n"
    "ds_read_b64_tr_b16 v[102:103], v11 offset:37120\n"
    "s_waitcnt lgkmcnt(15)\n"
    "v_mfma_f32_16x16x32_bf16 v[24:27], v[104:107], a[28:31], v[24:27]\n"
    "v_sub_f32 v118, v118, v124\n"
    "v_sub_f32 v119, v119, v124\n"
    "v_sub_f32 v120, v120, v124\n"
    "v_mfma_f32_16x16x32_bf16 v[40:43], v[104:107], a[92:95], v[40:43]\n"
    "ds_read_b64_tr_b16 v[104:105], v10 offset:37376\n"
    "ds_read_b64_tr_b16 v[106:107], v10 offset:37632\n"
    "v_mfma_f32_16x16x32_bf16 v[28:31], v[108:111], a[28:31], v[28:31]\n"
    "v_sub_f32 v121, v121, v124\n"
    "v_sub_f32 v122, v122, v124\n"
    "v_sub_f32 v123, v123, v124\n"
    "v_mfma_f32_16x16x32_bf16 v[44:47], v[108:111], a[92:95], v[44:47]\n"
    "ds_read_b64_tr_b16 v[108:109], v11 offset:37376\n"
    "ds_read_b64_tr_b16 v[110:111], v11 offset:37632\n"
    "s_waitcnt lgkmcnt(15)\n"
    "v_mfma_f32_16x16x32_bf16 v[16:19], v[80:83], a[32:35], v[16:19]\n"
    "s_waitcnt vmcnt(6)\n"
    "ds_read_b128 a[48:51], v8 offset:0\n"
    "ds_read_b128 a[52:55], v9 offset:0\n"
    "ds_read_b128 a[112:115], v8 offset:2048\n"
    "v_mfma_f32_16x16x32_bf16 v[32:35], v[80:83], a[96:99], v[32:35]\n"
    "ds_read_b64_tr_b16 v[80:81], v10 offset:40960\n"
    "ds_read_b64_tr_b16 v[82:83], v10 offset:41216\n"
    "v_mfma_f32_16x16x32_bf16 v[20:23], v[84:87], a[32:35], v[20:23]\n"
    "ds_read_b128 a[116:119], v9 offset:2048\n"
    "v_mul_f32 v124, v116, v116\n"
    "v_fmac_f32 v124, v117, v117\n"
    "v_mfma_f32_16x16x32_bf16 v[36:39], v[84:87], a[96:99], v[36:39]\n"
    "ds_read_b64_tr_b16 v[84:85], v11 offset:40960\n"
    "ds_read_b64_tr_b16 v[86:87], v11 offset:41216\n"
    "s_waitcnt lgkmcnt(15)\n"
    "v_mfma_f32_16x16x32_bf16 v[24:27], v[88:91], a[32:35], v[24:27]\n"
    "v_fmac_f32 v124, v118, v118\n"
    "v_fmac_f32 v124, v119, v119\n"
    "v_fmac_f32 v124, v120, v120\n"
    "v_mfma_f32_16x16x32_bf16 v[40:43], v[88:91], a[96:99], v[40:43]\n"
    "ds_read_b64_tr_b16 v[88:89], v10 offset:41472\n"
    "ds_read_b64_tr_b16 v[90:91], v10 offset:41728\n"
    "v_mfma_f32_16x16x32_bf16 v[28:31], v[92:95], a[32:35], v[28:31]\n"
    "v_fmac_f32 v124, v121, v121\n"
    "v_fmac_f32 v124, v122, v122\n"
    "v_fmac_f32 v124, v123, v123\n"
    "v_mfma_f32_16x16x32_bf16 v[44:47], v[92:95], a[96:99], v[44:47]\n"
    "ds_read_b64_tr_b16 v[92:93], v11 offset:41472\n"
    "ds_read_b64_tr_b16 v[94:95], v11 offset:41728\n"
    "s_waitcnt lgkmcnt(15)\n"
    "v_mfma_f32_16x16x32_bf16 v[16:19], v[96:99], a[36:39], v[16:19]\n"
    "v_add_f32_dpp v124, v124, v124 quad_perm:[1,0,3,2] row_mask:0xf bank_mask:0xf\n"
    "s_nop 1\n"
    "v_add_f32_dpp v124, v124, v124 quad_perm:[2,3,0,1] row_mask:0xf bank_mask:0xf\n"
    "s_nop 1\n"
    "v_add_f32_dpp v124, v124, v124 row_half_mirror row_mask:0xf bank_mask:0xf\n"
    "v_mfma_f32_16x16x32_bf16 v[32:35], v[96:99], a[100:103], v[32:35]\n"
    "ds_read_b64_tr_b16 v[96:97], v10 offset:45056\n"
    "ds_read_b64_tr_b16 v[98:99], v10 offset:45312\n"
    "v_mfma_f32_16x16x32_bf16 v[20:23], v[100:103], a[36:39], v[20:23]\n"
    "v_add_f32_dpp v124, v124, v124 row_mirror row_mask:0xf bank_mask:0xf\n"
    "v_mov_b32 v125, v124\n"
    "s_nop 1\n"
    "v_permlane16_swap_b32 v124, v125\n"
    "v_mfma_f32_16x16x32_bf16 v[36:39], v[100:103], a[100:103], v[36:39]\n"
    "ds_read_b64_tr_b16 v[100:101], v11 offset:45056\n"
    "ds_read_b64_tr_b16 v[102:103], v11 offset:45312\n"
    "s_waitcnt lgkmcnt(15)\n"
    "v_mfma_f32_16x16x32_bf16 v[24:27], v[104:107], a[36:39], v[24:27]\n"
    "v_add_f32 v124, v124, v125\n"
    "v_mov_b32 v125, v124\n"
    "s_nop 1\n"
    "v_permlane32_swap_b32 v124, v125\n"
    "v_mfma_f32_16x16x32_bf16 v[40:43], v[104:107], a[100:103], v[40:43]\n"
    "ds_read_b64_tr_b16 v[104:105], v10 offset:45568\n"
    "ds_read_b64_tr_b16 v[106:107], v10 offset:45824\n"
    "v_mfma_f32_16x16x32_bf16 v[28:31], v[108:111], a[36:39], v[28:31]\n"
    "v_add_f32 v124, v124, v125\n"
    "v_mov_b32 v125, 0x3727c5ac\n"
    "v_fmac_f32 v125, 0x3b000000, v124\n"
    "v_mfma_f32_16x16x32_bf16 v[44:47], v[108:111], a[100:103], v[44:47]\n"
    "ds_read_b64_tr_b16 v[108:109], v11 offset:45568\n"
    "ds_read_b64_tr_b16 v[110:111], v11 offset:45824\n"
    "s_barrier\n"
    "s_waitcnt lgkmcnt(12)\n"
    "v_mfma_f32_16x16x32_bf16 v[16:19], v[80:83], a[40:43], v[16:19]\n"
    "s_add_u32 m0, s63, 16384\n"
    "s_mov_b32 s67, 1048704\n"
    "buffer_load_dwordx4 v5, s[28:31], s67 offen lds\n"
    "v_mfma_f32_16x16x32_bf16 v[32:35], v[80:83], a[104:107], v[32:35]\n"
    "ds_read_b64_tr_b16 v[80:81], v10 offset:49152\n"
    "ds_read_b64_tr_b16 v[82:83], v10 offset:49408\n"
    "v_mfma_f32_16x16x32_bf16 v[20:23], v[84:87], a[40:43], v[20:23]\n"
    "s_add_u32 m0, s63, 17408\n"
    "s_mov_b32 s67, 1114240\n"
    "buffer_load_dwordx4 v6, s[28:31], s67 offen lds\n"
    "v_mfma_f32_16x16x32_bf16 v[36:39], v[84:87], a[104:107], v[36:39]\n"
    "ds_read_b64_tr_b16 v[84:85], v11 offset:49152\n"
    "ds_read_b64_tr_b16 v[86:87], v11 offset:49408\n"
    "s_waitcnt lgkmcnt(12)\n"
    "v_mfma_f32_16x16x32_bf16 v[24:27], v[88:91], a[40:43], v[24:27]\n"
    "s_waitcnt vmcnt(2)\n"
    "ds_read_b128 a[56:59], v8 offset:32768\n"
    "ds_read_b128 a[60:63], v9 offset:32768\n"
    "ds_read_b128 a[120:123], v8 offset:34816\n"
    "v_mfma_f32_16x16x32_bf16 v[40:43], v[88:91], a[104:107], v[40:43]\n"
    "ds_read_b64_tr_b16 v[88:89], v10 offset:49664\n"
    "ds_read_b64_tr_b16 v[90:91], v10 offset:49920\n"
    "v_mfma_f32_16x16x32_bf16 v[28:31], v[92:95], a[40:43], v[28:31]\n"
    "ds_read_b128 a[124:127], v9 offset:34816\n"
    "v_rsq_f32 v124, v125\n"
    "s_nop 0\n"
    "v_mul_f32 v116, v116, v124\n"
    "v_mfma_f32_16x16x32_bf16 v[44:47], v[92:95], a[104:107], v[44:47]\n"
    "ds_read_b64_tr_b16 v[92:93], v11 offset:49664\n"
    "ds_read_b64_tr_b16 v[94:95], v11 offset:49920\n"
    "s_waitcnt lgkmcnt(15)\n"
    "v_mfma_f32_16x16x32_bf16 v[16:19], v[96:99], a[44:47], v[16:19]\n"
    "v_mul_f32 v117, v117, v124\n"
    "v_mul_f32 v118, v118, v124\n"
    "v_mul_f32 v119, v119, v124\n"
    "v_mfma_f32_16x16x32_bf16 v[32:35], v[96:99], a[108:111], v[32:35]\n"
    "ds_read_b64_tr_b16 v[96:97], v10 offset:53248\n"
    "ds_read_b64_tr_b16 v[98:99], v10 offset:53504\n"
    "v_mfma_f32_16x16x32_bf16 v[20:23], v[100:103], a[44:47], v[20:23]\n"
    "v_mul_f32 v120, v120, v124\n"
    "v_mul_f32 v121, v121, v124\n"
    "v_mul_f32 v122, v122, v124\n"
    "v_mfma_f32_16x16x32_bf16 v[36:39], v[100:103], a[108:111], v[36:39]\n"
    "ds_read_b64_tr_b16 v[100:101], v11 offset:53248\n"
    "ds_read_b64_tr_b16 v[102:103], v11 offset:53504\n"
    "s_waitcnt lgkmcnt(15)\n"
    "v_mfma_f32_16x16x32_bf16 v[24:27], v[104:107], a[44:47], v[24:27]\n"
    "v_mul_f32 v123, v123, v124\n"
    "v_cvt_f16_f32 v116, v116\n"
    "v_cvt_f16_f32 v117, v117\n"
    "v_mfma_f32_16x16x32_bf16 v[40:43], v[104:107], a[108:111], v[40:43]\n"
    "ds_read_b64_tr_b16 v[104:105], v10 offset:53760\n"
    "ds_read_b64_tr_b16 v[106:107], v10 offset:54016\n"
    "v_mfma_f32_16x16x32_bf16 v[28:31], v[108:111], a[44:47], v[28:31]\n"
    "v_cvt_f16_f32 v118, v118\n"
    "v_cvt_f16_f32 v119, v119\n"
    "v_cvt_f16_f32 v120, v120\n"
    "v_mfma_f32_16x16x32_bf16 v[44:47], v[108:111], a[108:111], v[44:47]\n"
    "ds_read_b64_tr_b16 v[108:109], v11 offset:53760\n"
    "ds_read_b64_tr_b16 v[110:111], v11 offset:54016\n"
    "s_waitcnt lgkmcnt(15)\n"
    "v_mfma_f32_16x16x32_bf16 v[16:19], v[80:83], a[48:51], v[16:19]\n"
    "v_cvt_f16_f32 v121, v121\n"
    "v_cvt_f16_f32 v122, v122\n"
    "v_cvt_f16_f32 v123, v123\n"
    "v_mfma_f32_16x16x32_bf16 v[32:35], v[80:83], a[112:115], v[32:35]\n"
    "ds_read_b64_tr_b16 v[80:81], v10 offset:57344\n"
    "ds_read_b64_tr_b16 v[82:83], v10 offset:57600\n"
    "v_mfma_f32_16x16x32_bf16 v[20:23], v[84:87], a[48:51], v[20:23]\n"
    "v_pack_b32_f16 v112, v116, v117\n"
    "v_pack_b32_f16 v113, v118, v119\n"
    "v_pack_b32_f16 v114, v120, v121\n"
    "v_mfma_f32_16x16x32_bf16 v[36:39], v[84:87], a[112:115], v[36:39]\n"
    "ds_read_b64_tr_b16 v[84:85], v11 offset:57344\n"
    "ds_read_b64_tr_b16 v[86:87], v11 offset:57600\n"
    "s_waitcnt lgkmcnt(12)\n"
    "v_mfma_f32_16x16x32_bf16 v[24:27], v[88:91], a[48:51], v[24:27]\n"
    "v_pack_b32_f16 v115, v122, v123\n"
    "v_mov_b32 v72, v112\n"
    "v_mov_b32 v73, v113\n"
    "v_mfma_f32_16x16x32_bf16 v[40:43], v[88:91], a[112:115], v[40:43]\n"
    "ds_read_b64_tr_b16 v[88:89], v10 offset:57856\n"
    "ds_read_b64_tr_b16 v[90:91], v10 offset:58112\n"
    "v_mfma_f32_16x16x32_bf16 v[28:31], v[92:95], a[48:51], v[28:31]\n"
    "v_mov_b32 v74, v114\n"
    "v_mov_b32 v75, v115\n"
    "s_and_b32 s66, %1, 7\n"
    "v_mfma_f32_16x16x32_bf16 v[44:47], v[92:95], a[112:115], v[44:47]\n"
    "ds_read_b64_tr_b16 v[92:93], v11 offset:57856\n"
    "ds_read_b64_tr_b16 v[94:95], v11 offset:58112\n"
    "s_waitcnt lgkmcnt(12)\n"
    "v_mfma_f32_16x16x32_bf16 v[16:19], v[96:99], a[52:55], v[16:19]\n"
    "s_mul_i32 s66, s66, 24\n"
    "s_lshr_b32 s23, %1, 3\n"
    "s_add_u32 s69, s66, s23\n"
    "v_mfma_f32_16x16x32_bf16 v[32:35], v[96:99], a[116:119], v[32:35]\n"
    "ds_read_b64_tr_b16 v[96:97], v10 offset:61440\n"
    "ds_read_b64_tr_b16 v[98:99], v10 offset:61696\n"
    "v_mfma_f32_16x16x32_bf16 v[20:23], v[100:103], a[52:55], v[20:23]\n"
    "s_lshl_b32 s66, s69, 5\n"
    "s_mul_i32 s23, s60, 393216\n"
    "s_add_u32 s66, s66, s23\n"
    "v_mfma_f32_16x16x32_bf16 v[36:39], v[100:103], a[116:119], v[36:39]\n"
    "ds_read_b64_tr_b16 v[100:101], v11 offset:61440\n"
    "ds_read_b64_tr_b16 v[102:103], v11 offset:61696\n"
    "s_waitcnt lgkmcnt(12)\n"
    "v_mfma_f32_16x16x32_bf16 v[24:27], v[104:107], a[52:55], v[24:27]\n"
    "s_cmp_lt_u32 %1, 192\n"
    "s_cselect_b32 s68, 393216, 0\n"
    "s_cselect_b32 s70, 64, 0\n"
    "s_cselect_b32 s66, s66, 0\n"
    "s_cselect_b32 s69, s69, 0\n"
    "v_mfma_f32_16x16x32_bf16 v[40:43], v[104:107], a[116:119], v[40:43]\n"
    "ds_read_b64_tr_b16 v[104:105], v10 offset:61952\n"
    "ds_read_b64_tr_b16 v[106:107], v10 offset:62208\n"
    "v_mfma_f32_16x16x32_bf16 v[28:31], v[108:111], a[52:55], v[28:31]\n"
    "s_mov_b64 s[52:53], s[18:19]\n"
    "s_add_u32 s52, s52, s66\n"
    "s_addc_u32 s53, s53, 0\n"
    "v_mfma_f32_16x16x32_bf16 v[44:47], v[108:111], a[116:119], v[44:47]\n"
    "ds_read_b64_tr_b16 v[108:109], v11 offset:61952\n"
    "ds_read_b64_tr_b16 v[110:111], v11 offset:62208\n"
    "s_barrier\n"
    "s_waitcnt lgkmcnt(12)\n"
    "v_mfma_f32_16x16x32_bf16 v[16:19], v[80:83], a[56:59], v[16:19]\n"
    "s_add_u32 m0, s63, 32768\n"
    "s_mov_b32 s67, 2097280\n"
    "buffer_load_dwordx4 v5, s[28:31], s67 offen lds\n"
    "v_mfma_f32_16x16x32_bf16 v[32:35], v[80:83], a[120:123], v[32:35]\n"
    "ds_read_b64_tr_b16 v[80:81], v10 offset:0\n"
    "ds_read_b64_tr_b16 v[82:83], v10 offset:256\n"
    "v_mfma_f32_16x16x32_bf16 v[20:23], v[84:87], a[56:59], v[20:23]\n"
    "s_add_u32 m0, s63, 33792\n"
    "s_mov_b32 s67, 2162816\n"
    "buffer_load_dwordx4 v6, s[28:31], s67 offen lds\n"
    "v_mfma_f32_16x16x32_bf16 v[36:39], v[84:87], a[120:123], v[36:39]\n"
    "ds_read_b64_tr_b16 v[84:85], v11 offset:0\n"
    "ds_read_b64_tr_b16 v[86:87], v11 offset:256\n"
    "s_waitcnt lgkmcnt(12)\n"
    "v_mfma_f32_16x16x32_bf16 v[24:27], v[88:91], a[56:59], v[24:27]\n"
    "s_add_u32 m0, s63, 65536\n"
    "s_mov_b32 s67, 3145856\n"
    "buffer_load_dwordx4 v5, s[28:31], s67 offen lds\n"
    "v_mfma_f32_16x16x32_bf16 v[40:43], v[88:91], a[120:123], v[40:43]\n"
    "ds_read_b64_tr_b16 v[88:89], v10 offset:512\n"
    "ds_read_b64_tr_b16 v[90:91], v10 offset:768\n"
    "v_mfma_f32_16x16x32_bf16 v[28:31], v[92:95], a[56:59], v[28:31]\n"
    "s_add_u32 m0, s63, 66560\n"
    "s_mov_b32 s67, 3211392\n"
    "buffer_load_dwordx4 v6, s[28:31], s67 offen lds\n"
    "v_mfma_f32_16x16x32_bf16 v[44:47], v[92:95], a[120:123], v[44:47]\n"
    "ds_read_b64_tr_b16 v[92:93], v11 offset:512\n"
    "ds_read_b64_tr_b16 v[94:95], v11 offset:768\n"
    "s_waitcnt lgkmcnt(12)\n"
    "v_mfma_f32_16x16x32_bf16 v[16:19], v[96:99], a[60:63], v[16:19]\n"
    "s_and_b32 s53, s53, 0xffff\n"
    "s_mov_b32 s54, s68\n"
    "s_mov_b32 s55, 0x20000\n"
    "v_mfma_f32_16x16x32_bf16 v[32:35], v[96:99], a[124:127], v[32:35]\n"
    "ds_read_b64_tr_b16 v[96:97], v10 offset:4096\n"
    "ds_read_b64_tr_b16 v[98:99], v10 offset:4352\n"
    "v_mfma_f32_16x16x32_bf16 v[20:23], v[100:103], a[60:63], v[20:23]\n"
    "s_lshl_b32 s66, s69, 6\n"
    "s_mov_b64 s[56:57], s[96:97]\n"
    "s_add_u32 s56, s56, s66\n"
    "s_addc_u32 s57, s57, 0\n"
    "v_mfma_f32_16x16x32_bf16 v[36:39], v[100:103], a[124:127], v[36:39]\n"
    "ds_read_b64_tr_b16 v[100:101], v11 offset:4096\n"
    "ds_read_b64_tr_b16 v[102:103], v11 offset:4352\n"
    "s_waitcnt lgkmcnt(12)\n"
    "v_mfma_f32_16x16x32_bf16 v[24:27], v[104:107], a[60:63], v[24:27]\n"
    "s_and_b32 s57, s57, 0xffff\n"
    "s_mov_b32 s58, s70\n"
    "s_mov_b32 s59, 0x20000\n"
    "v_mfma_f32_16x16x32_bf16 v[40:43], v[104:107], a[124:127], v[40:43]\n"
    "ds_read_b64_tr_b16 v[104:105], v10 offset:4608\n"
    "ds_read_b64_tr_b16 v[106:107], v10 offset:4864\n"
    "v_mfma_f32_16x16x32_bf16 v[28:31], v[108:111], a[60:63], v[28:31]\n"
    "v_lshrrev_b32 v120, 1, v3\n"
    "v_mul_u32_u24 v120, 6144, v120\n"
    "v_and_b32 v121, 1, v3\n"
    "v_mfma_f32_16x16x32_bf16 v[44:47], v[108:111], a[124:127], v[44:47]\n"
    "ds_read_b64_tr_b16 v[108:109], v11 offset:4608\n"
    "ds_read_b64_tr_b16 v[110:111], v11 offset:4864\n"
    "v_cvt_pk_bf16_f32 v48, v16, v17\n"
    "v_cvt_pk_bf16_f32 v49, v18, v19\n"
    "v_cvt_pk_bf16_f32 v50, v20, v21\n"
    "v_cvt_pk_bf16_f32 v51, v22, v23\n"
    "v_cvt_pk_bf16_f32 v52, v24, v25\n"
    "v_cvt_pk_bf16_f32 v53, v26, v27\n"
    "v_cvt_pk_bf16_f32 v54, v28, v29\n"
    "v_cvt_pk_bf16_f32 v55, v30, v31\n"
    "v_cvt_pk_bf16_f32 v56, v32, v33\n"
    "v_cvt_pk_bf16_f32 v57, v34, v35\n"
    "v_cvt_pk_bf16_f32 v58, v36, v37\n"
    "v_cvt_pk_bf16_f32 v59, v38, v39\n"
    "v_cvt_pk_bf16_f32 v60, v40, v41\n"
    "v_cvt_pk_bf16_f32 v61, v42, v43\n"
    "v_cvt_pk_bf16_f32 v62, v44, v45\n"
    "v_cvt_pk_bf16_f32 v63, v46, v47\n"
    "s_waitcnt lgkmcnt(12)\n"
    "v_mfma_f32_16x16x32_bf16 v[16:19], v[80:83], a[0:3], 0\n"
    "v_lshl_add_u32 v120, v121, 4, v120\n"
    "s_mov_b32 s23, 196608\n"
    "s_add_u32 s22, s22, 65536\n"
    "v_mfma_f32_16x16x32_bf16 v[32:35], v[80:83], a[64:67], 0\n"
    "ds_read_b64_tr_b16 v[80:81], v10 offset:8192\n"
    "ds_read_b64_tr_b16 v[82:83], v10 offset:8448\n"
    "v_mfma_f32_16x16x32_bf16 v[20:23], v[84:87], a[0:3], 0\n"
    "s_mov_b32 m0, s22\n"
    "s_nop 0\n"
    "buffer_load_dwordx4 v120, s[52:55], 0 offen lds\n"
    "s_add_u32 m0, s22, 1024\n"
    "s_nop 0\n"
    "buffer_load_dwordx4 v120, s[52:55], s23 offen lds\n"
    "v_mfma_f32_16x16x32_bf16 v[36:39], v[84:87], a[64:67], 0\n"
    "ds_read_b64_tr_b16 v[84:85], v11 offset:8192\n"
    "ds_read_b64_tr_b16 v[86:87], v11 offset:8448\n"
    "s_waitcnt lgkmcnt(12)\n"
    "v_mfma_f32_16x16x32_bf16 v[24:27], v[88:91], a[0:3], 0\n"
    "v_lshrrev_b32 v7, 4, v3\n"
    "v_lshlrev_b32 v7, 8, v7\n"
    "v_bfe_u32 v121, v3, 2, 2\n"
    "v_mfma_f32_16x16x32_bf16 v[40:43], v[88:91], a[64:67], 0\n"
    "ds_read_b64_tr_b16 v[88:89], v10 offset:8704\n"
    "ds_read_b64_tr_b16 v[90:91], v10 offset:8960\n"
    "v_mfma_f32_16x16x32_bf16 v[28:31], v[92:95], a[0:3], 0\n"
    "s_mov_b32 s71, 0\n"
    "buffer_store_dwordx4 v[48:51], v2, s[36:39], s71 offen offset:0 sc1\n"
    "v_mfma_f32_16x16x32_bf16 v[44:47], v[92:95], a[64:67], 0\n"
    "ds_read_b64_tr_b16 v[92:93], v11 offset:8704\n"
    "ds_read_b64_tr_b16 v[94:95], v11 offset:8960\n"
    "s_waitcnt lgkmcnt(12)\n"
    "v_mfma_f32_16x16x32_bf16 v[16:19], v[96:99], a[4:7], v[16:19]\n"
    "v_lshl_add_u32 v7, v121, 5, v7\n"
    "v_and_b32 v121, 3, v3\n"
    "v_lshl_add_u32 v7, v121, 3, v7\n"
    "v_mfma_f32_16x16x32_bf16 v[32:35], v[96:99], a[68:71], v[32:35]\n"
    "ds_read_b64_tr_b16 v[96:97], v10 offset:12288\n"
    "ds_read_b64_tr_b16 v[98:99], v10 offset:12544\n"
    "v_mfma_f32_16x16x32_bf16 v[20:23], v[100:103], a[4:7], v[20:23]\n"
    "v_add_u32 v7, s22, v7\n"
    "v_lshrrev_b32 v126, 3, v3\n"
    "v_lshlrev_b32 v126, 12, v126\n"
    "v_mfma_f32_16x16x32_bf16 v[36:39], v[100:103], a[68:71], v[36:39]\n"
    "ds_read_b64_tr_b16 v[100:101], v11 offset:12288\n"
    "ds_read_b64_tr_b16 v[102:103], v11 offset:12544\n"
    "s_waitcnt lgkmcnt(12)\n"
    "v_mfma_f32_16x16x32_bf16 v[24:27], v[104:107], a[4:7], v[24:27]\n"
    "v_and_b32 v127, 7, v3\n"
    "v_lshl_add_u32 v126, v127, 7, v126\n"
    "s_lshl_b32 s66, s60, 4\n"
    "v_mfma_f32_16x16x32_bf16 v[40:43], v[104:107], a[68:71], v[40:43]\n"
    "ds_read_b64_tr_b16 v[104:105], v10 offset:12800\n"
    "ds_read_b64_tr_b16 v[106:107], v10 offset:13056\n"
    "v_mfma_f32_16x16x32_bf16 v[28:31], v[108:111], a[4:7], v[28:31]\n"
    "s_add_u32 s66, s66, 133120\n"
    "v_add_u32 v126, s66, v126\n"
    "ds_write_b128 v126, v[72:75]\n"
    "v_mfma_f32_16x16x32_bf16 v[44:47], v[108:111], a[68:71], v[44:47]\n"
    "ds_read_b64_tr_b16 v[108:109], v11 offset:12800\n"
    "ds_read_b64_tr_b16 v[110:111], v11 offset:13056\n"
    "s_waitcnt vmcnt(7)\n"
    "s_barrier\n"
    "s_waitcnt lgkmcnt(13)\n"
    "v_mfma_f32_16x16x32_bf16 v[16:19], v[80:83], a[8:11], v[16:19]\n"
    "s_add_u32 m0, s63, 81920\n"
    "s_mov_b32 s67, 0\n"
    "buffer_load_dwordx4 v5, s[32:35], s67 offen lds\n"
    "v_mfma_f32_16x16x32_bf16 v[32:35], v[80:83], a[72:75], v[32:35]\n"
    "ds_read_b64_tr_b16 v[80:81], v10 offset:16384\n"
    "ds_read_b64_tr_b16 v[82:83], v10 offset:16640\n"
    "v_mfma_f32_16x16x32_bf16 v[20:23], v[84:87], a[8:11], v[20:23]\n"
    "s_add_u32 m0, s63, 82944\n"
    "s_mov_b32 s67, 65536\n"
    "buffer_load_dwordx4 v6, s[32:35], s67 offen lds\n"
    "v_mfma_f32_16x16x32_bf16 v[36:39], v[84:87], a[72:75], v[36:39]\n"
    "ds_read_b64_tr_b16 v[84:85], v11 offset:16384\n"
    "ds_read_b64_tr_b16 v[86:87], v11 offset:16640\n"
    "s_waitcnt lgkmcnt(13)\n"
    "v_mfma_f32_16x16x32_bf16 v[24:27], v[88:91], a[8:11], v[24:27]\n"
    "s_add_u32 m0, s63, 98304\n"
    "s_mov_b32 s67, 1048576\n"
    "buffer_load_dwordx4 v5, s[32:35], s67 offen lds\n"
    "v_mfma_f32_16x16x32_bf16 v[40:43], v[88:91], a[72:75], v[40:43]\n"
    "ds_read_b64_tr_b16 v[88:89], v10 offset:16896\n"
    "ds_read_b64_tr_b16 v[90:91], v10 offset:17152\n"
    "v_mfma_f32_16x16x32_bf16 v[28:31], v[92:95], a[8:11], v[28:31]\n"
    "s_add_u32 m0, s63, 99328\n"
    "s_mov_b32 s67, 1114112\n"
    "buffer_load_dwordx4 v6, s[32:35], s67 offen lds\n"
    "v_mfma_f32_16x16x32_bf16 v[44:47], v[92:95], a[72:75], v[44:47]\n"
    "ds_read_b64_tr_b16 v[92:93], v11 offset:16896\n"
    "ds_read_b64_tr_b16 v[94:95], v11 offset:17152\n"
    "s_waitcnt lgkmcnt(13)\n"
    "v_mfma_f32_16x16x32_bf16 v[16:19], v[96:99], a[12:15], v[16:19]\n"
    "v_mfma_f32_16x16x32_bf16 v[32:35], v[96:99], a[76:79], v[32:35]\n"
    "ds_read_b64_tr_b16 v[96:97], v10 offset:20480\n"
    "ds_read_b64_tr_b16 v[98:99], v10 offset:20736\n"
    "v_mfma_f32_16x16x32_bf16 v[20:23], v[100:103], a[12:15], v[20:23]\n"
    "v_mfma_f32_16x16x32_bf16 v[36:39], v[100:103], a[76:79], v[36:39]\n"
    "ds_read_b64_tr_b16 v[100:101], v11 offset:20480\n"
    "ds_read_b64_tr_b16 v[102:103], v11 offset:20736\n"
    "s_waitcnt lgkmcnt(12)\n"
    "v_mfma_f32_16x16x32_bf16 v[24:27], v[104:107], a[12:15], v[24:27]\n"
    "v_mfma_f32_16x16x32_bf16 v[40:43], v[104:107], a[76:79], v[40:43]\n"
    "ds_read_b64_tr_b16 v[104:105], v10 offset:20992\n"
    "ds_read_b64_tr_b16 v[106:107], v10 offset:21248\n"
    "v_mfma_f32_16x16x32_bf16 v[28:31], v[108:111], a[12:15], v[28:31]\n"
    "v_mfma_f32_16x16x32_bf16 v[44:47], v[108:111], a[76:79], v[44:47]\n"
    "ds_read_b64_tr_b16 v[108:109], v11 offset:20992\n"
    "ds_read_b64_tr_b16 v[110:111], v11 offset:21248\n"
    "s_waitcnt lgkmcnt(12)\n"
    "v_mfma_f32_16x16x32_bf16 v[16:19], v[80:83], a[16:19], v[16:19]\n"
    "s_waitcnt vmcnt(5)\n"
    "ds_read_b64_tr_b16 v[124:125], v7 offset:0\n"
    "ds_read_b64_tr_b16 v[126:127], v7 offset:128\n"
    "s_waitcnt lgkmcnt(0)\n"
    "v_mfma_f32_16x16x32_bf16 v[120:123], v[124:127], v[64:67], 0\n"
    "v_mfma_f32_16x16x32_bf16 v[32:35], v[80:83], a[80:83], v[32:35]\n"
    "ds_read_b64_tr_b16 v[80:81], v10 offset:24576\n"
    "ds_read_b64_tr_b16 v[82:83], v10 offset:24832\n"
    "v_mfma_f32_16x16x32_bf16 v[20:23], v[84:87], a[16:19], v[20:23]\n"
    "ds_read_b64_tr_b16 v[124:125], v7 offset:1024\n"
    "ds_read_b64_tr_b16 v[126:127], v7 offset:1152\n"
    "s_waitcnt lgkmcnt(0)\n"
    "v_mfma_f32_16x16x32_bf16 v[120:123], v[124:127], v[68:71], v[120:123]\n"
    "v_mfma_f32_16x16x32_bf16 v[36:39], v[84:87], a[80:83], v[36:39]\n"
    "ds_read_b64_tr_b16 v[84:85], v11 offset:24576\n"
    "ds_read_b64_tr_b16 v[86:87], v11 offset:24832\n"
    "v_mfma_f32_16x16x32_bf16 v[24:27], v[88:91], a[16:19], v[24:27]\n"
    "v_add_u32 v7, s22, v2\n"
    "s_nop 2\n"
    "ds_write_b128 v7, v[120:123]\n"
    "v_mfma_f32_16x16x32_bf16 v[40:43], v[88:91], a[80:83], v[40:43]\n"
    "ds_read_b64_tr_b16 v[88:89], v10 offset:25088\n"
    "ds_read_b64_tr_b16 v[90:91], v10 offset:25344\n"
    "v_mfma_f32_16x16x32_bf16 v[28:31], v[92:95], a[16:19], v[28:31]\n"
    "s_mov_b32 s71, 0\n"
    "buffer_store_dwordx4 v[52:55], v2, s[36:39], s71 offen offset:1024 sc1\n"
    "v_mfma_f32_16x16x32_bf16 v[44:47], v[92:95], a[80:83], v[44:47]\n"
    "ds_read_b64_tr_b16 v[92:93], v11 offset:25088\n"
    "ds_read_b64_tr_b16 v[94:95], v11 offset:25344\n"
    "v_mfma_f32_16x16x32_bf16 v[16:19], v[96:99], a[20:23], v[16:19]\n"
    "v_mfma_f32_16x16x32_bf16 v[32:35], v[96:99], a[84:87], v[32:35]\n"
    "ds_read_b64_tr_b16 v[96:97], v10 offset:28672\n"
    "ds_read_b64_tr_b16 v[98:99], v10 offset:28928\n"
    "v_mfma_f32_16x16x32_bf16 v[20:23], v[100:103], a[20:23], v[20:23]\n"
    "v_mfma_f32_16x16x32_bf16 v[36:39], v[100:103], a[84:87], v[36:39]\n"
    "ds_read_b64_tr_b16 v[100:101], v11 offset:28672\n"
    "ds_read_b64_tr_b16 v[102:103], v11 offset:28928\n"
    "v_mfma_f32_16x16x32_bf16 v[24:27], v[104:107], a[20:23], v[24:27]\n"
    "v_mfma_f32_16x16x32_bf16 v[40:43], v[104:107], a[84:87], v[40:43]\n"
    "ds_read_b64_tr_b16 v[104:105], v10 offset:29184\n"
    "ds_read_b64_tr_b16 v[106:107], v10 offset:29440\n"
    "v_mfma_f32_16x16x32_bf16 v[28:31], v[108:111], a[20:23], v[28:31]\n"
    "v_mfma_f32_16x16x32_bf16 v[44:47], v[108:111], a[84:87], v[44:47]\n"
    "ds_read_b64_tr_b16 v[108:109], v11 offset:29184\n"
    "ds_read_b64_tr_b16 v[110:111], v11 offset:29440\n"
    "s_waitcnt lgkmcnt(12)\n"
    "s_barrier\n"
    "v_mfma_f32_16x16x32_bf16 v[16:19], v[80:83], a[24:27], v[16:19]\n"
    "s_add_u32 m0, s63, 114688\n"
    "s_mov_b32 s67, 2097152\n"
    "buffer_load_dwordx4 v5, s[32:35], s67 offen lds\n"
    "v_mfma_f32_16x16x32_bf16 v[32:35], v[80:83], a[88:91], v[32:35]\n"
    "ds_read_b64_tr_b16 v[80:81], v10 offset:32768\n"
    "ds_read_b64_tr_b16 v[82:83], v10 offset:33024\n"
    "v_mfma_f32_16x16x32_bf16 v[20:23], v[84:87], a[24:27], v[20:23]\n"
    "s_add_u32 m0, s63, 115712\n"
    "s_mov_b32 s67, 2162688\n"
    "buffer_load_dwordx4 v6, s[32:35], s67 offen lds\n"
    "v_mfma_f32_16x16x32_bf16 v[36:39], v[84:87], a[88:91], v[36:39]\n"
    "ds_read_b64_tr_b16 v[84:85], v11 offset:32768\n"
    "ds_read_b64_tr_b16 v[86:87], v11 offset:33024\n"
    "s_waitcnt lgkmcnt(12)\n"
    "v_mfma_f32_16x16x32_bf16 v[24:27], v[88:91], a[24:27], v[24:27]\n"
    "ds_read_b128 v[124:127], v7 offset:2048\n"
    "v_lshrrev_b32 v122, 3, v3\n"
    "v_lshlrev_b32 v122, 8, v122\n"
    "v_mfma_f32_16x16x32_bf16 v[40:43], v[88:91], a[88:91], v[40:43]\n"
    "ds_read_b64_tr_b16 v[88:89], v10 offset:33280\n"
    "ds_read_b64_tr_b16 v[90:91], v10 offset:33536\n"
    "v_mfma_f32_16x16x32_bf16 v[28:31], v[92:95], a[24:27], v[28:31]\n"
    "s_mov_b32 s71, 4096\n"
    "buffer_store_dwordx4 v[56:59], v2, s[36:39], s71 offen offset:0 sc1\n"
    "v_mfma_f32_16x16x32_bf16 v[44:47], v[92:95], a[88:91], v[44:47]\n"
    "ds_read_b64_tr_b16 v[92:93], v11 offset:33280\n"
    "ds_read_b64_tr_b16 v[94:95], v11 offset:33536\n"
    "s_waitcnt lgkmcnt(13)\n"
    "v_mfma_f32_16x16x32_bf16 v[16:19], v[96:99], a[28:31], v[16:19]\n"
    "v_and_b32 v123, 7, v3\n"
    "v_lshl_add_u32 v122, v123, 4, v122\n"
    "s_waitcnt lgkmcnt(4)\n"
    "buffer_store_dwordx4 v[124:127], v122, s[44:47], 0 offen sc1\n"
    "v_mfma_f32_16x16x32_bf16 v[32:35], v[96:99], a[92:95], v[32:35]\n"
    "ds_read_b64_tr_b16 v[96:97], v10 offset:36864\n"
    "ds_read_b64_tr_b16 v[98:99], v10 offset:37120\n"
    "v_mfma_f32_16x16x32_bf16 v[20:23], v[100:103], a[28:31], v[20:23]\n"
    "v_mfma_f32_16x16x32_bf16 v[36:39], v[100:103], a[92:95], v[36:39]\n"
    "ds_read_b64_tr_b16 v[100:101], v11 offset:36864\n"
    "ds_read_b64_tr_b16 v[102:103], v11 offset:37120\n"
    "v_mfma_f32_16x16x32_bf16 v[24:27], v[104:107], a[28:31], v[24:27]\n"
    "v_mfma_f32_16x16x32_bf16 v[40:43], v[104:107], a[92:95], v[40:43]\n"
    "ds_read_b64_tr_b16 v[104:105], v10 offset:37376\n"
    "ds_read_b64_tr_b16 v[106:107], v10 offset:37632\n"
    "v_mfma_f32_16x16x32_bf16 v[28:31], v[108:111], a[28:31], v[28:31]\n"
    "v_mfma_f32_16x16x32_bf16 v[44:47], v[108:111], a[92:95], v[44:47]\n"
    "ds_read_b64_tr_b16 v[108:109], v11 offset:37376\n"
    "ds_read_b64_tr_b16 v[110:111], v11 offset:37632\n"
    "s_cmp_eq_u32 s60, 0\n"
    "s_cbranch_scc0 L_nomod_%=\n"
    "ds_read_b128 v[112:115], v7 offset:0\n"
    "ds_read_b128 v[116:119], v7 offset:4096\n"
    "ds_read_b128 v[120:123], v7 offset:8192\n"
    "ds_read_b128 v[124:127], v7 offset:12288\n"
    "s_waitcnt lgkmcnt(0)\n"
    "v_add_f32 v112, v112, v116\n"
    "v_add_f32 v113, v113, v117\n"
    "v_add_f32 v114, v114, v118\n"
    "v_add_f32 v115, v115, v119\n"
    "v_add_f32 v112, v112, v120\n"
    "v_add_f32 v113, v113, v121\n"
    "v_add_f32 v114, v114, v122\n"
    "v_add_f32 v115, v115, v123\n"
    "v_add_f32 v112, v112, v124\n"
    "v_add_f32 v113, v113, v125\n"
    "v_add_f32 v114, v114, v126\n"
    "v_add_f32 v115, v115, v127\n"
    "ds_read_b128 v[116:119], v7 offset:16384\n"
    "ds_read_b128 v[120:123], v7 offset:20480\n"
    "ds_read_b128 v[124:127], v7 offset:24576\n"
    "s_waitcnt lgkmcnt(0)\n"
    "v_add_f32 v112, v112, v116\n"
    "v_add_f32 v113, v113, v117\n"
    "v_add_f32 v114, v114, v118\n"
    "v_add_f32 v115, v115, v119\n"
    "v_add_f32 v112, v112, v120\n"
    "v_add_f32 v113, v113, v121\n"
    "v_add_f32 v114, v114, v122\n"
    "v_add_f32 v115, v115, v123\n"
    "v_add_f32 v112, v112, v124\n"
    "v_add_f32 v113, v113, v125\n"
    "v_add_f32 v114, v114, v126\n"
    "v_add_f32 v115, v115, v127\n"
    "ds_read_b128 v[116:119], v7 offset:28672\n"
    "v_and_b32 v121, 15, v3\n"
    "v_lshlrev_b32 v121, 8, v121\n"
    "v_and_b32 v122, 48, v3\n"
    "v_add_u32 v121, v121, v122\n"
    "s_waitcnt lgkmcnt(0)\n"
    "v_add_f32 v112, v112, v116\n"
    "v_add_f32 v113, v113, v117\n"
    "v_add_f32 v114, v114, v118\n"
    "v_add_f32 v115, v115, v119\n"
    "v_cvt_pk_bf16_f32 v116, v112, v113\n"
    "v_cvt_pk_bf16_f32 v117, v114, v115\n"
    "v_lshlrev_b32 v112, 16, v116\n"
    "v_and_b32 v113, s64, v116\n"
    "v_lshlrev_b32 v114, 16, v117\n"
    "v_and_b32 v115, s64, v117\n"
    "buffer_store_dwordx4 v[112:115], v121, s[56:59], 0 offen sc1\n"
    "L_nomod_%=:\n"
    "s_cmp_eq_u32 s60, 0\n"
    "s_cbranch_scc0 L_hsC_%=\n"
    "v_add_u32 v120, 51200, v2\n"
    "s_lshl_b32 s66, %1, 10\n"
    "s_add_u32 s66, s66, 393216\n"
    "v_add_u32 v121, s66, v2\n"
    "global_load_dwordx4 v[116:119], v120, s[96:97] sc1\n"
    "L_hsC_%=:\n"
    "v_mfma_f32_16x16x32_bf16 v[16:19], v[80:83], a[32:35], v[16:19]\n"
    "v_mfma_f32_16x16x32_bf16 v[32:35], v[80:83], a[96:99], v[32:35]\n"
    "ds_read_b64_tr_b16 v[80:81], v10 offset:40960\n"
    "ds_read_b64_tr_b16 v[82:83], v10 offset:41216\n"
    "v_mfma_f32_16x16x32_bf16 v[20:23], v[84:87], a[32:35], v[20:23]\n"
    "v_mfma_f32_16x16x32_bf16 v[36:39], v[84:87], a[96:99], v[36:39]\n"
    "ds_read_b64_tr_b16 v[84:85], v11 offset:40960\n"
    "ds_read_b64_tr_b16 v[86:87], v11 offset:41216\n"
    "s_waitcnt lgkmcnt(12)\n"
    "v_mfma_f32_16x16x32_bf16 v[24:27], v[88:91], a[32:35], v[24:27]\n"
    "v_mfma_f32_16x16x32_bf16 v[40:43], v[88:91], a[96:99], v[40:43]\n"
    "ds_read_b64_tr_b16 v[88:89], v10 offset:41472\n"
    "ds_read_b64_tr_b16 v[90:91], v10 offset:41728\n"
    "v_mfma_f32_16x16x32_bf16 v[28:31], v[92:95], a[32:35], v[28:31]\n"
    "s_mov_b32 s71, 4096\n"
    "buffer_store_dwordx4 v[60:63], v2, s[36:39], s71 offen offset:1024 sc1\n"
    "v_mfma_f32_16x16x32_bf16 v[44:47], v[92:95], a[96:99], v[44:47]\n"
    "ds_read_b64_tr_b16 v[92:93], v11 offset:41472\n"
    "ds_read_b64_tr_b16 v[94:95], v11 offset:41728\n"
    "s_waitcnt lgkmcnt(12)\n"
    "v_mfma_f32_16x16x32_bf16 v[16:19], v[96:99], a[36:39], v[16:19]\n"
    "v_mfma_f32_16x16x32_bf16 v[32:35], v[96:99], a[100:103], v[32:35]\n"
    "ds_read_b64_tr_b16 v[96:97], v10 offset:45056\n"
    "ds_read_b64_tr_b16 v[98:99], v10 offset:45312\n"
    "v_mfma_f32_16x16x32_bf16 v[20:23], v[100:103], a[36:39], v[20:23]\n"
    "v_mfma_f32_16x16x32_bf16 v[36:39], v[100:103], a[100:103], v[36:39]\n"
    "ds_read_b64_tr_b16 v[100:101], v11 offset:45056\n"
    "ds_read_b64_tr_b16 v[102:103], v11 offset:45312\n"
    "s_waitcnt lgkmcnt(12)\n"
    "v_mfma_f32_16x16x32_bf16 v[24:27], v[104:107], a[36:39], v[24:27]\n"
    "v_mfma_f32_16x16x32_bf16 v[40:43], v[104:107], a[100:103], v[40:43]\n"
    "ds_read_b64_tr_b16 v[104:105], v10 offset:45568\n"
    "ds_read_b64_tr_b16 v[106:107], v10 offset:45824\n"
    "v_mfma_f32_16x16x32_bf16 v[28:31], v[108:111], a[36:39], v[28:31]\n"
    "v_mfma_f32_16x16x32_bf16 v[44:47], v[108:111], a[100:103], v[44:47]\n"
    "ds_read_b64_tr_b16 v[108:109], v11 offset:45568\n"
    "ds_read_b64_tr_b16 v[110:111], v11 offset:45824\n"
    "s_barrier\n"
    "s_waitcnt lgkmcnt(12)\n"
    "v_mfma_f32_16x16x32_bf16 v[16:19], v[80:83], a[40:43], v[16:19]\n"
    "s_add_u32 m0, s63, 49152\n"
    "s_mov_b32 s67, 3145728\n"
    "buffer_load_dwordx4 v5, s[32:35], s67 offen lds\n"
    "v_mfma_f32_16x16x32_bf16 v[32:35], v[80:83], a[104:107], v[32:35]\n"
    "ds_read_b64_tr_b16 v[80:81], v12 offset:0\n"
    "ds_read_b64_tr_b16 v[82:83], v12 offset:256\n"
    "v_mfma_f32_16x16x32_bf16 v[20:23], v[84:87], a[40:43], v[20:23]\n"
    "s_add_u32 m0, s63, 50176\n"
    "s_mov_b32 s67, 3211264\n"
    "buffer_load_dwordx4 v6, s[32:35], s67 offen lds\n"
    "v_mfma_f32_16x16x32_bf16 v[36:39], v[84:87], a[104:107], v[36:39]\n"
    "ds_read_b64_tr_b16 v[84:85], v13 offset:0\n"
    "ds_read_b64_tr_b16 v[86:87], v13 offset:256\n"
    "s_waitcnt lgkmcnt(12)\n"
    "v_mfma_f32_16x16x32_bf16 v[24:27], v[88:91], a[40:43], v[24:27]\n"
    "v_mfma_f32_16x16x32_bf16 v[40:43], v[88:91], a[104:107], v[40:43]\n"
    "ds_read_b64_tr_b16 v[88:89], v12 offset:512\n"
    "ds_read_b64_tr_b16 v[90:91], v12 offset:768\n"
    "v_mfma_f32_16x16x32_bf16 v[28:31], v[92:95], a[40:43], v[28:31]\n"
    "v_mfma_f32_16x16x32_bf16 v[44:47], v[92:95], a[104:107], v[44:47]\n"
    "ds_read_b64_tr_b16 v[92:93], v13 offset:512\n"
    "ds_read_b64_tr_b16 v[94:95], v13 offset:768\n"
    "s_waitcnt lgkmcnt(12)\n"
    "v_mfma_f32_16x16x32_bf16 v[16:19], v[96:99], a[44:47], v[16:19]\n"
    "v_mfma_f32_16x16x32_bf16 v[32:35], v[96:99], a[108:111], v[32:35]\n"
    "ds_read_b64_tr_b16 v[96:97], v12 offset:4096\n"
    "ds_read_b64_tr_b16 v[98:99], v12 offset:4352\n"
    "v_mfma_f32_16x16x32_bf16 v[20:23], v[100:103], a[44:47], v[20:23]\n"
    "v_mfma_f32_16x16x32_bf16 v[36:39], v[100:103], a[108:111], v[36:39]\n"
    "ds_read_b64_tr_b16 v[100:101], v13 offset:4096\n"
    "ds_read_b64_tr_b16 v[102:103], v13 offset:4352\n"
    "s_waitcnt lgkmcnt(12)\n"
    "v_mfma_f32_16x16x32_bf16 v[24:27], v[104:107], a[44:47], v[24:27]\n"
    "v_mfma_f32_16x16x32_bf16 v[40:43], v[104:107], a[108:111], v[40:43]\n"
    "ds_read_b64_tr_b16 v[104:105], v12 offset:4608\n"
    "ds_read_b64_tr_b16 v[106:107], v12 offset:4864\n"
    "v_mfma_f32_16x16x32_bf16 v[28:31], v[108:111], a[44:47], v[28:31]\n"
    "v_mfma_f32_16x16x32_bf16 v[44:47], v[108:111], a[108:111], v[44:47]\n"
    "ds_read_b64_tr_b16 v[108:109], v13 offset:4608\n"
    "ds_read_b64_tr_b16 v[110:111], v13 offset:4864\n"
    "s_waitcnt lgkmcnt(12)\n"
    "v_mfma_f32_16x16x32_bf16 v[16:19], v[80:83], a[48:51], v[16:19]\n"
    "v_mfma_f32_16x16x32_bf16 v[32:35], v[80:83], a[112:115], v[32:35]\n"
    "ds_read_b64_tr_b16 v[80:81], v12 offset:8192\n"
    "ds_read_b64_tr_b16 v[82:83], v12 offset:8448\n"
    "v_mfma_f32_16x16x32_bf16 v[20:23], v[84:87], a[48:51], v[20:23]\n"
    "v_mfma_f32_16x16x32_bf16 v[36:39], v[84:87], a[112:115], v[36:39]\n"
    "ds_read_b64_tr_b16 v[84:85], v13 offset:8192\n"
    "ds_read_b64_tr_b16 v[86:87], v13 offset:8448\n"
    "s_waitcnt lgkmcnt(12)\n"
    "v_mfma_f32_16x16x32_bf16 v[24:27], v[88:91], a[48:51], v[24:27]\n"
    "v_mfma_f32_16x16x32_bf16 v[40:43], v[88:91], a[112:115], v[40:43]\n"
    "ds_read_b64_tr_b16 v[88:89], v12 offset:8704\n"
    "ds_read_b64_tr_b16 v[90:91], v12 offset:8960\n"
    "v_mfma_f32_16x16x32_bf16 v[28:31], v[92:95], a[48:51], v[28:31]\n"
    "v_mfma_f32_16x16x32_bf16 v[44:47], v[92:95], a[112:115], v[44:47]\n"
    "ds_read_b64_tr_b16 v[92:93], v13 offset:8704\n"
    "ds_read_b64_tr_b16 v[94:95], v13 offset:8960\n"
    "s_waitcnt lgkmcnt(12)\n"
    "v_mfma_f32_16x16x32_bf16 v[16:19], v[96:99], a[52:55], v[16:19]\n"
    "v_mfma_f32_16x16x32_bf16 v[32:35], v[96:99], a[116:119], v[32:35]\n"
    "ds_read_b64_tr_b16 v[96:97], v12 offset:12288\n"
    "ds_read_b64_tr_b16 v[98:99], v12 offset:12544\n"
    "v_mfma_f32_16x16x32_bf16 v[20:23], v[100:103], a[52:55], v[20:23]\n"
    "v_mfma_f32_16x16x32_bf16 v[36:39], v[100:103], a[116:119], v[36:39]\n"
    "ds_read_b64_tr_b16 v[100:101], v13 offset:12288\n"
    "ds_read_b64_tr_b16 v[102:103], v13 offset:12544\n"
    "s_waitcnt lgkmcnt(12)\n"
    "v_mfma_f32_16x16x32_bf16 v[24:27], v[104:107], a[52:55], v[24:27]\n"
    "v_mfma_f32_16x16x32_bf16 v[40:43], v[104:107], a[116:119], v[40:43]\n"
    "ds_read_b64_tr_b16 v[104:105], v12 offset:12800\n"
    "ds_read_b64_tr_b16 v[106:107], v12 offset:13056\n"
    "v_mfma_f32_16x16x32_bf16 v[28:31], v[108:111], a[52:55], v[28:31]\n"
    "v_mfma_f32_16x16x32_bf16 v[44:47], v[108:111], a[116:119], v[44:47]\n"
    "ds_read_b64_tr_b16 v[108:109], v13 offset:12800\n"
    "ds_read_b64_tr_b16 v[110:111], v13 offset:13056\n"
    "s_waitcnt vmcnt(10)\n"
    "s_waitcnt vmcnt(3)\n"
    "s_barrier\n"
    "s_waitcnt lgkmcnt(12)\n"
    "v_mfma_f32_16x16x32_bf16 v[16:19], v[80:83], a[56:59], v[16:19]\n"
    "s_add_u32 m0, s63, 0\n"
    "s_mov_b32 s67, 128\n"
    "buffer_load_dwordx4 v5, s[32:35], s67 offen lds\n"
    "v_mfma_f32_16x16x32_bf16 v[32:35], v[80:83], a[120:123], v[32:35]\n"
    "ds_read_b64_tr_b16 v[80:81], v12 offset:16384\n"
    "ds_read_b64_tr_b16 v[82:83], v12 offset:16640\n"
    "v_mfma_f32_16x16x32_bf16 v[20:23], v[84:87], a[56:59], v[20:23]\n"
    "s_add_u32 m0, s63, 1024\n"
    "s_mov_b32 s67, 65664\n"
    "buffer_load_dwordx4 v6, s[32:35], s67 offen lds\n"
    "v_mfma_f32_16x16x32_bf16 v[36:39], v[84:87], a[120:123], v[36:39]\n"
    "ds_read_b64_tr_b16 v[84:85], v13 offset:16384\n"
    "ds_read_b64_tr_b16 v[86:87], v13 offset:16640\n"
    "s_waitcnt lgkmcnt(12)\n"
    "v_mfma_f32_16x16x32_bf16 v[24:27], v[88:91], a[56:59], v[24:27]\n"
    "v_mfma_f32_16x16x32_bf16 v[40:43], v[88:91], a[120:123], v[40:43]\n"
    "ds_read_b64_tr_b16 v[88:89], v12 offset:16896\n"
    "ds_read_b64_tr_b16 v[90:91], v12 offset:17152\n"
    "v_mfma_f32_16x16x32_bf16 v[28:31], v[92:95], a[56:59], v[28:31]\n"
    "v_mfma_f32_16x16x32_bf16 v[44:47], v[92:95], a[120:123], v[44:47]\n"
    "ds_read_b64_tr_b16 v[92:93], v13 offset:16896\n"
    "ds_read_b64_tr_b16 v[94:95], v13 offset:17152\n"
    "s_waitcnt lgkmcnt(12)\n"
    "v_mfma_f32_16x16x32_bf16 v[16:19], v[96:99], a[60:63], v[16:19]\n"
    "v_mfma_f32_16x16x32_bf16 v[32:35], v[96:99], a[124:127], v[32:35]\n"
    "ds_read_b64_tr_b16 v[96:97], v12 offset:20480\n"
    "ds_read_b64_tr_b16 v[98:99], v12 offset:20736\n"
    "v_mfma_f32_16x16x32_bf16 v[20:23], v[100:103], a[60:63], v[20:23]\n"
    "v_mfma_f32_16x16x32_bf16 v[36:39], v[100:103], a[124:127], v[36:39]\n"
    "ds_read_b64_tr_b16 v[100:101], v13 offset:20480\n"
    "ds_read_b64_tr_b16 v[102:103], v13 offset:20736\n"
    "s_waitcnt lgkmcnt(12)\n"
    "v_mfma_f32_16x16x32_bf16 v[24:27], v[104:107], a[60:63], v[24:27]\n"
    "v_mfma_f32_16x16x32_bf16 v[40:43], v[104:107], a[124:127], v[40:43]\n"
    "ds_read_b64_tr_b16 v[104:105], v12 offset:20992\n"
    "ds_read_b64_tr_b16 v[106:107], v12 offset:21248\n"
    "v_mfma_f32_16x16x32_bf16 v[28:31], v[108:111], a[60:63], v[28:31]\n"
    "v_mfma_f32_16x16x32_bf16 v[44:47], v[108:111], a[124:127], v[44:47]\n"
    "ds_read_b64_tr_b16 v[108:109], v13 offset:20992\n"
    "ds_read_b64_tr_b16 v[110:111], v13 offset:21248\n"
    "v_cvt_pk_bf16_f32 v48, v16, v17\n"
    "v_cvt_pk_bf16_f32 v49, v18, v19\n"
    "v_cvt_pk_bf16_f32 v50, v20, v21\n"
    "v_cvt_pk_bf16_f32 v51, v22, v23\n"
    "v_cvt_pk_bf16_f32 v52, v24, v25\n"
    "v_cvt_pk_bf16_f32 v53, v26, v27\n"
    "v_cvt_pk_bf16_f32 v54, v28, v29\n"
    "v_cvt_pk_bf16_f32 v55, v30, v31\n"
    "v_cvt_pk_bf16_f32 v56, v32, v33\n"
    "v_cvt_pk_bf16_f32 v57, v34, v35\n"
    "v_cvt_pk_bf16_f32 v58, v36, v37\n"
    "v_cvt_pk_bf16_f32 v59, v38, v39\n"
    "v_cvt_pk_bf16_f32 v60, v40, v41\n"
    "v_cvt_pk_bf16_f32 v61, v42, v43\n"
    "v_cvt_pk_bf16_f32 v62, v44, v45\n"
    "v_cvt_pk_bf16_f32 v63, v46, v47\n"
    "s_cmp_eq_u32 s60, 0\n"
    "s_cbranch_scc0 L_hsD_%=\n"
    "s_waitcnt vmcnt(5)\n"
    "global_store_dwordx4 v121, v[116:119], s[96:97] sc1\n"
    "L_hsD_%=:\n"
    "s_waitcnt lgkmcnt(12)\n"
    "v_mfma_f32_16x16x32_bf16 v[16:19], a[0:3], v[80:83], 0\n"
    "s_mov_b64 s[24:25], s[94:95]\n"
    "s_and_b32 s25, s25, 0xffff\n"
    "s_cmp_lt_u32 s60, 2\n"
    "s_cselect_b32 s26, 0x400000, 0\n"
    "v_mfma_f32_16x16x32_bf16 v[20:23], a[64:67], v[80:83], 0\n"
    "ds_read_b64_tr_b16 v[80:81], v12 offset:24576\n"
    "ds_read_b64_tr_b16 v[82:83], v12 offset:24832\n"
    "v_mfma_f32_16x16x32_bf16 v[24:27], a[0:3], v[84:87], 0\n"
    "s_lshr_b32 s66, %1, 3\n"
    "s_lshl_b32 s66, s66, 1\n"
    "s_add_u32 s66, s66, s60\n"
    "v_mfma_f32_16x16x32_bf16 v[28:31], a[64:67], v[84:87], 0\n"
    "ds_read_b64_tr_b16 v[84:85], v13 offset:24576\n"
    "ds_read_b64_tr_b16 v[86:87], v13 offset:24832\n"
    "s_waitcnt lgkmcnt(12)\n"
    "v_mfma_f32_16x16x32_bf16 v[32:35], a[0:3], v[88:91], 0\n"
    "s_lshl_b32 s66, s66, 16\n"
    "s_and_b32 s68, %1, 7\n"
    "s_lshl_b32 s68, s68, 10\n"
    "v_mfma_f32_16x16x32_bf16 v[36:39], a[64:67], v[88:91], 0\n"
    "ds_read_b64_tr_b16 v[88:89], v12 offset:25088\n"
    "ds_read_b64_tr_b16 v[90:91], v12 offset:25344\n"
    "v_mfma_f32_16x16x32_bf16 v[40:43], a[0:3], v[92:95], 0\n"
    "s_mov_b32 s71, 0\n"
    "buffer_store_dwordx4 v[48:51], v2, s[36:39], s71 offen offset:2048 sc1\n"
    "v_mfma_f32_16x16x32_bf16 v[44:47], a[64:67], v[92:95], 0\n"
    "ds_read_b64_tr_b16 v[92:93], v13 offset:25088\n"
    "ds_read_b64_tr_b16 v[94:95], v13 offset:25344\n"
    "s_waitcnt lgkmcnt(12)\n"
    "v_mfma_f32_16x16x32_bf16 v[16:19], a[4:7], v[96:99], v[16:19]\n"
    "s_add_u32 s66, s66, s68\n"
    "v_lshrrev_b32 v14, 3, v3\n"
    "v_lshlrev_b32 v7, 13, v14\n"
    "v_mfma_f32_16x16x32_bf16 v[20:23], a[68:71], v[96:99], v[20:23]\n"
    "ds_read_b64_tr_b16 v[96:97], v12 offset:28672\n"
    "ds_read_b64_tr_b16 v[98:99], v12 offset:28928\n"
    "v_mfma_f32_16x16x32_bf16 v[24:27], a[4:7], v[100:103], v[24:27]\n"
    "v_and_b32 v14, 7, v3\n"
    "v_lshl_add_u32 v7, v14, 7, v7\n"
    "buffer_load_dword v1, v7, s[24:27], s66 offen\n"
    "v_mfma_f32_16x16x32_bf16 v[28:31], a[68:71], v[100:103], v[28:31]\n"
    "ds_read_b64_tr_b16 v[100:101], v13 offset:28672\n"
    "ds_read_b64_tr_b16 v[102:103], v13 offset:28928\n"
    "s_waitcnt lgkmcnt(12)\n"
    "v_mfma_f32_16x16x32_bf16 v[32:35], a[4:7], v[104:107], v[32:35]\n"
    "v_mfma_f32_16x16x32_bf16 v[36:39], a[68:71], v[104:107], v[36:39]\n"
    "ds_read_b64_tr_b16 v[104:105], v12 offset:29184\n"
    "ds_read_b64_tr_b16 v[106:107], v12 offset:29440\n"
    "v_mfma_f32_16x16x32_bf16 v[40:43], a[4:7], v[108:111], v[40:43]\n"
    "v_mfma_f32_16x16x32_bf16 v[44:47], a[68:71], v[108:111], v[44:47]\n"
    "ds_read_b64_tr_b16 v[108:109], v13 offset:29184\n"
    "ds_read_b64_tr_b16 v[110:111], v13 offset:29440\n"
    "s_barrier\n"
    "s_waitcnt lgkmcnt(12)\n"
    "v_mfma_f32_16x16x32_bf16 v[16:19], a[8:11], v[80:83], v[16:19]\n"
    "s_add_u32 m0, s63, 65536\n"
    "s_mov_b32 s67, 1048704\n"
    "buffer_load_dwordx4 v5, s[32:35], s67 offen lds\n"
    "v_mfma_f32_16x16x32_bf16 v[20:23], a[72:75], v[80:83], v[20:23]\n"
    "ds_read_b64_tr_b16 v[80:81], v12 offset:32768\n"
    "ds_read_b64_tr_b16 v[82:83], v12 offset:33024\n"
    "v_mfma_f32_16x16x32_bf16 v[24:27], a[8:11], v[84:87], v[24:27]\n"
    "s_add_u32 m0, s63, 66560\n"
    "s_mov_b32 s67, 1114240\n"
    "buffer_load_dwordx4 v6, s[32:35], s67 offen lds\n"
    "v_mfma_f32_16x16x32_bf16 v[28:31], a[72:75], v[84:87], v[28:31]\n"
    "ds_read_b64_tr_b16 v[84:85], v13 offset:32768\n"
    "ds_read_b64_tr_b16 v[86:87], v13 offset:33024\n"
    "s_waitcnt lgkmcnt(12)\n"
    "v_mfma_f32_16x16x32_bf16 v[32:35], a[8:11], v[88:91], v[32:35]\n"
    "v_mfma_f32_16x16x32_bf16 v[36:39], a[72:75], v[88:91], v[36:39]\n"
    "ds_read_b64_tr_b16 v[88:89], v12 offset:33280\n"
    "ds_read_b64_tr_b16 v[90:91], v12 offset:33536\n"
    "v_mfma_f32_16x16x32_bf16 v[40:43], a[8:11], v[92:95], v[40:43]\n"
    "s_mov_b32 s71, 0\n"
    "buffer_store_dwordx4 v[52:55], v2, s[36:39], s71 offen offset:3072 sc1\n"
    "v_mfma_f32_16x16x32_bf16 v[44:47], a[72:75], v[92:95], v[44:47]\n"
    "ds_read_b64_tr_b16 v[92:93], v13 offset:33280\n"
    "ds_read_b64_tr_b16 v[94:95], v13 offset:33536\n"
    "s_waitcnt lgkmcnt(12)\n"
    "v_mfma_f32_16x16x32_bf16 v[16:19], a[12:15], v[96:99], v[16:19]\n"
    "v_mfma_f32_16x16x32_bf16 v[20:23], a[76:79], v[96:99], v[20:23]\n"
    "ds_read_b64_tr_b16 v[96:97], v12 offset:36864\n"
    "ds_read_b64_tr_b16 v[98:99], v12 offset:37120\n"
    "v_mfma_f32_16x16x32_bf16 v[24:27], a[12:15], v[100:103], v[24:27]\n"
    "v_mfma_f32_16x16x32_bf16 v[28:31], a[76:79], v[100:103], v[28:31]\n"
    "ds_read_b64_tr_b16 v[100:101], v13 offset:36864\n"
    "ds_read_b64_tr_b16 v[102:103], v13 offset:37120\n"
    "s_waitcnt lgkmcnt(12)\n"
    "v_mfma_f32_16x16x32_bf16 v[32:35], a[12:15], v[104:107], v[32:35]\n"
    "v_mfma_f32_16x16x32_bf16 v[36:39], a[76:79], v[104:107], v[36:39]\n"
    "ds_read_b64_tr_b16 v[104:105], v12 offset:37376\n"
    "ds_read_b64_tr_b16 v[106:107], v12 offset:37632\n"
    "v_mfma_f32_16x16x32_bf16 v[40:43], a[12:15], v[108:111], v[40:43]\n"
    "v_mfma_f32_16x16x32_bf16 v[44:47], a[76:79], v[108:111], v[44:47]\n"
    "ds_read_b64_tr_b16 v[108:109], v13 offset:37376\n"
    "ds_read_b64_tr_b16 v[110:111], v13 offset:37632\n"
    "s_waitcnt lgkmcnt(12)\n"
    "v_mfma_f32_16x16x32_bf16 v[16:19], a[16:19], v[80:83], v[16:19]\n"
    "s_lshl_b32 s66, s61, 8\n"
    "s_sub_u32 s68, 0x400000, s66\n"
    "s_mov_b64 s[28:29], s[94:95]\n"
    "v_mfma_f32_16x16x32_bf16 v[20:23], a[80:83], v[80:83], v[20:23]\n"
    "ds_read_b64_tr_b16 v[80:81], v12 offset:40960\n"
    "ds_read_b64_tr_b16 v[82:83], v12 offset:41216\n"
    "v_mfma_f32_16x16x32_bf16 v[24:27], a[16:19], v[84:87], v[24:27]\n"
    "s_add_u32 s28, s28, s66\n"
    "s_addc_u32 s29, s29, 0\n"
    "s_and_b32 s29, s29, 0xffff\n"
    "v_mfma_f32_16x16x32_bf16 v[28:31], a[80:83], v[84:87], v[28:31]\n"
    "ds_read_b64_tr_b16 v[84:85], v13 offset:40960\n"
    "ds_read_b64_tr_b16 v[86:87], v13 offset:41216\n"
    "s_waitcnt lgkmcnt(12)\n"
    "v_mfma_f32_16x16x32_bf16 v[32:35], a[16:19], v[88:91], v[32:35]\n"
    "s_mov_b32 s30, s68\n"
    "s_mov_b32 s31, 0x20000\n"
    "v_bfe_u32 v15, v3, 2, 3\n"
    "v_mfma_f32_16x16x32_bf16 v[36:39], a[80:83], v[88:91], v[36:39]\n"
    "ds_read_b64_tr_b16 v[88:89], v12 offset:41472\n"
    "ds_read_b64_tr_b16 v[90:91], v12 offset:41728\n"
    "v_mfma_f32_16x16x32_bf16 v[40:43], a[16:19], v[92:95], v[40:43]\n"
    "s_mov_b32 s71, 4096\n"
    "buffer_store_dwordx4 v[56:59], v2, s[36:39], s71 offen offset:2048 sc1\n"
    "v_mfma_f32_16x16x32_bf16 v[44:47], a[80:83], v[92:95], v[44:47]\n"
    "ds_read_b64_tr_b16 v[92:93], v13 offset:41472\n"
    "ds_read_b64_tr_b16 v[94:95], v13 offset:41728\n"
    "s_waitcnt lgkmcnt(12)\n"
    "v_mfma_f32_16x16x32_bf16 v[16:19], a[20:23], v[96:99], v[16:19]\n"
    "v_lshl_add_u32 v15, s60, 3, v15\n"
    "v_lshlrev_b32 v7, 13, v15\n"
    "v_lshrrev_b32 v15, 5, v3\n"
    "v_mfma_f32_16x16x32_bf16 v[20:23], a[84:87], v[96:99], v[20:23]\n"
    "ds_read_b64_tr_b16 v[96:97], v12 offset:45056\n"
    "ds_read_b64_tr_b16 v[98:99], v12 offset:45312\n"
    "v_mfma_f32_16x16x32_bf16 v[24:27], a[20:23], v[100:103], v[24:27]\n"
    "v_lshl_add_u32 v7, v15, 6, v7\n"
    "v_and_b32 v14, 3, v3\n"
    "v_bfe_u32 v15, v3, 4, 1\n"
    "v_mfma_f32_16x16x32_bf16 v[28:31], a[84:87], v[100:103], v[28:31]\n"
    "ds_read_b64_tr_b16 v[100:101], v13 offset:45056\n"
    "ds_read_b64_tr_b16 v[102:103], v13 offset:45312\n"
    "s_waitcnt lgkmcnt(12)\n"
    "v_mfma_f32_16x16x32_bf16 v[32:35], a[20:23], v[104:107], v[32:35]\n"
    "v_lshlrev_b32 v15, 1, v15\n"
    "v_xor_b32 v14, v14, v15\n"
    "v_lshl_add_u32 v7, v14, 4, v7\n"
    "v_mfma_f32_16x16x32_bf16 v[36:39], a[84:87], v[104:107], v[36:39]\n"
    "ds_read_b64_tr_b16 v[104:105], v12 offset:45568\n"
    "ds_read_b64_tr_b16 v[106:107], v12 offset:45824\n"
    "v_mfma_f32_16x16x32_bf16 v[40:43], a[20:23], v[108:111], v[40:43]\n"
    "s_and_b32 s70, s60, 3\n"
    "s_sub_u32 s66, 15, s62\n"
    "s_lshl_b32 s66, s66, 2\n"
    "v_mfma_f32_16x16x32_bf16 v[44:47], a[84:87], v[108:111], v[44:47]\n"
    "ds_read_b64_tr_b16 v[108:109], v13 offset:45568\n"
    "ds_read_b64_tr_b16 v[110:111], v13 offset:45824\n"
    "s_barrier\n"
    "s_waitcnt lgkmcnt(12)\n"
    "v_mfma_f32_16x16x32_bf16 v[16:19], a[24:27], v[80:83], v[16:19]\n"
    "s_add_u32 m0, s63, 81920\n"
    "s_mov_b32 s67, 2097280\n"
    "buffer_load_dwordx4 v5, s[32:35], s67 offen lds\n"
    "v_mfma_f32_16x16x32_bf16 v[20:23], a[88:91], v[80:83], v[20:23]\n"
    "ds_read_b64_tr_b16 v[80:81], v12 offset:49152\n"
    "ds_read_b64_tr_b16 v[82:83], v12 offset:49408\n"
    "v_mfma_f32_16x16x32_bf16 v[24:27], a[24:27], v[84:87], v[24:27]\n"
    "s_add_u32 m0, s63, 82944\n"
    "s_mov_b32 s67, 2162816\n"
    "buffer_load_dwordx4 v6, s[32:35], s67 offen lds\n"
    "v_mfma_f32_16x16x32_bf16 v[28:31], a[88:91], v[84:87], v[28:31]\n"
    "ds_read_b64_tr_b16 v[84:85], v13 offset:49152\n"
    "ds_read_b64_tr_b16 v[86:87], v13 offset:49408\n"
    "s_waitcnt lgkmcnt(12)\n"
    "v_mfma_f32_16x16x32_bf16 v[32:35], a[24:27], v[88:91], v[32:35]\n"
    "s_add_u32 s66, s66, s70\n"
    "s_lshl_b32 s68, s62, 2\n"
    "s_add_u32 s68, s68, 3\n"
    "v_mfma_f32_16x16x32_bf16 v[36:39], a[88:91], v[88:91], v[36:39]\n"
    "ds_read_b64_tr_b16 v[88:89], v12 offset:49664\n"
    "ds_read_b64_tr_b16 v[90:91], v12 offset:49920\n"
    "v_mfma_f32_16x16x32_bf16 v[40:43], a[24:27], v[92:95], v[40:43]\n"
    "s_mov_b32 s71, 4096\n"
    "buffer_store_dwordx4 v[60:63], v2, s[36:39], s71 offen offset:3072 sc1\n"
    "v_mfma_f32_16x16x32_bf16 v[44:47], a[88:91], v[92:95], v[44:47]\n"
    "ds_read_b64_tr_b16 v[92:93], v13 offset:49664\n"
    "ds_read_b64_tr_b16 v[94:95], v13 offset:49920\n"
    "s_waitcnt lgkmcnt(12)\n"
    "v_mfma_f32_16x16x32_bf16 v[16:19], a[28:31], v[96:99], v[16:19]\n"
    "s_sub_u32 s68, s68, s70\n"
    "s_cmp_ge_u32 s60, 4\n"
    "s_cselect_b32 s66, s68, s66\n"
    "v_mfma_f32_16x16x32_bf16 v[20:23], a[92:95], v[96:99], v[20:23]\n"
    "ds_read_b64_tr_b16 v[96:97], v12 offset:53248\n"
    "ds_read_b64_tr_b16 v[98:99], v12 offset:53504\n"
    "v_mfma_f32_16x16x32_bf16 v[24:27], a[28:31], v[100:103], v[24:27]\n"
    "s_lshl_b32 s66, s66, 15\n"
    "s_mov_b64 s[40:41], s[76:77]\n"
    "s_add_u32 s40, s40, s66\n"
    "s_addc_u32 s41, s41, 0\n"
    "v_mfma_f32_16x16x32_bf16 v[28:31], a[92:95], v[100:103], v[28:31]\n"
    "ds_read_b64_tr_b16 v[100:101], v13 offset:53248\n"
    "ds_read_b64_tr_b16 v[102:103], v13 offset:53504\n"
    "s_waitcnt lgkmcnt(12)\n"
    "v_mfma_f32_16x16x32_bf16 v[32:35], a[28:31], v[104:107], v[32:35]\n"
    "s_and_b32 s41, s41, 0xffff\n"
    "s_mov_b32 s42, 0x8000\n"
    "s_mov_b32 s43, 0x20000\n"
    "v_mfma_f32_16x16x32_bf16 v[36:39], a[92:95], v[104:107], v[36:39]\n"
    "ds_read_b64_tr_b16 v[104:105], v12 offset:53760\n"
    "ds_read_b64_tr_b16 v[106:107], v12 offset:54016\n"
    "v_mfma_f32_16x16x32_bf16 v[40:43], a[28:31], v[108:111], v[40:43]\n"
    "s_mov_b32 s23, 16384\n"
    "v_lshlrev_b32 v4, 2, %2\n"
    "v_lshrrev_b32 v15, 5, %2\n"
    "v_mfma_f32_16x16x32_bf16 v[44:47], a[92:95], v[108:111], v[44:47]\n"
    "ds_read_b64_tr_b16 v[108:109], v13 offset:53760\n"
    "ds_read_b64_tr_b16 v[110:111], v13 offset:54016\n"
    "s_waitcnt lgkmcnt(12)\n"
    "v_mfma_f32_16x16x32_bf16 v[16:19], a[32:35], v[80:83], v[16:19]\n"
    "v_lshlrev_b32 v15, 8, v15\n"
    "v_bfe_u32 v14, %2, 2, 2\n"
    "v_lshl_add_u32 v15, v14, 6, v15\n"
    "v_mfma_f32_16x16x32_bf16 v[20:23], a[96:99], v[80:83], v[20:23]\n"
    "ds_read_b64_tr_b16 v[80:81], v12 offset:57344\n"
    "ds_read_b64_tr_b16 v[82:83], v12 offset:57600\n"
    "v_mfma_f32_16x16x32_bf16 v[24:27], a[32:35], v[84:87], v[24:27]\n"
    "v_and_b32 v14, 3, %2\n"
    "v_lshl_add_u32 v15, v14, 2, v15\n"
    "v_bfe_u32 v14, %2, 4, 1\n"
    "v_mfma_f32_16x16x32_bf16 v[28:31], a[96:99], v[84:87], v[28:31]\n"
    "ds_read_b64_tr_b16 v[84:85], v13 offset:57344\n"
    "ds_read_b64_tr_b16 v[86:87], v13 offset:57600\n"
    "s_waitcnt lgkmcnt(12)\n"
    "v_mfma_f32_16x16x32_bf16 v[32:35], a[32:35], v[88:91], v[32:35]\n"
    "v_lshl_add_u32 v15, v14, 4, v15\n"
    "v_add_u32 v15, 147456, v15\n"
    "v_mfma_f32_16x16x32_bf16 v[36:39], a[96:99], v[88:91], v[36:39]\n"
    "ds_read_b64_tr_b16 v[88:89], v12 offset:57856\n"
    "ds_read_b64_tr_b16 v[90:91], v12 offset:58112\n"
    "v_mfma_f32_16x16x32_bf16 v[40:43], a[32:35], v[92:95], v[40:43]\n"
    "v_mfma_f32_16x16x32_bf16 v[44:47], a[96:99], v[92:95], v[44:47]\n"
    "ds_read_b64_tr_b16 v[92:93], v13 offset:57856\n"
    "ds_read_b64_tr_b16 v[94:95], v13 offset:58112\n"
    "s_waitcnt lgkmcnt(12)\n"
    "v_mfma_f32_16x16x32_bf16 v[16:19], a[36:39], v[96:99], v[16:19]\n"
    "v_mfma_f32_16x16x32_bf16 v[20:23], a[100:103], v[96:99], v[20:23]\n"
    "ds_read_b64_tr_b16 v[96:97], v12 offset:61440\n"
    "ds_read_b64_tr_b16 v[98:99], v12 offset:61696\n"
    "v_mfma_f32_16x16x32_bf16 v[24:27], a[36:39], v[100:103], v[24:27]\n"
    "v_mfma_f32_16x16x32_bf16 v[28:31], a[100:103], v[100:103], v[28:31]\n"
    "ds_read_b64_tr_b16 v[100:101], v13 offset:61440\n"
    "ds_read_b64_tr_b16 v[102:103], v13 offset:61696\n"
    "s_waitcnt lgkmcnt(12)\n"
    "v_mfma_f32_16x16x32_bf16 v[32:35], a[36:39], v[104:107], v[32:35]\n"
    "v_mfma_f32_16x16x32_bf16 v[36:39], a[100:103], v[104:107], v[36:39]\n"
    "ds_read_b64_tr_b16 v[104:105], v12 offset:61952\n"
    "ds_read_b64_tr_b16 v[106:107], v12 offset:62208\n"
    "v_mfma_f32_16x16x32_bf16 v[40:43], a[36:39], v[108:111], v[40:43]\n"
    "v_mfma_f32_16x16x32_bf16 v[44:47], a[100:103], v[108:111], v[44:47]\n"
    "ds_read_b64_tr_b16 v[108:109], v13 offset:61952\n"
    "ds_read_b64_tr_b16 v[110:111], v13 offset:62208\n"
    "s_waitcnt vmcnt(9)\n"
    "s_barrier\n"
    "s_waitcnt lgkmcnt(12)\n"
    "v_mfma_f32_16x16x32_bf16 v[16:19], a[40:43], v[80:83], v[16:19]\n"
    "s_add_u32 m0, s63, 98304\n"
    "s_mov_b32 s67, 3145856\n"
    "buffer_load_dwordx4 v5, s[32:35], s67 offen lds\n"
    "v_mfma_f32_16x16x32_bf16 v[20:23], a[104:107], v[80:83], v[20:23]\n"
    "ds_read_b64_tr_b16 v[80:81], v10 offset:49152\n"
    "ds_read_b64_tr_b16 v[82:83], v10 offset:49408\n"
    "v_mfma_f32_16x16x32_bf16 v[24:27], a[40:43], v[84:87], v[24:27]\n"
    "s_add_u32 m0, s63, 99328\n"
    "s_mov_b32 s67, 3211392\n"
    "buffer_load_dwordx4 v6, s[32:35], s67 offen lds\n"
    "v_mfma_f32_16x16x32_bf16 v[28:31], a[104:107], v[84:87], v[28:31]\n"
    "ds_read_b64_tr_b16 v[84:85], v11 offset:49152\n"
    "ds_read_b64_tr_b16 v[86:87], v11 offset:49408\n"
    "s_waitcnt lgkmcnt(12)\n"
    "v_mfma_f32_16x16x32_bf16 v[32:35], a[40:43], v[88:91], v[32:35]\n"
    "s_add_u32 m0, s63, 16384\n"
    "s_mov_b32 s67, 0\n"
    "buffer_load_dwordx4 v7, s[28:31], s67 offen lds\n"
    "v_mfma_f32_16x16x32_bf16 v[36:39], a[104:107], v[88:91], v[36:39]\n"
    "ds_read_b64_tr_b16 v[88:89], v10 offset:49664\n"
    "ds_read_b64_tr_b16 v[90:91], v10 offset:49920\n"
    "v_mfma_f32_16x16x32_bf16 v[40:43], a[40:43], v[92:95], v[40:43]\n"
    "s_add_u32 m0, s63, 17408\n"
    "s_mov_b32 s67, 128\n"
    "buffer_load_dwordx4 v7, s[28:31], s67 offen lds\n"
    "v_mfma_f32_16x16x32_bf16 v[44:47], a[104:107], v[92:95], v[44:47]\n"
    "ds_read_b64_tr_b16 v[92:93], v11 offset:49664\n"
    "ds_read_b64_tr_b16 v[94:95], v11 offset:49920\n"
    "s_waitcnt lgkmcnt(12)\n"
    "v_mfma_f32_16x16x32_bf16 v[16:19], a[44:47], v[96:99], v[16:19]\n"
    "s_add_u32 m0, s63, 32768\n"
    "s_mov_b32 s67, 524288\n"
    "buffer_load_dwordx4 v7, s[28:31], s67 offen lds\n"
    "v_mfma_f32_16x16x32_bf16 v[20:23], a[108:111], v[96:99], v[20:23]\n"
    "ds_read_b64_tr_b16 v[96:97], v10 offset:53248\n"
    "ds_read_b64_tr_b16 v[98:99], v10 offset:53504\n"
    "v_mfma_f32_16x16x32_bf16 v[24:27], a[44:47], v[100:103], v[24:27]\n"
    "s_add_u32 m0, s63, 33792\n"
    "s_mov_b32 s67, 524416\n"
    "buffer_load_dwordx4 v7, s[28:31], s67 offen lds\n"
    "v_mfma_f32_16x16x32_bf16 v[28:31], a[108:111], v[100:103], v[28:31]\n"
    "ds_read_b64_tr_b16 v[100:101], v11 offset:53248\n"
    "ds_read_b64_tr_b16 v[102:103], v11 offset:53504\n"
    "s_waitcnt lgkmcnt(12)\n"
    "v_mfma_f32_16x16x32_bf16 v[32:35], a[44:47], v[104:107], v[32:35]\n"
    "v_mfma_f32_16x16x32_bf16 v[36:39], a[108:111], v[104:107], v[36:39]\n"
    "ds_read_b64_tr_b16 v[104:105], v10 offset:53760\n"
    "ds_read_b64_tr_b16 v[106:107], v10 offset:54016\n"
    "v_mfma_f32_16x16x32_bf16 v[40:43], a[44:47], v[108:111], v[40:43]\n"
    "v_mfma_f32_16x16x32_bf16 v[44:47], a[108:111], v[108:111], v[44:47]\n"
    "ds_read_b64_tr_b16 v[108:109], v11 offset:53760\n"
    "ds_read_b64_tr_b16 v[110:111], v11 offset:54016\n"
    "s_waitcnt lgkmcnt(12)\n"
    "v_mfma_f32_16x16x32_bf16 v[16:19], a[48:51], v[80:83], v[16:19]\n"
    "v_mfma_f32_16x16x32_bf16 v[20:23], a[112:115], v[80:83], v[20:23]\n"
    "ds_read_b64_tr_b16 v[80:81], v10 offset:57344\n"
    "ds_read_b64_tr_b16 v[82:83], v10 offset:57600\n"
    "v_mfma_f32_16x16x32_bf16 v[24:27], a[48:51], v[84:87], v[24:27]\n"
    "v_mfma_f32_16x16x32_bf16 v[28:31], a[112:115], v[84:87], v[28:31]\n"
    "ds_read_b64_tr_b16 v[84:85], v11 offset:57344\n"
    "ds_read_b64_tr_b16 v[86:87], v11 offset:57600\n"
    "s_waitcnt lgkmcnt(12)\n"
    "v_mfma_f32_16x16x32_bf16 v[32:35], a[48:51], v[88:91], v[32:35]\n"
    "v_mfma_f32_16x16x32_bf16 v[36:39], a[112:115], v[88:91], v[36:39]\n"
    "ds_read_b64_tr_b16 v[88:89], v10 offset:57856\n"
    "ds_read_b64_tr_b16 v[90:91], v10 offset:58112\n"
    "v_mfma_f32_16x16x32_bf16 v[40:43], a[48:51], v[92:95], v[40:43]\n"
    "v_mfma_f32_16x16x32_bf16 v[44:47], a[112:115], v[92:95], v[44:47]\n"
    "ds_read_b64_tr_b16 v[92:93], v11 offset:57856\n"
    "ds_read_b64_tr_b16 v[94:95], v11 offset:58112\n"
    "s_waitcnt lgkmcnt(12)\n"
    "v_mfma_f32_16x16x32_bf16 v[16:19], a[52:55], v[96:99], v[16:19]\n"
    "v_mfma_f32_16x16x32_bf16 v[20:23], a[116:119], v[96:99], v[20:23]\n"
    "ds_read_b64_tr_b16 v[96:97], v10 offset:61440\n"
    "ds_read_b64_tr_b16 v[98:99], v10 offset:61696\n"
    "v_mfma_f32_16x16x32_bf16 v[24:27], a[52:55], v[100:103], v[24:27]\n"
    "v_mfma_f32_16x16x32_bf16 v[28:31], a[116:119], v[100:103], v[28:31]\n"
    "ds_read_b64_tr_b16 v[100:101], v11 offset:61440\n"
    "ds_read_b64_tr_b16 v[102:103], v11 offset:61696\n"
    "s_waitcnt lgkmcnt(12)\n"
    "v_mfma_f32_16x16x32_bf16 v[32:35], a[52:55], v[104:107], v[32:35]\n"
    "v_mfma_f32_16x16x32_bf16 v[36:39], a[116:119], v[104:107], v[36:39]\n"
    "ds_read_b64_tr_b16 v[104:105], v10 offset:61952\n"
    "ds_read_b64_tr_b16 v[106:107], v10 offset:62208\n"
    "v_mfma_f32_16x16x32_bf16 v[40:43], a[52:55], v[108:111], v[40:43]\n"
    "v_mfma_f32_16x16x32_bf16 v[44:47], a[116:119], v[108:111], v[44:47]\n"
    "ds_read_b64_tr_b16 v[108:109], v11 offset:61952\n"
    "ds_read_b64_tr_b16 v[110:111], v11 offset:62208\n"
    "s_waitcnt lgkmcnt(12)\n"
    "v_mfma_f32_16x16x32_bf16 v[16:19], a[56:59], v[80:83], v[16:19]\n"
    "v_mfma_f32_16x16x32_bf16 v[20:23], a[120:123], v[80:83], v[20:23]\n"
    "ds_read_b64_tr_b16 v[80:81], v10 offset:0\n"
    "ds_read_b64_tr_b16 v[82:83], v10 offset:256\n"
    "v_mfma_f32_16x16x32_bf16 v[24:27], a[56:59], v[84:87], v[24:27]\n"
    "v_mfma_f32_16x16x32_bf16 v[28:31], a[120:123], v[84:87], v[28:31]\n"
    "ds_read_b64_tr_b16 v[84:85], v11 offset:0\n"
    "ds_read_b64_tr_b16 v[86:87], v11 offset:256\n"
    "s_waitcnt lgkmcnt(12)\n"
    "v_mfma_f32_16x16x32_bf16 v[32:35], a[56:59], v[88:91], v[32:35]\n"
    "v_mfma_f32_16x16x32_bf16 v[36:39], a[120:123], v[88:91], v[36:39]\n"
    "ds_read_b64_tr_b16 v[88:89], v10 offset:512\n"
    "ds_read_b64_tr_b16 v[90:91], v10 offset:768\n"
    "v_mfma_f32_16x16x32_bf16 v[40:43], a[56:59], v[92:95], v[40:43]\n"
    "v_mfma_f32_16x16x32_bf16 v[44:47], a[120:123], v[92:95], v[44:47]\n"
    "ds_read_b64_tr_b16 v[92:93], v11 offset:512\n"
    "ds_read_b64_tr_b16 v[94:95], v11 offset:768\n"
    "s_waitcnt lgkmcnt(12)\n"
    "v_mfma_f32_16x16x32_bf16 v[16:19], a[60:63], v[96:99], v[16:19]\n"
    "v_mfma_f32_16x16x32_bf16 v[20:23], a[124:127], v[96:99], v[20:23]\n"
    "ds_read_b64_tr_b16 v[96:97], v10 offset:4096\n"
    "ds_read_b64_tr_b16 v[98:99], v10 offset:4352\n"
    "v_mfma_f32_16x16x32_bf16 v[24:27], a[60:63], v[100:103], v[24:27]\n"
    "v_mfma_f32_16x16x32_bf16 v[28:31], a[124:127], v[100:103], v[28:31]\n"
    "ds_read_b64_tr_b16 v[100:101], v11 offset:4096\n"
    "ds_read_b64_tr_b16 v[102:103], v11 offset:4352\n"
    "s_waitcnt lgkmcnt(12)\n"
    "v_mfma_f32_16x16x32_bf16 v[32:35], a[60:63], v[104:107], v[32:35]\n"
    "v_mfma_f32_16x16x32_bf16 v[36:39], a[124:127], v[104:107], v[36:39]\n"
    "ds_read_b64_tr_b16 v[104:105], v10 offset:4608\n"
    "ds_read_b64_tr_b16 v[106:107], v10 offset:4864\n"
    "v_mfma_f32_16x16x32_bf16 v[40:43], a[60:63], v[108:111], v[40:43]\n"
    "v_mfma_f32_16x16x32_bf16 v[44:47], a[124:127], v[108:111], v[44:47]\n"
    "ds_read_b64_tr_b16 v[108:109], v11 offset:4608\n"
    "ds_read_b64_tr_b16 v[110:111], v11 offset:4864\n"
    "v_cvt_pk_bf16_f32 v48, v16, v17\n"
    "v_cvt_pk_bf16_f32 v49, v18, v19\n"
    "v_cvt_pk_bf16_f32 v50, v20, v21\n"
    "v_cvt_pk_bf16_f32 v51, v22, v23\n"
    "v_cvt_pk_bf16_f32 v52, v24, v25\n"
    "v_cvt_pk_bf16_f32 v53, v26, v27\n"
    "v_cvt_pk_bf16_f32 v54, v28, v29\n"
    "v_cvt_pk_bf16_f32 v55, v30, v31\n"
    "v_cvt_pk_bf16_f32 v56, v32, v33\n"
    "v_cvt_pk_bf16_f32 v57, v34, v35\n"
    "v_cvt_pk_bf16_f32 v58, v36, v37\n"
    "v_cvt_pk_bf16_f32 v59, v38, v39\n"
    "v_cvt_pk_bf16_f32 v60, v40, v41\n"
    "v_cvt_pk_bf16_f32 v61, v42, v43\n"
    "v_cvt_pk_bf16_f32 v62, v44, v45\n"
    "v_cvt_pk_bf16_f32 v63, v46, v47\n"
    "s_cmp_eq_u32 s60, 0\n"
    "s_cbranch_scc0 L_hsE_%=\n"
    "s_add_u32 s66, s20, 393216\n"
    "v_lshlrev_b32 v122, 10, v3\n"
    "v_add_u32 v122, s66, v122\n"
    "global_load_dword v112, v122, s[96:97] sc1\n"
    "v_add_u32 v122, 0x10000, v122\n"
    "global_load_dword v113, v122, s[96:97] sc1\n"
    "v_add_u32 v122, 0x10000, v122\n"
    "global_load_dword v114, v122, s[96:97] sc1\n"
    "v_add_u32 v122, 0x10000, v122\n"
    "global_load_dword v115, v122, s[96:97] sc1\n"
    "L_hsE_%=:\n"
    "s_waitcnt lgkmcnt(12)\n"
    "v_mfma_f32_16x16x32_bf16 v[16:19], a[0:3], v[80:83], 0\n"
    "v_mfma_f32_16x16x32_bf16 v[20:23], a[64:67], v[80:83], 0\n"
    "ds_read_b64_tr_b16 v[80:81], v10 offset:8192\n"
    "ds_read_b64_tr_b16 v[82:83], v10 offset:8448\n"
    "v_mfma_f32_16x16x32_bf16 v[24:27], a[0:3], v[84:87], 0\n"
    "v_mfma_f32_16x16x32_bf16 v[28:31], a[64:67], v[84:87], 0\n"
    "ds_read_b64_tr_b16 v[84:85], v11 offset:8192\n"
    "ds_read_b64_tr_b16 v[86:87], v11 offset:8448\n"
    "s_waitcnt lgkmcnt(12)\n"
    "v_mfma_f32_16x16x32_bf16 v[32:35], a[0:3], v[88:91], 0\n"
    "v_mfma_f32_16x16x32_bf16 v[36:39], a[64:67], v[88:91], 0\n"
    "ds_read_b64_tr_b16 v[88:89], v10 offset:8704\n"
    "ds_read_b64_tr_b16 v[90:91], v10 offset:8960\n"
    "v_mfma_f32_16x16x32_bf16 v[40:43], a[0:3], v[92:95], 0\n"
    "s_mov_b32 s71, 8192\n"
    "buffer_store_dwordx4 v[48:51], v2, s[36:39], s71 offen offset:0 sc1\n"
    "v_mfma_f32_16x16x32_bf16 v[44:47], a[64:67], v[92:95], 0\n"
    "ds_read_b64_tr_b16 v[92:93], v11 offset:8704\n"
    "ds_read_b64_tr_b16 v[94:95], v11 offset:8960\n"
    "s_waitcnt lgkmcnt(12)\n"
    "v_mfma_f32_16x16x32_bf16 v[16:19], a[4:7], v[96:99], v[16:19]\n"
    "v_mfma_f32_16x16x32_bf16 v[20:23], a[68:71], v[96:99], v[20:23]\n"
    "ds_read_b64_tr_b16 v[96:97], v10 offset:12288\n"
    "ds_read_b64_tr_b16 v[98:99], v10 offset:12544\n"
    "v_mfma_f32_16x16x32_bf16 v[24:27], a[4:7], v[100:103], v[24:27]\n"
    "v_mfma_f32_16x16x32_bf16 v[28:31], a[68:71], v[100:103], v[28:31]\n"
    "ds_read_b64_tr_b16 v[100:101], v11 offset:12288\n"
    "ds_read_b64_tr_b16 v[102:103], v11 offset:12544\n"
    "s_waitcnt lgkmcnt(12)\n"
    "v_mfma_f32_16x16x32_bf16 v[32:35], a[4:7], v[104:107], v[32:35]\n"
    "v_mfma_f32_16x16x32_bf16 v[36:39], a[68:71], v[104:107], v[36:39]\n"
    "ds_read_b64_tr_b16 v[104:105], v10 offset:12800\n"
    "ds_read_b64_tr_b16 v[106:107], v10 offset:13056\n"
    "v_mfma_f32_16x16x32_bf16 v[40:43], a[4:7], v[108:111], v[40:43]\n"
    "v_mfma_f32_16x16x32_bf16 v[44:47], a[68:71], v[108:111], v[44:47]\n"
    "ds_read_b64_tr_b16 v[108:109], v11 offset:12800\n"
    "ds_read_b64_tr_b16 v[110:111], v11 offset:13056\n"
    "s_waitcnt vmcnt(5)\n"
    "s_barrier\n"
    "s_waitcnt lgkmcnt(12)\n"
    "v_mfma_f32_16x16x32_bf16 v[16:19], a[8:11], v[80:83], v[16:19]\n"
    "s_add_u32 m0, s63, 49152\n"
    "s_mov_b32 s67, 1048576\n"
    "buffer_load_dwordx4 v7, s[28:31], s67 offen lds\n"
    "v_mfma_f32_16x16x32_bf16 v[20:23], a[72:75], v[80:83], v[20:23]\n"
    "ds_read_b64_tr_b16 v[80:81], v12 offset:0\n"
    "ds_read_b64_tr_b16 v[82:83], v12 offset:256\n"
    "v_mfma_f32_16x16x32_bf16 v[24:27], a[8:11], v[84:87], v[24:27]\n"
    "s_add_u32 m0, s63, 50176\n"
    "s_mov_b32 s67, 1048704\n"
    "buffer_load_dwordx4 v7, s[28:31], s67 offen lds\n"
    "v_mfma_f32_16x16x32_bf16 v[28:31], a[72:75], v[84:87], v[28:31]\n"
    "ds_read_b64_tr_b16 v[84:85], v13 offset:0\n"
    "ds_read_b64_tr_b16 v[86:87], v13 offset:256\n"
    "s_waitcnt lgkmcnt(12)\n"
    "v_mfma_f32_16x16x32_bf16 v[32:35], a[8:11], v[88:91], v[32:35]\n"
    "v_mfma_f32_16x16x32_bf16 v[36:39], a[72:75], v[88:91], v[36:39]\n"
    "ds_read_b64_tr_b16 v[88:89], v12 offset:512\n"
    "ds_read_b64_tr_b16 v[90:91], v12 offset:768\n"
    "v_mfma_f32_16x16x32_bf16 v[40:43], a[8:11], v[92:95], v[40:43]\n"
    "s_mov_b32 s71, 8192\n"
    "buffer_store_dwordx4 v[52:55], v2, s[36:39], s71 offen offset:1024 sc1\n"
    "v_mfma_f32_16x16x32_bf16 v[44:47], a[72:75], v[92:95], v[44:47]\n"
    "ds_read_b64_tr_b16 v[92:93], v13 offset:512\n"
    "ds_read_b64_tr_b16 v[94:95], v13 offset:768\n"
    "s_waitcnt lgkmcnt(12)\n"
    "v_mfma_f32_16x16x32_bf16 v[16:19], a[12:15], v[96:99], v[16:19]\n"
    "v_mfma_f32_16x16x32_bf16 v[20:23], a[76:79], v[96:99], v[20:23]\n"
    "ds_read_b64_tr_b16 v[96:97], v12 offset:4096\n"
    "ds_read_b64_tr_b16 v[98:99], v12 offset:4352\n"
    "v_mfma_f32_16x16x32_bf16 v[24:27], a[12:15], v[100:103], v[24:27]\n"
    "v_mfma_f32_16x16x32_bf16 v[28:31], a[76:79], v[100:103], v[28:31]\n"
    "ds_read_b64_tr_b16 v[100:101], v13 offset:4096\n"
    "ds_read_b64_tr_b16 v[102:103], v13 offset:4352\n"
    "s_waitcnt lgkmcnt(12)\n"
    "v_mfma_f32_16x16x32_bf16 v[32:35], a[12:15], v[104:107], v[32:35]\n"
    "v_mfma_f32_16x16x32_bf16 v[36:39], a[76:79], v[104:107], v[36:39]\n"
    "ds_read_b64_tr_b16 v[104:105], v12 offset:4608\n"
    "ds_read_b64_tr_b16 v[106:107], v12 offset:4864\n"
    "v_mfma_f32_16x16x32_bf16 v[40:43], a[12:15], v[108:111], v[40:43]\n"
    "v_mfma_f32_16x16x32_bf16 v[44:47], a[76:79], v[108:111], v[44:47]\n"
    "ds_read_b64_tr_b16 v[108:109], v13 offset:4608\n"
    "ds_read_b64_tr_b16 v[110:111], v13 offset:4864\n"
    "s_waitcnt lgkmcnt(12)\n"
    "v_mfma_f32_16x16x32_bf16 v[16:19], a[16:19], v[80:83], v[16:19]\n"
    "v_mfma_f32_16x16x32_bf16 v[20:23], a[80:83], v[80:83], v[20:23]\n"
    "ds_read_b64_tr_b16 v[80:81], v12 offset:8192\n"
    "ds_read_b64_tr_b16 v[82:83], v12 offset:8448\n"
    "v_mfma_f32_16x16x32_bf16 v[24:27], a[16:19], v[84:87], v[24:27]\n"
    "v_mfma_f32_16x16x32_bf16 v[28:31], a[80:83], v[84:87], v[28:31]\n"
    "ds_read_b64_tr_b16 v[84:85], v13 offset:8192\n"
    "ds_read_b64_tr_b16 v[86:87], v13 offset:8448\n"
    "s_waitcnt lgkmcnt(12)\n"
    "v_mfma_f32_16x16x32_bf16 v[32:35], a[16:19], v[88:91], v[32:35]\n"
    "v_mfma_f32_16x16x32_bf16 v[36:39], a[80:83], v[88:91], v[36:39]\n"
    "ds_read_b64_tr_b16 v[88:89], v12 offset:8704\n"
    "ds_read_b64_tr_b16 v[90:91], v12 offset:8960\n"
    "v_mfma_f32_16x16x32_bf16 v[40:43], a[16:19], v[92:95], v[40:43]\n"
    "s_mov_b32 s71, 8192\n"
    "buffer_store_dwordx4 v[56:59], v2, s[36:39], s71 offen offset:2048 sc1\n"
    "v_mfma_f32_16x16x32_bf16 v[44:47], a[80:83], v[92:95], v[44:47]\n"
    "ds_read_b64_tr_b16 v[92:93], v13 offset:8704\n"
    "ds_read_b64_tr_b16 v[94:95], v13 offset:8960\n"
    "s_waitcnt lgkmcnt(12)\n"
    "v_mfma_f32_16x16x32_bf16 v[16:19], a[20:23], v[96:99], v[16:19]\n"
    "v_mfma_f32_16x16x32_bf16 v[20:23], a[84:87], v[96:99], v[20:23]\n"
    "ds_read_b64_tr_b16 v[96:97], v12 offset:12288\n"
    "ds_read_b64_tr_b16 v[98:99], v12 offset:12544\n"
    "v_mfma_f32_16x16x32_bf16 v[24:27], a[20:23], v[100:103], v[24:27]\n"
    "v_mfma_f32_16x16x32_bf16 v[28:31], a[84:87], v[100:103], v[28:31]\n"
    "ds_read_b64_tr_b16 v[100:101], v13 offset:12288\n"
    "ds_read_b64_tr_b16 v[102:103], v13 offset:12544\n"
    "s_waitcnt lgkmcnt(12)\n"
    "v_mfma_f32_16x16x32_bf16 v[32:35], a[20:23], v[104:107], v[32:35]\n"
    "v_mfma_f32_16x16x32_bf16 v[36:39], a[84:87], v[104:107], v[36:39]\n"
    "ds_read_b64_tr_b16 v[104:105], v12 offset:12800\n"
    "ds_read_b64_tr_b16 v[106:107], v12 offset:13056\n"
    "v_mfma_f32_16x16x32_bf16 v[40:43], a[20:23], v[108:111], v[40:43]\n"
    "v_mfma_f32_16x16x32_bf16 v[44:47], a[84:87], v[108:111], v[44:47]\n"
    "ds_read_b64_tr_b16 v[108:109], v13 offset:12800\n"
    "ds_read_b64_tr_b16 v[110:111], v13 offset:13056\n"
    "s_waitcnt lgkmcnt(12)\n"
    "v_mfma_f32_16x16x32_bf16 v[16:19], a[24:27], v[80:83], v[16:19]\n"
    "v_mfma_f32_16x16x32_bf16 v[20:23], a[88:91], v[80:83], v[20:23]\n"
    "ds_read_b64_tr_b16 v[80:81], v12 offset:16384\n"
    "ds_read_b64_tr_b16 v[82:83], v12 offset:16640\n"
    "v_mfma_f32_16x16x32_bf16 v[24:27], a[24:27], v[84:87], v[24:27]\n"
    "v_mfma_f32_16x16x32_bf16 v[28:31], a[88:91], v[84:87], v[28:31]\n"
    "ds_read_b64_tr_b16 v[84:85], v13 offset:16384\n"
    "ds_read_b64_tr_b16 v[86:87], v13 offset:16640\n"
    "s_waitcnt lgkmcnt(12)\n"
    "v_mfma_f32_16x16x32_bf16 v[32:35], a[24:27], v[88:91], v[32:35]\n"
    "v_mfma_f32_16x16x32_bf16 v[36:39], a[88:91], v[88:91], v[36:39]\n"
    "ds_read_b64_tr_b16 v[88:89], v12 offset:16896\n"
    "ds_read_b64_tr_b16 v[90:91], v12 offset:17152\n"
    "v_mfma_f32_16x16x32_bf16 v[40:43], a[24:27], v[92:95], v[40:43]\n"
    "s_mov_b32 s71, 8192\n"
    "buffer_store_dwordx4 v[60:63], v2, s[36:39], s71 offen offset:3072 sc1\n"
    "v_mfma_f32_16x16x32_bf16 v[44:47], a[88:91], v[92:95], v[44:47]\n"
    "ds_read_b64_tr_b16 v[92:93], v13 offset:16896\n"
    "ds_read_b64_tr_b16 v[94:95], v13 offset:17152\n"
    "s_waitcnt lgkmcnt(12)\n"
    "v_mfma_f32_16x16x32_bf16 v[16:19], a[28:31], v[96:99], v[16:19]\n"
    "v_mfma_f32_16x16x32_bf16 v[20:23], a[92:95], v[96:99], v[20:23]\n"
    "ds_read_b64_tr_b16 v[96:97], v12 offset:20480\n"
    "ds_read_b64_tr_b16 v[98:99], v12 offset:20736\n"
    "v_mfma_f32_16x16x32_bf16 v[24:27], a[28:31], v[100:103], v[24:27]\n"
    "v_mfma_f32_16x16x32_bf16 v[28:31], a[92:95], v[100:103], v[28:31]\n"
    "ds_read_b64_tr_b16 v[100:101], v13 offset:20480\n"
    "ds_read_b64_tr_b16 v[102:103], v13 offset:20736\n"
    "s_waitcnt lgkmcnt(12)\n"
    "v_mfma_f32_16x16x32_bf16 v[32:35], a[28:31], v[104:107], v[32:35]\n"
    "v_mfma_f32_16x16x32_bf16 v[36:39], a[92:95], v[104:107], v[36:39]\n"
    "ds_read_b64_tr_b16 v[104:105], v12 offset:20992\n"
    "ds_read_b64_tr_b16 v[106:107], v12 offset:21248\n"
    "v_mfma_f32_16x16x32_bf16 v[40:43], a[28:31], v[108:111], v[40:43]\n"
    "v_mfma_f32_16x16x32_bf16 v[44:47], a[92:95], v[108:111], v[44:47]\n"
    "ds_read_b64_tr_b16 v[108:109], v13 offset:20992\n"
    "ds_read_b64_tr_b16 v[110:111], v13 offset:21248\n"
    "s_cmp_eq_u32 s60, 0\n"
    "s_cbranch_scc0 L_nopoll_%=\n"
    "s_mov_b32 s69, 100000\n"
    "s_waitcnt vmcnt(6)\n"
    "L_poll1_%=:\n"
    "v_xor_b32 v112, s21, v112\n"
    "v_xor_b32 v113, s21, v113\n"
    "v_xor_b32 v114, s21, v114\n"
    "v_xor_b32 v115, s21, v115\n"
    "v_or3_b32 v112, v112, v113, v114\n"
    "v_or_b32 v112, v112, v115\n"
    "v_cmp_ne_u32 vcc, 0, v112\n"
    "s_sub_u32 s69, s69, 1\n"
    "s_cbranch_vccz L_nopoll_%=\n"
    "s_and_b32 s66, s69, 7\n"
    "s_cbranch_scc1 L_noecho_%=\n"
    "global_load_dwordx4 v[116:119], v120, s[96:97] sc1\n"
    "s_waitcnt vmcnt(0)\n"
    "global_store_dwordx4 v121, v[116:119], s[96:97] sc1\n"
    "L_noecho_%=:\n"
    "s_add_u32 s66, s20, 393216\n"
    "v_lshlrev_b32 v122, 10, v3\n"
    "v_add_u32 v122, s66, v122\n"
    "global_load_dword v112, v122, s[96:97] sc1\n"
    "v_add_u32 v122, 0x10000, v122\n"
    "global_load_dword v113, v122, s[96:97] sc1\n"
    "v_add_u32 v122, 0x10000, v122\n"
    "global_load_dword v114, v122, s[96:97] sc1\n"
    "v_add_u32 v122, 0x10000, v122\n"
    "global_load_dword v115, v122, s[96:97] sc1\n"
    "s_waitcnt vmcnt(0)\n"
    "s_cmp_lg_u32 s69, 0\n"
    "s_cbranch_scc1 L_poll1_%=\n"
    "L_nopoll_%=:\n"
    "s_waitcnt lgkmcnt(12)\n"
    "v_mfma_f32_16x16x32_bf16 v[16:19], a[32:35], v[80:83], v[16:19]\n"
    "v_mfma_f32_16x16x32_bf16 v[20:23], a[96:99], v[80:83], v[20:23]\n"
    "ds_read_b64_tr_b16 v[80:81], v12 offset:24576\n"
    "ds_read_b64_tr_b16 v[82:83], v12 offset:24832\n"
    "v_mfma_f32_16x16x32_bf16 v[24:27], a[32:35], v[84:87], v[24:27]\n"
    "v_mfma_f32_16x16x32_bf16 v[28:31], a[96:99], v[84:87], v[28:31]\n"
    "ds_read_b64_tr_b16 v[84:85], v13 offset:24576\n"
    "ds_read_b64_tr_b16 v[86:87], v13 offset:24832\n"
    "s_waitcnt lgkmcnt(12)\n"
    "v_mfma_f32_16x16x32_bf16 v[32:35], a[32:35], v[88:91], v[32:35]\n"
    "v_mfma_f32_16x16x32_bf16 v[36:39], a[96:99], v[88:91], v[36:39]\n"
    "ds_read_b64_tr_b16 v[88:89], v12 offset:25088\n"
    "ds_read_b64_tr_b16 v[90:91], v12 offset:25344\n"
    "v_mfma_f32_16x16x32_bf16 v[40:43], a[32:35], v[92:95], v[40:43]\n"
    "v_mfma_f32_16x16x32_bf16 v[44:47], a[96:99], v[92:95], v[44:47]\n"
    "ds_read_b64_tr_b16 v[92:93], v13 offset:25088\n"
    "ds_read_b64_tr_b16 v[94:95], v13 offset:25344\n"
    "s_waitcnt lgkmcnt(12)\n"
    "v_mfma_f32_16x16x32_bf16 v[16:19], a[36:39], v[96:99], v[16:19]\n"
    "v_mfma_f32_16x16x32_bf16 v[20:23], a[100:103], v[96:99], v[20:23]\n"
    "ds_read_b64_tr_b16 v[96:97], v12 offset:28672\n"
    "ds_read_b64_tr_b16 v[98:99], v12 offset:28928\n"
    "v_mfma_f32_16x16x32_bf16 v[24:27], a[36:39], v[100:103], v[24:27]\n"
    "v_mfma_f32_16x16x32_bf16 v[28:31], a[100:103], v[100:103], v[28:31]\n"
    "ds_read_b64_tr_b16 v[100:101], v13 offset:28672\n"
    "ds_read_b64_tr_b16 v[102:103], v13 offset:28928\n"
    "s_waitcnt lgkmcnt(12)\n"
    "v_mfma_f32_16x16x32_bf16 v[32:35], a[36:39], v[104:107], v[32:35]\n"
    "v_mfma_f32_16x16x32_bf16 v[36:39], a[100:103], v[104:107], v[36:39]\n"
    "ds_read_b64_tr_b16 v[104:105], v12 offset:29184\n"
    "ds_read_b64_tr_b16 v[106:107], v12 offset:29440\n"
    "v_mfma_f32_16x16x32_bf16 v[40:43], a[36:39], v[108:111], v[40:43]\n"
    "v_mfma_f32_16x16x32_bf16 v[44:47], a[100:103], v[108:111], v[44:47]\n"
    "ds_read_b64_tr_b16 v[108:109], v13 offset:29184\n"
    "ds_read_b64_tr_b16 v[110:111], v13 offset:29440\n"
    "s_barrier\n"
    "s_waitcnt lgkmcnt(12)\n"
    "v_mfma_f32_16x16x32_bf16 v[16:19], a[40:43], v[80:83], v[16:19]\n"
    "global_load_dword v8, v4, s[96:97] sc1\n"
    "v_mfma_f32_16x16x32_bf16 v[20:23], a[104:107], v[80:83], v[20:23]\n"
    "ds_read_b64_tr_b16 v[80:81], v12 offset:32768\n"
    "ds_read_b64_tr_b16 v[82:83], v12 offset:33024\n"
    "v_mfma_f32_16x16x32_bf16 v[24:27], a[40:43], v[84:87], v[24:27]\n"
    "global_load_dword v9, v4, s[96:97] offset:2048 sc1\n"
    "v_mfma_f32_16x16x32_bf16 v[28:31], a[104:107], v[84:87], v[28:31]\n"
    "ds_read_b64_tr_b16 v[84:85], v13 offset:32768\n"
    "ds_read_b64_tr_b16 v[86:87], v13 offset:33024\n"
    "s_waitcnt lgkmcnt(12)\n"
    "v_mfma_f32_16x16x32_bf16 v[32:35], a[40:43], v[88:91], v[32:35]\n"
    "buffer_load_dwordx4 v[64:67], v2, s[40:43], 0 offen offset:0 sc1\n"
    "v_mfma_f32_16x16x32_bf16 v[36:39], a[104:107], v[88:91], v[36:39]\n"
    "ds_read_b64_tr_b16 v[88:89], v12 offset:33280\n"
    "ds_read_b64_tr_b16 v[90:91], v12 offset:33536\n"
    "v_mfma_f32_16x16x32_bf16 v[40:43], a[40:43], v[92:95], v[40:43]\n"
    "buffer_load_dwordx4 v[68:71], v2, s[40:43], s23 offen offset:0 sc1\n"
    "v_mfma_f32_16x16x32_bf16 v[44:47], a[104:107], v[92:95], v[44:47]\n"
    "ds_read_b64_tr_b16 v[92:93], v13 offset:33280\n"
    "ds_read_b64_tr_b16 v[94:95], v13 offset:33536\n"
    "s_waitcnt lgkmcnt(12)\n"
    "v_mfma_f32_16x16x32_bf16 v[16:19], a[44:47], v[96:99], v[16:19]\n"
    "buffer_load_dwordx4 v[72:75], v2, s[40:43], 0 offen offset:1024 sc1\n"
    "v_mfma_f32_16x16x32_bf16 v[20:23], a[108:111], v[96:99], v[20:23]\n"
    "ds_read_b64_tr_b16 v[96:97], v12 offset:36864\n"
    "ds_read_b64_tr_b16 v[98:99], v12 offset:37120\n"
    "v_mfma_f32_16x16x32_bf16 v[24:27], a[44:47], v[100:103], v[24:27]\n"
    "buffer_load_dwordx4 v[76:79], v2, s[40:43], s23 offen offset:1024 sc1\n"
    "v_mfma_f32_16x16x32_bf16 v[28:31], a[108:111], v[100:103], v[28:31]\n"
    "ds_read_b64_tr_b16 v[100:101], v13 offset:36864\n"
    "ds_read_b64_tr_b16 v[102:103], v13 offset:37120\n"
    "s_waitcnt lgkmcnt(12)\n"
    "v_mfma_f32_16x16x32_bf16 v[32:35], a[44:47], v[104:107], v[32:35]\n"
    "v_mfma_f32_16x16x32_bf16 v[36:39], a[108:111], v[104:107], v[36:39]\n"
    "ds_read_b64_tr_b16 v[104:105], v12 offset:37376\n"
    "ds_read_b64_tr_b16 v[106:107], v12 offset:37632\n"
    "v_mfma_f32_16x16x32_bf16 v[40:43], a[44:47], v[108:111], v[40:43]\n"
    "v_mfma_f32_16x16x32_bf16 v[44:47], a[108:111], v[108:111], v[44:47]\n"
    "ds_read_b64_tr_b16 v[108:109], v13 offset:37376\n"
    "ds_read_b64_tr_b16 v[110:111], v13 offset:37632\n"
    "s_waitcnt lgkmcnt(12)\n"
    "v_mfma_f32_16x16x32_bf16 v[16:19], a[48:51], v[80:83], v[16:19]\n"
    "s_nop 0\n"
    "s_nop 0\n"
    "s_nop 0\n"
    "v_mfma_f32_16x16x32_bf16 v[20:23], a[112:115], v[80:83], v[20:23]\n"
    "ds_read_b64_tr_b16 v[80:81], v12 offset:40960\n"
    "ds_read_b64_tr_b16 v[82:83], v12 offset:41216\n"
    "v_mfma_f32_16x16x32_bf16 v[24:27], a[48:51], v[84:87], v[24:27]\n"
    "s_nop 0\n"
    "s_nop 0\n"
    "s_nop 0\n"
    "v_mfma_f32_16x16x32_bf16 v[28:31], a[112:115], v[84:87], v[28:31]\n"
    "ds_read_b64_tr_b16 v[84:85], v13 offset:40960\n"
    "ds_read_b64_tr_b16 v[86:87], v13 offset:41216\n"
    "s_waitcnt lgkmcnt(12)\n"
    "v_mfma_f32_16x16x32_bf16 v[32:35], a[48:51], v[88:91], v[32:35]\n"
    "s_nop 0\n"
    "s_nop 0\n"
    "s_nop 0\n"
    "v_mfma_f32_16x16x32_bf16 v[36:39], a[112:115], v[88:91], v[36:39]\n"
    "ds_read_b64_tr_b16 v[88:89], v12 offset:41472\n"
    "ds_read_b64_tr_b16 v[90:91], v12 offset:41728\n"
    "v_mfma_f32_16x16x32_bf16 v[40:43], a[48:51], v[92:95], v[40:43]\n"
    "s_nop 0\n"
    "s_nop 0\n"
    "s_nop 0\n"
    "v_mfma_f32_16x16x32_bf16 v[44:47], a[112:115], v[92:95], v[44:47]\n"
    "ds_read_b64_tr_b16 v[92:93], v13 offset:41472\n"
    "ds_read_b64_tr_b16 v[94:95], v13 offset:41728\n"
    "s_waitcnt lgkmcnt(12)\n"
    "v_mfma_f32_16x16x32_bf16 v[16:19], a[52:55], v[96:99], v[16:19]\n"
    "s_waitcnt vmcnt(4)\n"
    "v_add_f32 v9, 1.0, v9\n"
    "v_cvt_pk_bf16_f32 v9, v9, v9\n"
    "v_lshlrev_b32 v9, 16, v9\n"
    "v_mfma_f32_16x16x32_bf16 v[20:23], a[116:119], v[96:99], v[20:23]\n"
    "ds_read_b64_tr_b16 v[96:97], v12 offset:45056\n"
    "ds_read_b64_tr_b16 v[98:99], v12 offset:45312\n"
    "v_mfma_f32_16x16x32_bf16 v[24:27], a[52:55], v[100:103], v[24:27]\n"
    "v_mul_f32 v9, 0x3e0293ee, v9\n"
    "v_mul_f32 v8, 0x3e0293ee, v8\n"
    "ds_write_b32 v15, v9\n"
    "v_mfma_f32_16x16x32_bf16 v[28:31], a[116:119], v[100:103], v[28:31]\n"
    "ds_read_b64_tr_b16 v[100:101], v13 offset:45056\n"
    "ds_read_b64_tr_b16 v[102:103], v13 offset:45312\n"
    "s_waitcnt lgkmcnt(13)\n"
    "v_mfma_f32_16x16x32_bf16 v[32:35], a[52:55], v[104:107], v[32:35]\n"
    "ds_write_b32 v15, v8 offset:32\n"
    "v_mfma_f32_16x16x32_bf16 v[36:39], a[116:119], v[104:107], v[36:39]\n"
    "ds_read_b64_tr_b16 v[104:105], v12 offset:45568\n"
    "ds_read_b64_tr_b16 v[106:107], v12 offset:45824\n"
    "v_mfma_f32_16x16x32_bf16 v[40:43], a[52:55], v[108:111], v[40:43]\n"
    "v_mfma_f32_16x16x32_bf16 v[44:47], a[116:119], v[108:111], v[44:47]\n"
    "ds_read_b64_tr_b16 v[108:109], v13 offset:45568\n"
    "ds_read_b64_tr_b16 v[110:111], v13 offset:45824\n"
    "s_waitcnt lgkmcnt(14)\n"
    "v_mfma_f32_16x16x32_bf16 v[16:19], a[56:59], v[80:83], v[16:19]\n"
    "v_mfma_f32_16x16x32_bf16 v[20:23], a[120:123], v[80:83], v[20:23]\n"
    "v_mfma_f32_16x16x32_bf16 v[24:27], a[56:59], v[84:87], v[24:27]\n"
    "v_mfma_f32_16x16x32_bf16 v[28:31], a[120:123], v[84:87], v[28:31]\n"
    "s_waitcnt lgkmcnt(10)\n"
    "v_mfma_f32_16x16x32_bf16 v[32:35], a[56:59], v[88:91], v[32:35]\n"
    "v_mfma_f32_16x16x32_bf16 v[36:39], a[120:123], v[88:91], v[36:39]\n"
    "v_mfma_f32_16x16x32_bf16 v[40:43], a[56:59], v[92:95], v[40:43]\n"
    "v_mfma_f32_16x16x32_bf16 v[44:47], a[120:123], v[92:95], v[44:47]\n"
    "s_waitcnt lgkmcnt(5)\n"
    "v_mfma_f32_16x16x32_bf16 v[16:19], a[60:63], v[96:99], v[16:19]\n"
    "v_mfma_f32_16x16x32_bf16 v[20:23], a[124:127], v[96:99], v[20:23]\n"
    "v_mfma_f32_16x16x32_bf16 v[24:27], a[60:63], v[100:103], v[24:27]\n"
    "v_mfma_f32_16x16x32_bf16 v[28:31], a[124:127], v[100:103], v[28:31]\n"
    "s_waitcnt lgkmcnt(0)\n"
    "v_mfma_f32_16x16x32_bf16 v[32:35], a[60:63], v[104:107], v[32:35]\n"
    "v_mfma_f32_16x16x32_bf16 v[36:39], a[124:127], v[104:107], v[36:39]\n"
    "v_mfma_f32_16x16x32_bf16 v[40:43], a[60:63], v[108:111], v[40:43]\n"
    "v_mfma_f32_16x16x32_bf16 v[44:47], a[124:127], v[108:111], v[44:47]\n"
    "v_cvt_pk_bf16_f32 v112, v16, v17\n"
    "v_cvt_pk_bf16_f32 v113, v18, v19\n"
    "v_cvt_pk_bf16_f32 v114, v20, v21\n"
    "v_cvt_pk_bf16_f32 v115, v22, v23\n"
    "v_cvt_pk_bf16_f32 v116, v24, v25\n"
    "v_cvt_pk_bf16_f32 v117, v26, v27\n"
    "v_cvt_pk_bf16_f32 v118, v28, v29\n"
    "v_cvt_pk_bf16_f32 v119, v30, v31\n"
    "v_cvt_pk_bf16_f32 v120, v32, v33\n"
    "v_cvt_pk_bf16_f32 v121, v34, v35\n"
    "v_cvt_pk_bf16_f32 v122, v36, v37\n"
    "v_cvt_pk_bf16_f32 v123, v38, v39\n"
    "v_cvt_pk_bf16_f32 v124, v40, v41\n"
    "v_cvt_pk_bf16_f32 v125, v42, v43\n"
    "v_cvt_pk_bf16_f32 v126, v44, v45\n"
    "v_cvt_pk_bf16_f32 v127, v46, v47\n"
    "s_mov_b64 s[84:85], s[36:37]\n"
    "s_mov_b64 s[86:87], s[38:39]\n"
    "s_waitcnt vmcnt(0)\n"
    "s_load_dwordx2 s[88:89], %0, 40\n"
    "s_load_dwordx2 s[90:91], %0, 48\n"
    "s_load_dwordx2 s[92:93], %0, 56\n"
    "s_waitcnt lgkmcnt(0)\n"
    "s_barrier\n"
    "v_and_b32 v8, 63, %2\n"
    "v_lshrrev_b32 v9, 6, %2\n"
    "v_lshlrev_b32 v2, 4, v8\n"
    "v_readfirstlane_b32 s34, v9\n"
    "v_lshlrev_b32 v10, 2, %2\n"
    "s_and_b32 s52, %1, 7\n"
    "s_lshr_b32 s53, %1, 3\n"
    "s_and_b32 s35, s53, 7\n"
    "s_lshr_b32 s53, s53, 3\n"
    "s_lshl_b32 s52, s52, 2\n"
    "s_add_u32 s36, s52, s53\n"
    "s_lshl_b32 s52, s36, 8\n"
    "s_sub_u32 s54, 0x400000, s52\n"
    "s_mov_b64 s[24:25], s[94:95]\n"
    "s_add_u32 s24, s24, s52\n"
    "s_addc_u32 s25, s25, 0\n"
    "s_and_b32 s25, s25, 0xffff\n"
    "s_mov_b32 s26, s54\n"
    "s_mov_b32 s27, 0x20000\n"
    "v_bfe_u32 v9, v8, 2, 3\n"
    "v_lshl_add_u32 v9, s34, 3, v9\n"
    "v_lshlrev_b32 v16, 13, v9\n"
    "v_lshrrev_b32 v9, 5, v8\n"
    "v_lshl_add_u32 v16, v9, 6, v16\n"
    "v_and_b32 v10, 3, v8\n"
    "v_bfe_u32 v9, v8, 4, 1\n"
    "v_lshlrev_b32 v9, 1, v9\n"
    "v_xor_b32 v10, v10, v9\n"
    "v_lshl_add_u32 v16, v10, 4, v16\n"
    "s_lshl_b32 s64, s34, 11\n"
    "s_lshr_b32 s50, s34, 2\n"
    "s_and_b32 s51, s34, 3\n"
    "s_sub_u32 s52, 15, s35\n"
    "s_lshl_b32 s52, s52, 2\n"
    "s_add_u32 s49, s52, 4\n"
    "s_add_u32 s33, s52, s51\n"
    "s_lshl_b32 s53, s35, 2\n"
    "s_add_u32 s53, s53, 3\n"
    "s_sub_u32 s53, s53, s51\n"
    "s_add_u32 s32, s53, 3\n"
    "s_cmp_eq_u32 s50, 1\n"
    "s_cselect_b32 s37, s53, s33\n"
    "s_lshl_b32 s52, s37, 15\n"
    "s_mov_b64 s[20:21], s[76:77]\n"
    "s_add_u32 s20, s20, s52\n"
    "s_addc_u32 s21, s21, 0\n"
    "s_and_b32 s21, s21, 0xffff\n"
    "s_mov_b32 s22, 0x8000\n"
    "s_mov_b32 s23, 0x20000\n"
    "s_mov_b32 s62, 0\n"
    "s_mov_b32 s59, 16384\n"
    "s_cmp_lt_u32 s32, s33\n"
    "s_cselect_b32 s32, s32, 0x1000\n"
    "s_lshl_b32 s52, s35, 2\n"
    "s_add_u32 s52, s52, 6\n"
    "s_sub_u32 s53, s49, 4\n"
    "s_cmp_lt_u32 s52, s53\n"
    "s_cselect_b32 s65, s52, 0x1000\n"
    "s_lshl_b32 s52, s36, 20\n"
    "s_lshl_b32 s54, s49, 14\n"
    "s_mov_b64 s[16:17], s[74:75]\n"
    "s_add_u32 s16, s16, s52\n"
    "s_addc_u32 s17, s17, 0\n"
    "s_and_b32 s17, s17, 0xffff\n"
    "s_mov_b32 s18, s54\n"
    "s_mov_b32 s19, 0x20000\n"
    "s_lshl_b32 s52, s37, 18\n"
    "s_lshl_b32 s53, s36, 12\n"
    "s_add_u32 s52, s52, s53\n"
    "s_mov_b64 s[28:29], s[78:79]\n"
    "s_add_u32 s28, s28, s52\n"
    "s_addc_u32 s29, s29, 0\n"
    "s_and_b32 s29, s29, 0xffff\n"
    "s_mov_b32 s30, 0x21000\n"
    "s_mov_b32 s31, 0x20000\n"
    "s_mov_b32 s44, 0xffff0000\n"
    "s_mov_b32 s43, 0x4b800000\n"
    "buffer_load_dwordx4 v[80:83], v2, s[20:23], s62 offen offset:2048 sc1\n"
    "buffer_load_dwordx4 v[84:87], v2, s[20:23], s59 offen offset:2048 sc1\n"
    "buffer_load_dwordx4 v[88:91], v2, s[20:23], s62 offen offset:3072 sc1\n"
    "buffer_load_dwordx4 v[92:95], v2, s[20:23], s59 offen offset:3072 sc1\n"
    "s_add_u32 m0, s64, 65536\n"
    "s_mov_b32 s53, 1572864\n"
    "buffer_load_dwordx4 v16, s[24:27], s53 offen lds\n"
    "s_add_u32 m0, s64, 66560\n"
    "s_mov_b32 s53, 1572992\n"
    "buffer_load_dwordx4 v16, s[24:27], s53 offen lds\n"
    "s_mov_b32 s52, 12288\n"
    "buffer_store_dwordx4 v[112:115], v2, s[84:87], s52 offen offset:0 sc1\n"
    "buffer_store_dwordx4 v[116:119], v2, s[84:87], s52 offen offset:1024 sc1\n"
    "buffer_store_dwordx4 v[120:123], v2, s[84:87], s52 offen offset:2048 sc1\n"
    "buffer_store_dwordx4 v[124:127], v2, s[84:87], s52 offen offset:3072 sc1\n"
    "v_and_b32 v8, 63, %2\n"
    "v_lshrrev_b32 v9, 5, v8\n"
    "v_lshlrev_b32 v13, 11, v9\n"
    "v_bfe_u32 v10, v8, 2, 3\n"
    "v_lshl_add_u32 v13, v10, 6, v13\n"
    "v_and_b32 v10, 3, v8\n"
    "v_lshl_add_u32 v13, v10, 3, v13\n"
    "v_bfe_u32 v10, v8, 4, 1\n"
    "v_lshl_add_u32 v13, v10, 5, v13\n"
    "v_add_u32 v13, 0x4000, v13\n"
    "v_xor_b32 v14, 32, v13\n"
    "v_lshrrev_b32 v9, 4, v8\n"
    "v_lshlrev_b32 v15, 6, v9\n"
    "v_add_u32 v15, 147456, v15\n"
    "v_and_b32 v18, 15, v8\n"
    "v_lshlrev_b32 v9, 2, v9\n"
    "v_sub_u32 v18, v18, v9\n"
    "v_add_u32 v19, s64, v2\n"
    "v_lshrrev_b32 v9, 5, %2\n"
    "v_lshlrev_b32 v9, 8, v9\n"
    "v_bfe_u32 v10, %2, 2, 2\n"
    "v_lshl_add_u32 v9, v10, 6, v9\n"
    "v_and_b32 v10, 3, %2\n"
    "v_lshl_add_u32 v9, v10, 2, v9\n"
    "v_bfe_u32 v10, %2, 4, 1\n"
    "v_lshl_add_u32 v9, v10, 4, v9\n"
    "v_add_u32 v9, 147456, v9\n"
    "s_cmp_eq_u32 s50, 1\n"
    "s_cselect_b64 vcc, -1, 0\n"
    "s_cbranch_scc0 L_noprio_%=\n"
    "s_setprio 1\n"
    "L_noprio_%=:\n"
    "s_mov_b32 s61, 0\n"
    "ds_read_b128 v[32:35], v15 offset:0\n"
    "ds_read_b128 v[36:39], v15 offset:16\n"
    "ds_read_b128 v[40:43], v15 offset:32\n"
    "ds_read_b128 v[44:47], v15 offset:48\n"
    "ds_read_b128 v[48:51], v15 offset:256\n"
    "ds_read_b128 v[52:55], v15 offset:272\n"
    "ds_read_b128 v[56:59], v15 offset:288\n"
    "ds_read_b128 v[60:63], v15 offset:304\n"
    "ds_read_b64_tr_b16 a[64:65], v13 offset:0\n"
    "ds_read_b64_tr_b16 a[66:67], v13 offset:4096\n"
    "ds_read_b64_tr_b16 a[68:69], v14 offset:0\n"
    "ds_read_b64_tr_b16 a[70:71], v14 offset:4096\n"
    "ds_read_b64_tr_b16 a[72:73], v13 offset:512\n"
    "ds_read_b64_tr_b16 a[74:75], v13 offset:4608\n"
    "ds_read_b64_tr_b16 a[76:77], v14 offset:512\n"
    "ds_read_b64_tr_b16 a[78:79], v14 offset:4608\n"
    "ds_read_b64_tr_b16 a[80:81], v13 offset:1024\n"
    "ds_read_b64_tr_b16 a[82:83], v13 offset:5120\n"
    "ds_read_b64_tr_b16 a[84:85], v14 offset:1024\n"
    "ds_read_b64_tr_b16 a[86:87], v14 offset:5120\n"
    "ds_read_b64_tr_b16 a[88:89], v13 offset:1536\n"
    "ds_read_b64_tr_b16 a[90:91], v13 offset:5632\n"
    "ds_read_b64_tr_b16 a[92:93], v14 offset:1536\n"
    "ds_read_b64_tr_b16 a[94:95], v14 offset:5632\n"
    "s_waitcnt lgkmcnt(15)\n"
    "v_fma_mix_f32 v112, v64, v32, v40 op_sel:[0,0,0] op_sel_hi:[1,0,0]\n"
    "v_fma_mix_f32 v113, v64, v33, v41 op_sel:[1,0,0] op_sel_hi:[1,0,0]\n"
    "v_fma_mix_f32 v114, v65, v34, v42 op_sel:[0,0,0] op_sel_hi:[1,0,0]\n"
    "v_fma_mix_f32 v115, v65, v35, v43 op_sel:[1,0,0] op_sel_hi:[1,0,0]\n"
    "v_fma_mix_f32 v116, v66, v36, v44 op_sel:[0,0,0] op_sel_hi:[1,0,0]\n"
    "v_fma_mix_f32 v117, v66, v37, v45 op_sel:[1,0,0] op_sel_hi:[1,0,0]\n"
    "v_fma_mix_f32 v118, v67, v38, v46 op_sel:[0,0,0] op_sel_hi:[1,0,0]\n"
    "v_fma_mix_f32 v119, v67, v39, v47 op_sel:[1,0,0] op_sel_hi:[1,0,0]\n"
    "v_cvt_pk_bf16_f32 v96, v112, v113\n"
    "v_cvt_pk_bf16_f32 v97, v114, v115\n"
    "v_cvt_pk_bf16_f32 v98, v116, v117\n"
    "v_cvt_pk_bf16_f32 v99, v118, v119\n"
    "v_fma_mix_f32 v120, v68, v32, v40 op_sel:[0,0,0] op_sel_hi:[1,0,0]\n"
    "v_fma_mix_f32 v121, v68, v33, v41 op_sel:[1,0,0] op_sel_hi:[1,0,0]\n"
    "v_fma_mix_f32 v122, v69, v34, v42 op_sel:[0,0,0] op_sel_hi:[1,0,0]\n"
    "v_fma_mix_f32 v123, v69, v35, v43 op_sel:[1,0,0] op_sel_hi:[1,0,0]\n"
    "v_fma_mix_f32 v124, v70, v36, v44 op_sel:[0,0,0] op_sel_hi:[1,0,0]\n"
    "v_fma_mix_f32 v125, v70, v37, v45 op_sel:[1,0,0] op_sel_hi:[1,0,0]\n"
    "v_fma_mix_f32 v126, v71, v38, v46 op_sel:[0,0,0] op_sel_hi:[1,0,0]\n"
    "v_fma_mix_f32 v127, v71, v39, v47 op_sel:[1,0,0] op_sel_hi:[1,0,0]\n"
    "v_cvt_pk_bf16_f32 v100, v120, v121\n"
    "v_cvt_pk_bf16_f32 v101, v122, v123\n"
    "v_cvt_pk_bf16_f32 v102, v124, v125\n"
    "v_cvt_pk_bf16_f32 v103, v126, v127\n"
    "v_fma_mix_f32 v112, v72, v48, v56 op_sel:[0,0,0] op_sel_hi:[1,0,0]\n"
    "v_fma_mix_f32 v113, v72, v49, v57 op_sel:[1,0,0] op_sel_hi:[1,0,0]\n"
    "v_fma_mix_f32 v114, v73, v50, v58 op_sel:[0,0,0] op_sel_hi:[1,0,0]\n"
    "v_fma_mix_f32 v115, v73, v51, v59 op_sel:[1,0,0] op_sel_hi:[1,0,0]\n"
    "v_fma_mix_f32 v116, v74, v52, v60 op_sel:[0,0,0] op_sel_hi:[1,0,0]\n"
    "v_fma_mix_f32 v117, v74, v53, v61 op_sel:[1,0,0] op_sel_hi:[1,0,0]\n"
    "v_fma_mix_f32 v118, v75, v54, v62 op_sel:[0,0,0] op_sel_hi:[1,0,0]\n"
    "v_fma_mix_f32 v119, v75, v55, v63 op_sel:[1,0,0] op_sel_hi:[1,0,0]\n"
    "v_cvt_pk_bf16_f32 v104, v112, v113\n"
    "v_cvt_pk_bf16_f32 v105, v114, v115\n"
    "v_cvt_pk_bf16_f32 v106, v116, v117\n"
    "v_cvt_pk_bf16_f32 v107, v118, v119\n"
    "v_fma_mix_f32 v120, v76, v48, v56 op_sel:[0,0,0] op_sel_hi:[1,0,0]\n"
    "v_fma_mix_f32 v121, v76, v49, v57 op_sel:[1,0,0] op_sel_hi:[1,0,0]\n"
    "v_fma_mix_f32 v122, v77, v50, v58 op_sel:[0,0,0] op_sel_hi:[1,0,0]\n"
    "v_fma_mix_f32 v123, v77, v51, v59 op_sel:[1,0,0] op_sel_hi:[1,0,0]\n"
    "v_fma_mix_f32 v124, v78, v52, v60 op_sel:[0,0,0] op_sel_hi:[1,0,0]\n"
    "v_fma_mix_f32 v125, v78, v53, v61 op_sel:[1,0,0] op_sel_hi:[1,0,0]\n"
    "v_fma_mix_f32 v126, v79, v54, v62 op_sel:[0,0,0] op_sel_hi:[1,0,0]\n"
    "v_fma_mix_f32 v127, v79, v55, v63 op_sel:[1,0,0] op_sel_hi:[1,0,0]\n"
    "v_cvt_pk_bf16_f32 v108, v120, v121\n"
    "v_cvt_pk_bf16_f32 v109, v122, v123\n"
    "v_cvt_pk_bf16_f32 v110, v124, v125\n"
    "v_cvt_pk_bf16_f32 v111, v126, v127\n"
    "s_cbranch_vccz L_qa0_0_%=\n"
    "s_barrier\n"
    "L_qa0_0_%=:\n"
    "s_waitcnt lgkmcnt(8)\n"
    "v_mfma_f32_16x16x32_bf16 a[0:3], a[64:67], v[96:99], 0\n"
    "ds_read_b128 v[32:35], v15 offset:512\n"
    "s_add_u32 m0, s64, 81920\n"
    "s_mov_b32 s53, 2097152\n"
    "buffer_load_dwordx4 v16, s[24:27], s53 offen lds\n"
    "v_mfma_f32_16x16x32_bf16 a[32:35], a[64:67], v[100:103], 0\n"
    "ds_read_b64_tr_b16 a[64:65], v13 offset:8192\n"
    "ds_read_b64_tr_b16 a[66:67], v13 offset:12288\n"
    "v_mfma_f32_16x16x32_bf16 a[4:7], a[68:71], v[96:99], 0\n"
    "s_add_u32 m0, s64, 82944\n"
    "s_mov_b32 s53, 2097280\n"
    "buffer_load_dwordx4 v16, s[24:27], s53 offen lds\n"
    "v_mfma_f32_16x16x32_bf16 a[36:39], a[68:71], v[100:103], 0\n"
    "ds_read_b64_tr_b16 a[68:69], v14 offset:8192\n"
    "ds_read_b64_tr_b16 a[70:71], v14 offset:12288\n"
    "v_mfma_f32_16x16x32_bf16 a[8:11], a[72:75], v[96:99], 0\n"
    "ds_read_b128 v[36:39], v15 offset:528\n"
    "s_add_u32 m0, s64, 98304\n"
    "s_mov_b32 s53, 2621440\n"
    "buffer_load_dwordx4 v16, s[24:27], s53 offen lds\n"
    "v_mfma_f32_16x16x32_bf16 a[40:43], a[72:75], v[100:103], 0\n"
    "ds_read_b64_tr_b16 a[72:73], v13 offset:8704\n"
    "ds_read_b64_tr_b16 a[74:75], v13 offset:12800\n"
    "v_mfma_f32_16x16x32_bf16 a[12:15], a[76:79], v[96:99], 0\n"
    "s_add_u32 m0, s64, 99328\n"
    "s_mov_b32 s53, 2621568\n"
    "buffer_load_dwordx4 v16, s[24:27], s53 offen lds\n"
    "v_mfma_f32_16x16x32_bf16 a[44:47], a[76:79], v[100:103], 0\n"
    "ds_read_b64_tr_b16 a[76:77], v14 offset:8704\n"
    "ds_read_b64_tr_b16 a[78:79], v14 offset:12800\n"
    "s_waitcnt lgkmcnt(10)\n"
    "v_mfma_f32_16x16x32_bf16 a[16:19], a[80:83], v[96:99], 0\n"
    "ds_read_b128 v[40:43], v15 offset:544\n"
    "v_mfma_f32_16x16x32_bf16 a[48:51], a[80:83], v[100:103], 0\n"
    "ds_read_b64_tr_b16 a[80:81], v13 offset:9216\n"
    "ds_read_b64_tr_b16 a[82:83], v13 offset:13312\n"
    "v_mfma_f32_16x16x32_bf16 a[20:23], a[84:87], v[96:99], 0\n"
    "s_add_u32 s62, s62, 4096\n"
    "v_mfma_f32_16x16x32_bf16 a[52:55], a[84:87], v[100:103], 0\n"
    "ds_read_b64_tr_b16 a[84:85], v14 offset:9216\n"
    "ds_read_b64_tr_b16 a[86:87], v14 offset:13312\n"
    "v_mfma_f32_16x16x32_bf16 a[24:27], a[88:91], v[96:99], 0\n"
    "ds_read_b128 v[44:47], v15 offset:560\n"
    "v_mfma_f32_16x16x32_bf16 a[56:59], a[88:91], v[100:103], 0\n"
    "ds_read_b64_tr_b16 a[88:89], v13 offset:9728\n"
    "ds_read_b64_tr_b16 a[90:91], v13 offset:13824\n"
    "v_mfma_f32_16x16x32_bf16 a[28:31], a[92:95], v[96:99], 0\n"
    "s_add_u32 s59, s59, 4096\n"
    "v_mfma_f32_16x16x32_bf16 a[60:63], a[92:95], v[100:103], 0\n"
    "ds_read_b64_tr_b16 a[92:93], v14 offset:9728\n"
    "ds_read_b64_tr_b16 a[94:95], v14 offset:13824\n"
    "s_waitcnt lgkmcnt(10)\n"
    "v_mfma_f32_16x16x32_bf16 a[0:3], a[64:67], v[104:107], a[0:3]\n"
    "ds_read_b128 v[48:51], v15 offset:768\n"
    "v_mfma_f32_16x16x32_bf16 a[32:35], a[64:67], v[108:111], a[32:35]\n"
    "ds_read_b64_tr_b16 a[64:65], v13 offset:16384\n"
    "ds_read_b64_tr_b16 a[66:67], v13 offset:20480\n"
    "v_mfma_f32_16x16x32_bf16 a[4:7], a[68:71], v[104:107], a[4:7]\n"
    "buffer_load_dwordx4 v[64:67], v2, s[20:23], s62 offen offset:0 sc1\n"
    "v_mfma_f32_16x16x32_bf16 a[36:39], a[68:71], v[108:111], a[36:39]\n"
    "ds_read_b64_tr_b16 a[68:69], v14 offset:16384\n"
    "ds_read_b64_tr_b16 a[70:71], v14 offset:20480\n"
    "v_mfma_f32_16x16x32_bf16 a[8:11], a[72:75], v[104:107], a[8:11]\n"
    "ds_read_b128 v[52:55], v15 offset:784\n"
    "v_mfma_f32_16x16x32_bf16 a[40:43], a[72:75], v[108:111], a[40:43]\n"
    "ds_read_b64_tr_b16 a[72:73], v13 offset:16896\n"
    "ds_read_b64_tr_b16 a[74:75], v13 offset:20992\n"
    "v_mfma_f32_16x16x32_bf16 a[12:15], a[76:79], v[104:107], a[12:15]\n"
    "buffer_load_dwordx4 v[68:71], v2, s[20:23], s59 offen offset:0 sc1\n"
    "v_mfma_f32_16x16x32_bf16 a[44:47], a[76:79], v[108:111], a[44:47]\n"
    "ds_read_b64_tr_b16 a[76:77], v14 offset:16896\n"
    "ds_read_b64_tr_b16 a[78:79], v14 offset:20992\n"
    "s_waitcnt lgkmcnt(10)\n"
    "v_mfma_f32_16x16x32_bf16 a[16:19], a[80:83], v[104:107], a[16:19]\n"
    "ds_read_b128 v[56:59], v15 offset:800\n"
    "v_mfma_f32_16x16x32_bf16 a[48:51], a[80:83], v[108:111], a[48:51]\n"
    "ds_read_b64_tr_b16 a[80:81], v13 offset:17408\n"
    "ds_read_b64_tr_b16 a[82:83], v13 offset:21504\n"
    "v_mfma_f32_16x16x32_bf16 a[20:23], a[84:87], v[104:107], a[20:23]\n"
    "buffer_load_dwordx4 v[72:75], v2, s[20:23], s62 offen offset:1024 sc1\n"
    "v_mfma_f32_16x16x32_bf16 a[52:55], a[84:87], v[108:111], a[52:55]\n"
    "ds_read_b64_tr_b16 a[84:85], v14 offset:17408\n"
    "ds_read_b64_tr_b16 a[86:87], v14 offset:21504\n"
    "v_mfma_f32_16x16x32_bf16 a[24:27], a[88:91], v[104:107], a[24:27]\n"
    "ds_read_b128 v[60:63], v15 offset:816\n"
    "v_mfma_f32_16x16x32_bf16 a[56:59], a[88:91], v[108:111], a[56:59]\n"
    "ds_read_b64_tr_b16 a[88:89], v13 offset:17920\n"
    "ds_read_b64_tr_b16 a[90:91], v13 offset:22016\n"
    "s_cbranch_vccnz L_qb0_0_%=\n"
    "s_barrier\n"
    "L_qb0_0_%=:\n"
    "v_mfma_f32_16x16x32_bf16 a[28:31], a[92:95], v[104:107], a[28:31]\n"
    "buffer_load_dwordx4 v[76:79], v2, s[20:23], s59 offen offset:1024 sc1\n"
    "v_mfma_f32_16x16x32_bf16 a[60:63], a[92:95], v[108:111], a[60:63]\n"
    "ds_read_b64_tr_b16 a[92:93], v14 offset:17920\n"
    "ds_read_b64_tr_b16 a[94:95], v14 offset:22016\n"
    "s_waitcnt vmcnt(14)\n"
    "s_waitcnt lgkmcnt(4)\n"
    "v_fma_mix_f32 v112, v80, v32, v40 op_sel:[0,0,0] op_sel_hi:[1,0,0]\n"
    "v_fma_mix_f32 v113, v80, v33, v41 op_sel:[1,0,0] op_sel_hi:[1,0,0]\n"
    "v_fma_mix_f32 v114, v81, v34, v42 op_sel:[0,0,0] op_sel_hi:[1,0,0]\n"
    "v_fma_mix_f32 v115, v81, v35, v43 op_sel:[1,0,0] op_sel_hi:[1,0,0]\n"
    "v_fma_mix_f32 v116, v82, v36, v44 op_sel:[0,0,0] op_sel_hi:[1,0,0]\n"
    "v_fma_mix_f32 v117, v82, v37, v45 op_sel:[1,0,0] op_sel_hi:[1,0,0]\n"
    "v_fma_mix_f32 v118, v83, v38, v46 op_sel:[0,0,0] op_sel_hi:[1,0,0]\n"
    "v_fma_mix_f32 v119, v83, v39, v47 op_sel:[1,0,0] op_sel_hi:[1,0,0]\n"
    "v_cvt_pk_bf16_f32 v96, v112, v113\n"
    "v_cvt_pk_bf16_f32 v97, v114, v115\n"
    "v_cvt_pk_bf16_f32 v98, v116, v117\n"
    "v_cvt_pk_bf16_f32 v99, v118, v119\n"
    "v_fma_mix_f32 v120, v84, v32, v40 op_sel:[0,0,0] op_sel_hi:[1,0,0]\n"
    "v_fma_mix_f32 v121, v84, v33, v41 op_sel:[1,0,0] op_sel_hi:[1,0,0]\n"
    "v_fma_mix_f32 v122, v85, v34, v42 op_sel:[0,0,0] op_sel_hi:[1,0,0]\n"
    "v_fma_mix_f32 v123, v85, v35, v43 op_sel:[1,0,0] op_sel_hi:[1,0,0]\n"
    "v_fma_mix_f32 v124, v86, v36, v44 op_sel:[0,0,0] op_sel_hi:[1,0,0]\n"
    "v_fma_mix_f32 v125, v86, v37, v45 op_sel:[1,0,0] op_sel_hi:[1,0,0]\n"
    "v_fma_mix_f32 v126, v87, v38, v46 op_sel:[0,0,0] op_sel_hi:[1,0,0]\n"
    "v_fma_mix_f32 v127, v87, v39, v47 op_sel:[1,0,0] op_sel_hi:[1,0,0]\n"
    "v_cvt_pk_bf16_f32 v100, v120, v121\n"
    "v_cvt_pk_bf16_f32 v101, v122, v123\n"
    "v_cvt_pk_bf16_f32 v102, v124, v125\n"
    "v_cvt_pk_bf16_f32 v103, v126, v127\n"
    "v_fma_mix_f32 v112, v88, v48, v56 op_sel:[0,0,0] op_sel_hi:[1,0,0]\n"
    "v_fma_mix_f32 v113, v88, v49, v57 op_sel:[1,0,0] op_sel_hi:[1,0,0]\n"
    "v_fma_mix_f32 v114, v89, v50, v58 op_sel:[0,0,0] op_sel_hi:[1,0,0]\n"
    "v_fma_mix_f32 v115, v89, v51, v59 op_sel:[1,0,0] op_sel_hi:[1,0,0]\n"
    "v_fma_mix_f32 v116, v90, v52, v60 op_sel:[0,0,0] op_sel_hi:[1,0,0]\n"
    "v_fma_mix_f32 v117, v90, v53, v61 op_sel:[1,0,0] op_sel_hi:[1,0,0]\n"
    "v_fma_mix_f32 v118, v91, v54, v62 op_sel:[0,0,0] op_sel_hi:[1,0,0]\n"
    "v_fma_mix_f32 v119, v91, v55, v63 op_sel:[1,0,0] op_sel_hi:[1,0,0]\n"
    "v_cvt_pk_bf16_f32 v104, v112, v113\n"
    "v_cvt_pk_bf16_f32 v105, v114, v115\n"
    "v_cvt_pk_bf16_f32 v106, v116, v117\n"
    "v_cvt_pk_bf16_f32 v107, v118, v119\n"
    "v_fma_mix_f32 v120, v92, v48, v56 op_sel:[0,0,0] op_sel_hi:[1,0,0]\n"
    "v_fma_mix_f32 v121, v92, v49, v57 op_sel:[1,0,0] op_sel_hi:[1,0,0]\n"
    "v_fma_mix_f32 v122, v93, v50, v58 op_sel:[0,0,0] op_sel_hi:[1,0,0]\n"
    "v_fma_mix_f32 v123, v93, v51, v59 op_sel:[1,0,0] op_sel_hi:[1,0,0]\n"
    "v_fma_mix_f32 v124, v94, v52, v60 op_sel:[0,0,0] op_sel_hi:[1,0,0]\n"
    "v_fma_mix_f32 v125, v94, v53, v61 op_sel:[1,0,0] op_sel_hi:[1,0,0]\n"
    "v_fma_mix_f32 v126, v95, v54, v62 op_sel:[0,0,0] op_sel_hi:[1,0,0]\n"
    "v_fma_mix_f32 v127, v95, v55, v63 op_sel:[1,0,0] op_sel_hi:[1,0,0]\n"
    "v_cvt_pk_bf16_f32 v108, v120, v121\n"
    "v_cvt_pk_bf16_f32 v109, v122, v123\n"
    "v_cvt_pk_bf16_f32 v110, v124, v125\n"
    "v_cvt_pk_bf16_f32 v111, v126, v127\n"
    "s_waitcnt lgkmcnt(0)\n"
    "s_cbranch_vccz L_qa0_1_%=\n"
    "s_waitcnt vmcnt(8)\n"
    "s_barrier\n"
    "L_qa0_1_%=:\n"
    "v_add_u32 v15, 1024, v15\n"
    "v_mfma_f32_16x16x32_bf16 a[0:3], a[64:67], v[96:99], a[0:3]\n"
    "ds_read_b128 v[32:35], v15 offset:0\n"
    "v_mfma_f32_16x16x32_bf16 a[32:35], a[64:67], v[100:103], a[32:35]\n"
    "ds_read_b64_tr_b16 a[64:65], v13 offset:24576\n"
    "ds_read_b64_tr_b16 a[66:67], v13 offset:28672\n"
    "v_mfma_f32_16x16x32_bf16 a[4:7], a[68:71], v[96:99], a[4:7]\n"
    "s_add_u32 m0, s64, 114688\n"
    "s_mov_b32 s53, 3145728\n"
    "buffer_load_dwordx4 v16, s[24:27], s53 offen lds\n"
    "v_mfma_f32_16x16x32_bf16 a[36:39], a[68:71], v[100:103], a[36:39]\n"
    "ds_read_b64_tr_b16 a[68:69], v14 offset:24576\n"
    "ds_read_b64_tr_b16 a[70:71], v14 offset:28672\n"
    "v_mfma_f32_16x16x32_bf16 a[8:11], a[72:75], v[96:99], a[8:11]\n"
    "ds_read_b128 v[36:39], v15 offset:16\n"
    "v_mfma_f32_16x16x32_bf16 a[40:43], a[72:75], v[100:103], a[40:43]\n"
    "ds_read_b64_tr_b16 a[72:73], v13 offset:25088\n"
    "ds_read_b64_tr_b16 a[74:75], v13 offset:29184\n"
    "v_mfma_f32_16x16x32_bf16 a[12:15], a[76:79], v[96:99], a[12:15]\n"
    "s_add_u32 m0, s64, 115712\n"
    "s_mov_b32 s53, 3145856\n"
    "buffer_load_dwordx4 v16, s[24:27], s53 offen lds\n"
    "v_mfma_f32_16x16x32_bf16 a[44:47], a[76:79], v[100:103], a[44:47]\n"
    "ds_read_b64_tr_b16 a[76:77], v14 offset:25088\n"
    "ds_read_b64_tr_b16 a[78:79], v14 offset:29184\n"
    "v_mfma_f32_16x16x32_bf16 a[16:19], a[80:83], v[96:99], a[16:19]\n"
    "ds_read_b128 v[40:43], v15 offset:32\n"
    "v_mfma_f32_16x16x32_bf16 a[48:51], a[80:83], v[100:103], a[48:51]\n"
    "ds_read_b64_tr_b16 a[80:81], v13 offset:25600\n"
    "ds_read_b64_tr_b16 a[82:83], v13 offset:29696\n"
    "v_mfma_f32_16x16x32_bf16 a[20:23], a[84:87], v[96:99], a[20:23]\n"
    "s_add_u32 m0, s64, 131072\n"
    "s_mov_b32 s53, 3670016\n"
    "buffer_load_dwordx4 v16, s[24:27], s53 offen lds\n"
    "v_mfma_f32_16x16x32_bf16 a[52:55], a[84:87], v[100:103], a[52:55]\n"
    "ds_read_b64_tr_b16 a[84:85], v14 offset:25600\n"
    "ds_read_b64_tr_b16 a[86:87], v14 offset:29696\n"
    "v_mfma_f32_16x16x32_bf16 a[24:27], a[88:91], v[96:99], a[24:27]\n"
    "ds_read_b128 v[44:47], v15 offset:48\n"
    "v_mfma_f32_16x16x32_bf16 a[56:59], a[88:91], v[100:103], a[56:59]\n"
    "ds_read_b64_tr_b16 a[88:89], v13 offset:26112\n"
    "ds_read_b64_tr_b16 a[90:91], v13 offset:30208\n"
    "v_mfma_f32_16x16x32_bf16 a[28:31], a[92:95], v[96:99], a[28:31]\n"
    "s_add_u32 m0, s64, 132096\n"
    "s_mov_b32 s53, 3670144\n"
    "buffer_load_dwordx4 v16, s[24:27], s53 offen lds\n"
    "v_mfma_f32_16x16x32_bf16 a[60:63], a[92:95], v[100:103], a[60:63]\n"
    "ds_read_b64_tr_b16 a[92:93], v14 offset:26112\n"
    "ds_read_b64_tr_b16 a[94:95], v14 offset:30208\n"
    "s_waitcnt lgkmcnt(10)\n"
    "v_mfma_f32_16x16x32_bf16 a[0:3], a[64:67], v[104:107], a[0:3]\n"
    "ds_read_b128 v[48:51], v15 offset:256\n"
    "v_mfma_f32_16x16x32_bf16 a[32:35], a[64:67], v[108:111], a[32:35]\n"
    "ds_read_b64_tr_b16 a[64:65], v13 offset:32768\n"
    "ds_read_b64_tr_b16 a[66:67], v13 offset:36864\n"
    "v_mfma_f32_16x16x32_bf16 a[4:7], a[68:71], v[104:107], a[4:7]\n"
    "buffer_load_dwordx4 v[80:83], v2, s[20:23], s62 offen offset:2048 sc1\n"
    "v_mfma_f32_16x16x32_bf16 a[36:39], a[68:71], v[108:111], a[36:39]\n"
    "ds_read_b64_tr_b16 a[68:69], v14 offset:32768\n"
    "ds_read_b64_tr_b16 a[70:71], v14 offset:36864\n"
    "v_mfma_f32_16x16x32_bf16 a[8:11], a[72:75], v[104:107], a[8:11]\n"
    "ds_read_b128 v[52:55], v15 offset:272\n"
    "v_mfma_f32_16x16x32_bf16 a[40:43], a[72:75], v[108:111], a[40:43]\n"
    "ds_read_b64_tr_b16 a[72:73], v13 offset:33280\n"
    "ds_read_b64_tr_b16 a[74:75], v13 offset:37376\n"
    "v_mfma_f32_16x16x32_bf16 a[12:15], a[76:79], v[104:107], a[12:15]\n"
    "buffer_load_dwordx4 v[84:87], v2, s[20:23], s59 offen offset:2048 sc1\n"
    "v_mfma_f32_16x16x32_bf16 a[44:47], a[76:79], v[108:111], a[44:47]\n"
    "ds_read_b64_tr_b16 a[76:77], v14 offset:33280\n"
    "ds_read_b64_tr_b16 a[78:79], v14 offset:37376\n"
    "s_waitcnt lgkmcnt(10)\n"
    "v_mfma_f32_16x16x32_bf16 a[16:19], a[80:83], v[104:107], a[16:19]\n"
    "ds_read_b128 v[56:59], v15 offset:288\n"
    "v_mfma_f32_16x16x32_bf16 a[48:51], a[80:83], v[108:111], a[48:51]\n"
    "ds_read_b64_tr_b16 a[80:81], v13 offset:33792\n"
    "ds_read_b64_tr_b16 a[82:83], v13 offset:37888\n"
    "v_mfma_f32_16x16x32_bf16 a[20:23], a[84:87], v[104:107], a[20:23]\n"
    "buffer_load_dwordx4 v[88:91], v2, s[20:23], s62 offen offset:3072 sc1\n"
    "v_mfma_f32_16x16x32_bf16 a[52:55], a[84:87], v[108:111], a[52:55]\n"
    "ds_read_b64_tr_b16 a[84:85], v14 offset:33792\n"
    "ds_read_b64_tr_b16 a[86:87], v14 offset:37888\n"
    "v_mfma_f32_16x16x32_bf16 a[24:27], a[88:91], v[104:107], a[24:27]\n"
    "ds_read_b128 v[60:63], v15 offset:304\n"
    "v_mfma_f32_16x16x32_bf16 a[56:59], a[88:91], v[108:111], a[56:59]\n"
    "ds_read_b64_tr_b16 a[88:89], v13 offset:34304\n"
    "ds_read_b64_tr_b16 a[90:91], v13 offset:38400\n"
    "s_waitcnt vmcnt(15)\n"
    "s_cbranch_vccnz L_qb0_1_%=\n"
    "s_barrier\n"
    "L_qb0_1_%=:\n"
    "v_mfma_f32_16x16x32_bf16 a[28:31], a[92:95], v[104:107], a[28:31]\n"
    "buffer_load_dwordx4 v[92:95], v2, s[20:23], s59 offen offset:3072 sc1\n"
    "v_mfma_f32_16x16x32_bf16 a[60:63], a[92:95], v[108:111], a[60:63]\n"
    "ds_read_b64_tr_b16 a[92:93], v14 offset:34304\n"
    "ds_read_b64_tr_b16 a[94:95], v14 offset:38400\n"
    "s_cmp_eq_u32 s34, 0\n"
    "s_cbranch_scc0 L_fl1_%=\n"
    "s_lshl_b32 s52, s36, 5\n"
    "s_lshl_b32 s53, s35, 2\n"
    "s_add_u32 s52, s52, s53\n"
    "s_add_u32 s52, s52, 49152\n"
    "v_mov_b32 v20, s52\n"
    "v_mov_b32 v21, 1\n"
    "global_store_dword v20, v21, s[96:97] sc1\n"
    "v_add_u32 v22, 51200, v2\n"
    "global_load_dwordx4 v[24:27], v22, s[96:97] sc1\n"
    "L_fl1_%=:\n"
    "s_waitcnt vmcnt(8)\n"
    "s_waitcnt lgkmcnt(4)\n"
    "v_fma_mix_f32 v112, v64, v32, v40 op_sel:[0,0,0] op_sel_hi:[1,0,0]\n"
    "v_fma_mix_f32 v113, v64, v33, v41 op_sel:[1,0,0] op_sel_hi:[1,0,0]\n"
    "v_fma_mix_f32 v114, v65, v34, v42 op_sel:[0,0,0] op_sel_hi:[1,0,0]\n"
    "v_fma_mix_f32 v115, v65, v35, v43 op_sel:[1,0,0] op_sel_hi:[1,0,0]\n"
    "v_fma_mix_f32 v116, v66, v36, v44 op_sel:[0,0,0] op_sel_hi:[1,0,0]\n"
    "v_fma_mix_f32 v117, v66, v37, v45 op_sel:[1,0,0] op_sel_hi:[1,0,0]\n"
    "v_fma_mix_f32 v118, v67, v38, v46 op_sel:[0,0,0] op_sel_hi:[1,0,0]\n"
    "v_fma_mix_f32 v119, v67, v39, v47 op_sel:[1,0,0] op_sel_hi:[1,0,0]\n"
    "v_cvt_pk_bf16_f32 v96, v112, v113\n"
    "v_cvt_pk_bf16_f32 v97, v114, v115\n"
    "v_cvt_pk_bf16_f32 v98, v116, v117\n"
    "v_cvt_pk_bf16_f32 v99, v118, v119\n"
    "v_fma_mix_f32 v120, v68, v32, v40 op_sel:[0,0,0] op_sel_hi:[1,0,0]\n"
    "v_fma_mix_f32 v121, v68, v33, v41 op_sel:[1,0,0] op_sel_hi:[1,0,0]\n"
    "v_fma_mix_f32 v122, v69, v34, v42 op_sel:[0,0,0] op_sel_hi:[1,0,0]\n"
    "v_fma_mix_f32 v123, v69, v35, v43 op_sel:[1,0,0] op_sel_hi:[1,0,0]\n"
    "v_fma_mix_f32 v124, v70, v36, v44 op_sel:[0,0,0] op_sel_hi:[1,0,0]\n"
    "v_fma_mix_f32 v125, v70, v37, v45 op_sel:[1,0,0] op_sel_hi:[1,0,0]\n"
    "v_fma_mix_f32 v126, v71, v38, v46 op_sel:[0,0,0] op_sel_hi:[1,0,0]\n"
    "v_fma_mix_f32 v127, v71, v39, v47 op_sel:[1,0,0] op_sel_hi:[1,0,0]\n"
    "v_cvt_pk_bf16_f32 v100, v120, v121\n"
    "v_cvt_pk_bf16_f32 v101, v122, v123\n"
    "v_cvt_pk_bf16_f32 v102, v124, v125\n"
    "v_cvt_pk_bf16_f32 v103, v126, v127\n"
    "v_fma_mix_f32 v112, v72, v48, v56 op_sel:[0,0,0] op_sel_hi:[1,0,0]\n"
    "v_fma_mix_f32 v113, v72, v49, v57 op_sel:[1,0,0] op_sel_hi:[1,0,0]\n"
    "v_fma_mix_f32 v114, v73, v50, v58 op_sel:[0,0,0] op_sel_hi:[1,0,0]\n"
    "v_fma_mix_f32 v115, v73, v51, v59 op_sel:[1,0,0] op_sel_hi:[1,0,0]\n"
    "v_fma_mix_f32 v116, v74, v52, v60 op_sel:[0,0,0] op_sel_hi:[1,0,0]\n"
    "v_fma_mix_f32 v117, v74, v53, v61 op_sel:[1,0,0] op_sel_hi:[1,0,0]\n"
    "v_fma_mix_f32 v118, v75, v54, v62 op_sel:[0,0,0] op_sel_hi:[1,0,0]\n"
    "v_fma_mix_f32 v119, v75, v55, v63 op_sel:[1,0,0] op_sel_hi:[1,0,0]\n"
    "v_cvt_pk_bf16_f32 v104, v112, v113\n"
    "v_cvt_pk_bf16_f32 v105, v114, v115\n"
    "v_cvt_pk_bf16_f32 v106, v116, v117\n"
    "v_cvt_pk_bf16_f32 v107, v118, v119\n"
    "v_fma_mix_f32 v120, v76, v48, v56 op_sel:[0,0,0] op_sel_hi:[1,0,0]\n"
    "v_fma_mix_f32 v121, v76, v49, v57 op_sel:[1,0,0] op_sel_hi:[1,0,0]\n"
    "v_fma_mix_f32 v122, v77, v50, v58 op_sel:[0,0,0] op_sel_hi:[1,0,0]\n"
    "v_fma_mix_f32 v123, v77, v51, v59 op_sel:[1,0,0] op_sel_hi:[1,0,0]\n"
    "v_fma_mix_f32 v124, v78, v52, v60 op_sel:[0,0,0] op_sel_hi:[1,0,0]\n"
    "v_fma_mix_f32 v125, v78, v53, v61 op_sel:[1,0,0] op_sel_hi:[1,0,0]\n"
    "v_fma_mix_f32 v126, v79, v54, v62 op_sel:[0,0,0] op_sel_hi:[1,0,0]\n"
    "v_fma_mix_f32 v127, v79, v55, v63 op_sel:[1,0,0] op_sel_hi:[1,0,0]\n"
    "v_cvt_pk_bf16_f32 v108, v120, v121\n"
    "v_cvt_pk_bf16_f32 v109, v122, v123\n"
    "v_cvt_pk_bf16_f32 v110, v124, v125\n"
    "v_cvt_pk_bf16_f32 v111, v126, v127\n"
    "v_add_u32 v13, 0x8000, v13\n"
    "v_add_u32 v14, 0x8000, v14\n"
    "s_waitcnt lgkmcnt(0)\n"
    "s_cbranch_vccz L_qa1_0_%=\n"
    "s_barrier\n"
    "L_qa1_0_%=:\n"
    "v_mfma_f32_16x16x32_bf16 a[0:3], a[64:67], v[96:99], a[0:3]\n"
    "ds_read_b128 v[32:35], v15 offset:512\n"
    "v_mfma_f32_16x16x32_bf16 a[32:35], a[64:67], v[100:103], a[32:35]\n"
    "ds_read_b64_tr_b16 a[64:65], v13 offset:8192\n"
    "ds_read_b64_tr_b16 a[66:67], v13 offset:12288\n"
    "v_mfma_f32_16x16x32_bf16 a[4:7], a[68:71], v[96:99], a[4:7]\n"
    "s_add_u32 s62, s62, 4096\n"
    "v_mfma_f32_16x16x32_bf16 a[36:39], a[68:71], v[100:103], a[36:39]\n"
    "ds_read_b64_tr_b16 a[68:69], v14 offset:8192\n"
    "ds_read_b64_tr_b16 a[70:71], v14 offset:12288\n"
    "v_mfma_f32_16x16x32_bf16 a[8:11], a[72:75], v[96:99], a[8:11]\n"
    "ds_read_b128 v[36:39], v15 offset:528\n"
    "v_mfma_f32_16x16x32_bf16 a[40:43], a[72:75], v[100:103], a[40:43]\n"
    "ds_read_b64_tr_b16 a[72:73], v13 offset:8704\n"
    "ds_read_b64_tr_b16 a[74:75], v13 offset:12800\n"
    "v_mfma_f32_16x16x32_bf16 a[12:15], a[76:79], v[96:99], a[12:15]\n"
    "s_add_u32 s59, s59, 4096\n"
    "v_mfma_f32_16x16x32_bf16 a[44:47], a[76:79], v[100:103], a[44:47]\n"
    "ds_read_b64_tr_b16 a[76:77], v14 offset:8704\n"
    "ds_read_b64_tr_b16 a[78:79], v14 offset:12800\n"
    "v_mfma_f32_16x16x32_bf16 a[16:19], a[80:83], v[96:99], a[16:19]\n"
    "ds_read_b128 v[40:43], v15 offset:544\n"
    "v_mfma_f32_16x16x32_bf16 a[48:51], a[80:83], v[100:103], a[48:51]\n"
    "ds_read_b64_tr_b16 a[80:81], v13 offset:9216\n"
    "ds_read_b64_tr_b16 a[82:83], v13 offset:13312\n"
    "v_mfma_f32_16x16x32_bf16 a[20:23], a[84:87], v[96:99], a[20:23]\n"
    "buffer_load_dwordx4 v[64:67], v2, s[20:23], s62 offen offset:0 sc1\n"
    "v_mfma_f32_16x16x32_bf16 a[52:55], a[84:87], v[100:103], a[52:55]\n"
    "ds_read_b64_tr_b16 a[84:85], v14 offset:9216\n"
    "ds_read_b64_tr_b16 a[86:87], v14 offset:13312\n"
    "v_mfma_f32_16x16x32_bf16 a[24:27], a[88:91], v[96:99], a[24:27]\n"
    "ds_read_b128 v[44:47], v15 offset:560\n"
    "v_mfma_f32_16x16x32_bf16 a[56:59], a[88:91], v[100:103], a[56:59]\n"
    "ds_read_b64_tr_b16 a[88:89], v13 offset:9728\n"
    "ds_read_b64_tr_b16 a[90:91], v13 offset:13824\n"
    "v_mfma_f32_16x16x32_bf16 a[28:31], a[92:95], v[96:99], a[28:31]\n"
    "buffer_load_dwordx4 v[68:71], v2, s[20:23], s59 offen offset:0 sc1\n"
    "v_mfma_f32_16x16x32_bf16 a[60:63], a[92:95], v[100:103], a[60:63]\n"
    "ds_read_b64_tr_b16 a[92:93], v14 offset:9728\n"
    "ds_read_b64_tr_b16 a[94:95], v14 offset:13824\n"
    "s_waitcnt lgkmcnt(10)\n"
    "v_mfma_f32_16x16x32_bf16 a[0:3], a[64:67], v[104:107], a[0:3]\n"
    "ds_read_b128 v[48:51], v15 offset:768\n"
    "v_mfma_f32_16x16x32_bf16 a[32:35], a[64:67], v[108:111], a[32:35]\n"
    "ds_read_b64_tr_b16 a[64:65], v13 offset:16384\n"
    "ds_read_b64_tr_b16 a[66:67], v13 offset:20480\n"
    "v_mfma_f32_16x16x32_bf16 a[4:7], a[68:71], v[104:107], a[4:7]\n"
    "buffer_load_dwordx4 v[72:75], v2, s[20:23], s62 offen offset:1024 sc1\n"
    "v_mfma_f32_16x16x32_bf16 a[36:39], a[68:71], v[108:111], a[36:39]\n"
    "ds_read_b64_tr_b16 a[68:69], v14 offset:16384\n"
    "ds_read_b64_tr_b16 a[70:71], v14 offset:20480\n"
    "v_mfma_f32_16x16x32_bf16 a[8:11], a[72:75], v[104:107], a[8:11]\n"
    "ds_read_b128 v[52:55], v15 offset:784\n"
    "v_mfma_f32_16x16x32_bf16 a[40:43], a[72:75], v[108:111], a[40:43]\n"
    "ds_read_b64_tr_b16 a[72:73], v13 offset:16896\n"
    "ds_read_b64_tr_b16 a[74:75], v13 offset:20992\n"
    "v_mfma_f32_16x16x32_bf16 a[12:15], a[76:79], v[104:107], a[12:15]\n"
    "buffer_load_dwordx4 v[76:79], v2, s[20:23], s59 offen offset:1024 sc1\n"
    "v_mfma_f32_16x16x32_bf16 a[44:47], a[76:79], v[108:111], a[44:47]\n"
    "ds_read_b64_tr_b16 a[76:77], v14 offset:16896\n"
    "ds_read_b64_tr_b16 a[78:79], v14 offset:20992\n"
    "s_waitcnt lgkmcnt(10)\n"
    "v_mfma_f32_16x16x32_bf16 a[16:19], a[80:83], v[104:107], a[16:19]\n"
    "ds_read_b128 v[56:59], v15 offset:800\n"
    "v_mfma_f32_16x16x32_bf16 a[48:51], a[80:83], v[108:111], a[48:51]\n"
    "ds_read_b64_tr_b16 a[80:81], v13 offset:17408\n"
    "ds_read_b64_tr_b16 a[82:83], v13 offset:21504\n"
    "v_mfma_f32_16x16x32_bf16 a[20:23], a[84:87], v[104:107], a[20:23]\n"
    "v_mfma_f32_16x16x32_bf16 a[52:55], a[84:87], v[108:111], a[52:55]\n"
    "ds_read_b64_tr_b16 a[84:85], v14 offset:17408\n"
    "ds_read_b64_tr_b16 a[86:87], v14 offset:21504\n"
    "v_mfma_f32_16x16x32_bf16 a[24:27], a[88:91], v[104:107], a[24:27]\n"
    "ds_read_b128 v[60:63], v15 offset:816\n"
    "v_mfma_f32_16x16x32_bf16 a[56:59], a[88:91], v[108:111], a[56:59]\n"
    "ds_read_b64_tr_b16 a[88:89], v13 offset:17920\n"
    "ds_read_b64_tr_b16 a[90:91], v13 offset:22016\n"
    "s_cbranch_vccnz L_qb1_0_%=\n"
    "s_barrier\n"
    "L_qb1_0_%=:\n"
    "v_mfma_f32_16x16x32_bf16 a[28:31], a[92:95], v[104:107], a[28:31]\n"
    "v_mfma_f32_16x16x32_bf16 a[60:63], a[92:95], v[108:111], a[60:63]\n"
    "ds_read_b64_tr_b16 a[92:93], v14 offset:17920\n"
    "ds_read_b64_tr_b16 a[94:95], v14 offset:22016\n"
    "s_cmp_eq_u32 s34, 0\n"
    "s_cbranch_scc0 L_fl2_%=\n"
    "s_waitcnt vmcnt(4)\n"
    "s_lshl_b32 s52, %1, 10\n"
    "s_add_u32 s52, s52, 393216\n"
    "v_add_u32 v22, s52, v2\n"
    "global_store_dwordx4 v22, v[24:27], s[96:97] sc1\n"
    "s_lshl_b32 s52, s36, 5\n"
    "s_add_u32 s52, s52, 49152\n"
    "v_bfe_u32 v22, v2, 2, 5\n"
    "v_add_u32 v22, s52, v22\n"
    "global_load_dword v23, v22, s[96:97] sc1\n"
    "L_fl2_%=:\n"
    "s_waitcnt vmcnt(4)\n"
    "s_waitcnt lgkmcnt(4)\n"
    "v_fma_mix_f32 v112, v80, v32, v40 op_sel:[0,0,0] op_sel_hi:[1,0,0]\n"
    "v_fma_mix_f32 v113, v80, v33, v41 op_sel:[1,0,0] op_sel_hi:[1,0,0]\n"
    "v_fma_mix_f32 v114, v81, v34, v42 op_sel:[0,0,0] op_sel_hi:[1,0,0]\n"
    "v_fma_mix_f32 v115, v81, v35, v43 op_sel:[1,0,0] op_sel_hi:[1,0,0]\n"
    "v_fma_mix_f32 v116, v82, v36, v44 op_sel:[0,0,0] op_sel_hi:[1,0,0]\n"
    "v_fma_mix_f32 v117, v82, v37, v45 op_sel:[1,0,0] op_sel_hi:[1,0,0]\n"
    "v_fma_mix_f32 v118, v83, v38, v46 op_sel:[0,0,0] op_sel_hi:[1,0,0]\n"
    "v_fma_mix_f32 v119, v83, v39, v47 op_sel:[1,0,0] op_sel_hi:[1,0,0]\n"
    "v_cvt_pk_bf16_f32 v96, v112, v113\n"
    "v_cvt_pk_bf16_f32 v97, v114, v115\n"
    "v_cvt_pk_bf16_f32 v98, v116, v117\n"
    "v_cvt_pk_bf16_f32 v99, v118, v119\n"
    "v_fma_mix_f32 v120, v84, v32, v40 op_sel:[0,0,0] op_sel_hi:[1,0,0]\n"
    "v_fma_mix_f32 v121, v84, v33, v41 op_sel:[1,0,0] op_sel_hi:[1,0,0]\n"
    "v_fma_mix_f32 v122, v85, v34, v42 op_sel:[0,0,0] op_sel_hi:[1,0,0]\n"
    "v_fma_mix_f32 v123, v85, v35, v43 op_sel:[1,0,0] op_sel_hi:[1,0,0]\n"
    "v_fma_mix_f32 v124, v86, v36, v44 op_sel:[0,0,0] op_sel_hi:[1,0,0]\n"
    "v_fma_mix_f32 v125, v86, v37, v45 op_sel:[1,0,0] op_sel_hi:[1,0,0]\n"
    "v_fma_mix_f32 v126, v87, v38, v46 op_sel:[0,0,0] op_sel_hi:[1,0,0]\n"
    "v_fma_mix_f32 v127, v87, v39, v47 op_sel:[1,0,0] op_sel_hi:[1,0,0]\n"
    "v_cvt_pk_bf16_f32 v100, v120, v121\n"
    "v_cvt_pk_bf16_f32 v101, v122, v123\n"
    "v_cvt_pk_bf16_f32 v102, v124, v125\n"
    "v_cvt_pk_bf16_f32 v103, v126, v127\n"
    "v_fma_mix_f32 v112, v88, v48, v56 op_sel:[0,0,0] op_sel_hi:[1,0,0]\n"
    "v_fma_mix_f32 v113, v88, v49, v57 op_sel:[1,0,0] op_sel_hi:[1,0,0]\n"
    "v_fma_mix_f32 v114, v89, v50, v58 op_sel:[0,0,0] op_sel_hi:[1,0,0]\n"
    "v_fma_mix_f32 v115, v89, v51, v59 op_sel:[1,0,0] op_sel_hi:[1,0,0]\n"
    "v_fma_mix_f32 v116, v90, v52, v60 op_sel:[0,0,0] op_sel_hi:[1,0,0]\n"
    "v_fma_mix_f32 v117, v90, v53, v61 op_sel:[1,0,0] op_sel_hi:[1,0,0]\n"
    "v_fma_mix_f32 v118, v91, v54, v62 op_sel:[0,0,0] op_sel_hi:[1,0,0]\n"
    "v_fma_mix_f32 v119, v91, v55, v63 op_sel:[1,0,0] op_sel_hi:[1,0,0]\n"
    "v_cvt_pk_bf16_f32 v104, v112, v113\n"
    "v_cvt_pk_bf16_f32 v105, v114, v115\n"
    "v_cvt_pk_bf16_f32 v106, v116, v117\n"
    "v_cvt_pk_bf16_f32 v107, v118, v119\n"
    "v_fma_mix_f32 v120, v92, v48, v56 op_sel:[0,0,0] op_sel_hi:[1,0,0]\n"
    "v_fma_mix_f32 v121, v92, v49, v57 op_sel:[1,0,0] op_sel_hi:[1,0,0]\n"
    "v_fma_mix_f32 v122, v93, v50, v58 op_sel:[0,0,0] op_sel_hi:[1,0,0]\n"
    "v_fma_mix_f32 v123, v93, v51, v59 op_sel:[1,0,0] op_sel_hi:[1,0,0]\n"
    "v_fma_mix_f32 v124, v94, v52, v60 op_sel:[0,0,0] op_sel_hi:[1,0,0]\n"
    "v_fma_mix_f32 v125, v94, v53, v61 op_sel:[1,0,0] op_sel_hi:[1,0,0]\n"
    "v_fma_mix_f32 v126, v95, v54, v62 op_sel:[0,0,0] op_sel_hi:[1,0,0]\n"
    "v_fma_mix_f32 v127, v95, v55, v63 op_sel:[1,0,0] op_sel_hi:[1,0,0]\n"
    "v_cvt_pk_bf16_f32 v108, v120, v121\n"
    "v_cvt_pk_bf16_f32 v109, v122, v123\n"
    "v_cvt_pk_bf16_f32 v110, v124, v125\n"
    "v_cvt_pk_bf16_f32 v111, v126, v127\n"
    "s_waitcnt lgkmcnt(0)\n"
    "s_cbranch_vccz L_qa1_1_%=\n"
    "s_barrier\n"
    "L_qa1_1_%=:\n"
    "v_add_u32 v15, 1024, v15\n"
    "v_mfma_f32_16x16x32_bf16 a[0:3], a[64:67], v[96:99], a[0:3]\n"
    "ds_read_b128 v[32:35], v15 offset:0\n"
    "v_mfma_f32_16x16x32_bf16 a[32:35], a[64:67], v[100:103], a[32:35]\n"
    "ds_read_b64_tr_b16 a[64:65], v13 offset:24576\n"
    "ds_read_b64_tr_b16 a[66:67], v13 offset:28672\n"
    "v_mfma_f32_16x16x32_bf16 a[4:7], a[68:71], v[96:99], a[4:7]\n"
    "buffer_load_dwordx4 v[80:83], v2, s[20:23], s62 offen offset:2048 sc1\n"
    "v_mfma_f32_16x16x32_bf16 a[36:39], a[68:71], v[100:103], a[36:39]\n"
    "ds_read_b64_tr_b16 a[68:69], v14 offset:24576\n"
    "ds_read_b64_tr_b16 a[70:71], v14 offset:28672\n"
    "v_mfma_f32_16x16x32_bf16 a[8:11], a[72:75], v[96:99], a[8:11]\n"
    "ds_read_b128 v[36:39], v15 offset:16\n"
    "v_mfma_f32_16x16x32_bf16 a[40:43], a[72:75], v[100:103], a[40:43]\n"
    "ds_read_b64_tr_b16 a[72:73], v13 offset:25088\n"
    "ds_read_b64_tr_b16 a[74:75], v13 offset:29184\n"
    "v_mfma_f32_16x16x32_bf16 a[12:15], a[76:79], v[96:99], a[12:15]\n"
    "buffer_load_dwordx4 v[84:87], v2, s[20:23], s59 offen offset:2048 sc1\n"
    "v_mfma_f32_16x16x32_bf16 a[44:47], a[76:79], v[100:103], a[44:47]\n"
    "ds_read_b64_tr_b16 a[76:77], v14 offset:25088\n"
    "ds_read_b64_tr_b16 a[78:79], v14 offset:29184\n"
    "v_mfma_f32_16x16x32_bf16 a[16:19], a[80:83], v[96:99], a[16:19]\n"
    "ds_read_b128 v[40:43], v15 offset:32\n"
    "v_mfma_f32_16x16x32_bf16 a[48:51], a[80:83], v[100:103], a[48:51]\n"
    "ds_read_b64_tr_b16 a[80:81], v13 offset:25600\n"
    "ds_read_b64_tr_b16 a[82:83], v13 offset:29696\n"
    "v_mfma_f32_16x16x32_bf16 a[20:23], a[84:87], v[96:99], a[20:23]\n"
    "buffer_load_dwordx4 v[88:91], v2, s[20:23], s62 offen offset:3072 sc1\n"
    "v_mfma_f32_16x16x32_bf16 a[52:55], a[84:87], v[100:103], a[52:55]\n"
    "ds_read_b64_tr_b16 a[84:85], v14 offset:25600\n"
    "ds_read_b64_tr_b16 a[86:87], v14 offset:29696\n"
    "v_mfma_f32_16x16x32_bf16 a[24:27], a[88:91], v[96:99], a[24:27]\n"
    "ds_read_b128 v[44:47], v15 offset:48\n"
    "v_mfma_f32_16x16x32_bf16 a[56:59], a[88:91], v[100:103], a[56:59]\n"
    "ds_read_b64_tr_b16 a[88:89], v13 offset:26112\n"
    "ds_read_b64_tr_b16 a[90:91], v13 offset:30208\n"
    "v_mfma_f32_16x16x32_bf16 a[28:31], a[92:95], v[96:99], a[28:31]\n"
    "buffer_load_dwordx4 v[92:95], v2, s[20:23], s59 offen offset:3072 sc1\n"
    "v_mfma_f32_16x16x32_bf16 a[60:63], a[92:95], v[100:103], a[60:63]\n"
    "ds_read_b64_tr_b16 a[92:93], v14 offset:26112\n"
    "ds_read_b64_tr_b16 a[94:95], v14 offset:30208\n"
    "s_waitcnt lgkmcnt(10)\n"
    "v_mfma_f32_16x16x32_bf16 a[0:3], a[64:67], v[104:107], a[0:3]\n"
    "ds_read_b128 v[48:51], v15 offset:256\n"
    "v_mfma_f32_16x16x32_bf16 a[32:35], a[64:67], v[108:111], a[32:35]\n"
    "ds_read_b64_tr_b16 a[64:65], v13 offset:32768\n"
    "ds_read_b64_tr_b16 a[66:67], v13 offset:36864\n"
    "v_mfma_f32_16x16x32_bf16 a[4:7], a[68:71], v[104:107], a[4:7]\n"
    "v_mfma_f32_16x16x32_bf16 a[36:39], a[68:71], v[108:111], a[36:39]\n"
    "ds_read_b64_tr_b16 a[68:69], v14 offset:32768\n"
    "ds_read_b64_tr_b16 a[70:71], v14 offset:36864\n"
    "v_mfma_f32_16x16x32_bf16 a[8:11], a[72:75], v[104:107], a[8:11]\n"
    "ds_read_b128 v[52:55], v15 offset:272\n"
    "v_mfma_f32_16x16x32_bf16 a[40:43], a[72:75], v[108:111], a[40:43]\n"
    "ds_read_b64_tr_b16 a[72:73], v13 offset:33280\n"
    "ds_read_b64_tr_b16 a[74:75], v13 offset:37376\n"
    "v_mfma_f32_16x16x32_bf16 a[12:15], a[76:79], v[104:107], a[12:15]\n"
    "v_mfma_f32_16x16x32_bf16 a[44:47], a[76:79], v[108:111], a[44:47]\n"
    "ds_read_b64_tr_b16 a[76:77], v14 offset:33280\n"
    "ds_read_b64_tr_b16 a[78:79], v14 offset:37376\n"
    "s_waitcnt lgkmcnt(10)\n"
    "v_mfma_f32_16x16x32_bf16 a[16:19], a[80:83], v[104:107], a[16:19]\n"
    "ds_read_b128 v[56:59], v15 offset:288\n"
    "v_mfma_f32_16x16x32_bf16 a[48:51], a[80:83], v[108:111], a[48:51]\n"
    "ds_read_b64_tr_b16 a[80:81], v13 offset:33792\n"
    "ds_read_b64_tr_b16 a[82:83], v13 offset:37888\n"
    "v_mfma_f32_16x16x32_bf16 a[20:23], a[84:87], v[104:107], a[20:23]\n"
    "v_mfma_f32_16x16x32_bf16 a[52:55], a[84:87], v[108:111], a[52:55]\n"
    "ds_read_b64_tr_b16 a[84:85], v14 offset:33792\n"
    "ds_read_b64_tr_b16 a[86:87], v14 offset:37888\n"
    "v_mfma_f32_16x16x32_bf16 a[24:27], a[88:91], v[104:107], a[24:27]\n"
    "ds_read_b128 v[60:63], v15 offset:304\n"
    "v_mfma_f32_16x16x32_bf16 a[56:59], a[88:91], v[108:111], a[56:59]\n"
    "ds_read_b64_tr_b16 a[88:89], v13 offset:34304\n"
    "ds_read_b64_tr_b16 a[90:91], v13 offset:38400\n"
    "s_cbranch_vccnz L_qb1_1_%=\n"
    "s_barrier\n"
    "L_qb1_1_%=:\n"
    "v_mfma_f32_16x16x32_bf16 a[28:31], a[92:95], v[104:107], a[28:31]\n"
    "v_mfma_f32_16x16x32_bf16 a[60:63], a[92:95], v[108:111], a[60:63]\n"
    "ds_read_b64_tr_b16 a[92:93], v14 offset:34304\n"
    "ds_read_b64_tr_b16 a[94:95], v14 offset:38400\n"
    "s_cmp_eq_u32 s34, 0\n"
    "s_cbranch_scc0 L_fl3_%=\n"
    "s_mov_b32 s55, 400000\n"
    "s_waitcnt vmcnt(4)\n"
    "L_poll_%=:\n"
    "v_cmp_ne_u32 s[56:57], 1, v23\n"
    "s_sub_u32 s55, s55, 1\n"
    "s_cmp_eq_u64 s[56:57], 0\n"
    "s_cbranch_scc1 L_fl3_%=\n"
    "global_load_dword v23, v22, s[96:97] sc1\n"
    "s_waitcnt vmcnt(0)\n"
    "s_cmp_lg_u32 s55, 0\n"
    "s_cbranch_scc1 L_poll_%=\n"
    "L_fl3_%=:\n"
    "s_waitcnt vmcnt(4)\n"
    "s_waitcnt lgkmcnt(4)\n"
    "v_fma_mix_f32 v112, v64, v32, v40 op_sel:[0,0,0] op_sel_hi:[1,0,0]\n"
    "v_fma_mix_f32 v113, v64, v33, v41 op_sel:[1,0,0] op_sel_hi:[1,0,0]\n"
    "v_fma_mix_f32 v114, v65, v34, v42 op_sel:[0,0,0] op_sel_hi:[1,0,0]\n"
    "v_fma_mix_f32 v115, v65, v35, v43 op_sel:[1,0,0] op_sel_hi:[1,0,0]\n"
    "v_fma_mix_f32 v116, v66, v36, v44 op_sel:[0,0,0] op_sel_hi:[1,0,0]\n"
    "v_fma_mix_f32 v117, v66, v37, v45 op_sel:[1,0,0] op_sel_hi:[1,0,0]\n"
    "v_fma_mix_f32 v118, v67, v38, v46 op_sel:[0,0,0] op_sel_hi:[1,0,0]\n"
    "v_fma_mix_f32 v119, v67, v39, v47 op_sel:[1,0,0] op_sel_hi:[1,0,0]\n"
    "v_cvt_pk_bf16_f32 v96, v112, v113\n"
    "v_cvt_pk_bf16_f32 v97, v114, v115\n"
    "v_cvt_pk_bf16_f32 v98, v116, v117\n"
    "v_cvt_pk_bf16_f32 v99, v118, v119\n"
    "v_fma_mix_f32 v120, v68, v32, v40 op_sel:[0,0,0] op_sel_hi:[1,0,0]\n"
    "v_fma_mix_f32 v121, v68, v33, v41 op_sel:[1,0,0] op_sel_hi:[1,0,0]\n"
    "v_fma_mix_f32 v122, v69, v34, v42 op_sel:[0,0,0] op_sel_hi:[1,0,0]\n"
    "v_fma_mix_f32 v123, v69, v35, v43 op_sel:[1,0,0] op_sel_hi:[1,0,0]\n"
    "v_fma_mix_f32 v124, v70, v36, v44 op_sel:[0,0,0] op_sel_hi:[1,0,0]\n"
    "v_fma_mix_f32 v125, v70, v37, v45 op_sel:[1,0,0] op_sel_hi:[1,0,0]\n"
    "v_fma_mix_f32 v126, v71, v38, v46 op_sel:[0,0,0] op_sel_hi:[1,0,0]\n"
    "v_fma_mix_f32 v127, v71, v39, v47 op_sel:[1,0,0] op_sel_hi:[1,0,0]\n"
    "v_cvt_pk_bf16_f32 v100, v120, v121\n"
    "v_cvt_pk_bf16_f32 v101, v122, v123\n"
    "v_cvt_pk_bf16_f32 v102, v124, v125\n"
    "v_cvt_pk_bf16_f32 v103, v126, v127\n"
    "v_fma_mix_f32 v112, v72, v48, v56 op_sel:[0,0,0] op_sel_hi:[1,0,0]\n"
    "v_fma_mix_f32 v113, v72, v49, v57 op_sel:[1,0,0] op_sel_hi:[1,0,0]\n"
    "v_fma_mix_f32 v114, v73, v50, v58 op_sel:[0,0,0] op_sel_hi:[1,0,0]\n"
    "v_fma_mix_f32 v115, v73, v51, v59 op_sel:[1,0,0] op_sel_hi:[1,0,0]\n"
    "v_fma_mix_f32 v116, v74, v52, v60 op_sel:[0,0,0] op_sel_hi:[1,0,0]\n"
    "v_fma_mix_f32 v117, v74, v53, v61 op_sel:[1,0,0] op_sel_hi:[1,0,0]\n"
    "v_fma_mix_f32 v118, v75, v54, v62 op_sel:[0,0,0] op_sel_hi:[1,0,0]\n"
    "v_fma_mix_f32 v119, v75, v55, v63 op_sel:[1,0,0] op_sel_hi:[1,0,0]\n"
    "v_cvt_pk_bf16_f32 v104, v112, v113\n"
    "v_cvt_pk_bf16_f32 v105, v114, v115\n"
    "v_cvt_pk_bf16_f32 v106, v116, v117\n"
    "v_cvt_pk_bf16_f32 v107, v118, v119\n"
    "v_fma_mix_f32 v120, v76, v48, v56 op_sel:[0,0,0] op_sel_hi:[1,0,0]\n"
    "v_fma_mix_f32 v121, v76, v49, v57 op_sel:[1,0,0] op_sel_hi:[1,0,0]\n"
    "v_fma_mix_f32 v122, v77, v50, v58 op_sel:[0,0,0] op_sel_hi:[1,0,0]\n"
    "v_fma_mix_f32 v123, v77, v51, v59 op_sel:[1,0,0] op_sel_hi:[1,0,0]\n"
    "v_fma_mix_f32 v124, v78, v52, v60 op_sel:[0,0,0] op_sel_hi:[1,0,0]\n"
    "v_fma_mix_f32 v125, v78, v53, v61 op_sel:[1,0,0] op_sel_hi:[1,0,0]\n"
    "v_fma_mix_f32 v126, v79, v54, v62 op_sel:[0,0,0] op_sel_hi:[1,0,0]\n"
    "v_fma_mix_f32 v127, v79, v55, v63 op_sel:[1,0,0] op_sel_hi:[1,0,0]\n"
    "v_cvt_pk_bf16_f32 v108, v120, v121\n"
    "v_cvt_pk_bf16_f32 v109, v122, v123\n"
    "v_cvt_pk_bf16_f32 v110, v124, v125\n"
    "v_cvt_pk_bf16_f32 v111, v126, v127\n"
    "v_add_u32 v13, 0x8000, v13\n"
    "v_add_u32 v14, 0x8000, v14\n"
    "s_waitcnt lgkmcnt(0)\n"
    "s_mov_b32 s63, 2\n"
    "L_qloop_%=:\n"
    "s_cbranch_vccz L_qa2_0_%=\n"
    "s_barrier\n"
    "L_qa2_0_%=:\n"
    "v_mfma_f32_16x16x32_bf16 a[0:3], a[64:67], v[96:99], a[0:3]\n"
    "ds_read_b128 v[32:35], v15 offset:512\n"
    "v_mfma_f32_16x16x32_bf16 a[32:35], a[64:67], v[100:103], a[32:35]\n"
    "ds_read_b64_tr_b16 a[64:65], v13 offset:8192\n"
    "ds_read_b64_tr_b16 a[66:67], v13 offset:12288\n"
    "v_mfma_f32_16x16x32_bf16 a[4:7], a[68:71], v[96:99], a[4:7]\n"
    "s_cmp_eq_u32 s63, 2\n"
    "s_cbranch_scc1 L_nopf_%=\n"
    "s_add_u32 m0, s64, s61\n"
    "s_nop 0\n"
    "buffer_load_dwordx4 v19, s[16:19], s61 offen sc1 lds\n"
    "buffer_load_dwordx4 v19, s[16:19], s61 offen offset:1024 sc1 lds\n"
    "s_add_u32 s61, s61, 0x4000\n"
    "s_add_u32 m0, s64, s61\n"
    "s_nop 0\n"
    "buffer_load_dwordx4 v19, s[16:19], s61 offen sc1 lds\n"
    "buffer_load_dwordx4 v19, s[16:19], s61 offen offset:1024 sc1 lds\n"
    "s_add_u32 s61, s61, 0x4000\n"
    "L_nopf_%=:\n"
    "v_mfma_f32_16x16x32_bf16 a[36:39], a[68:71], v[100:103], a[36:39]\n"
    "ds_read_b64_tr_b16 a[68:69], v14 offset:8192\n"
    "ds_read_b64_tr_b16 a[70:71], v14 offset:12288\n"
    "v_mfma_f32_16x16x32_bf16 a[8:11], a[72:75], v[96:99], a[8:11]\n"
    "ds_read_b128 v[36:39], v15 offset:528\n"
    "v_mfma_f32_16x16x32_bf16 a[40:43], a[72:75], v[100:103], a[40:43]\n"
    "ds_read_b64_tr_b16 a[72:73], v13 offset:8704\n"
    "ds_read_b64_tr_b16 a[74:75], v13 offset:12800\n"
    "v_mfma_f32_16x16x32_bf16 a[12:15], a[76:79], v[96:99], a[12:15]\n"
    "s_add_u32 s62, s62, 4096\n"
    "v_mfma_f32_16x16x32_bf16 a[44:47], a[76:79], v[100:103], a[44:47]\n"
    "ds_read_b64_tr_b16 a[76:77], v14 offset:8704\n"
    "ds_read_b64_tr_b16 a[78:79], v14 offset:12800\n"
    "v_mfma_f32_16x16x32_bf16 a[16:19], a[80:83], v[96:99], a[16:19]\n"
    "ds_read_b128 v[40:43], v15 offset:544\n"
    "v_mfma_f32_16x16x32_bf16 a[48:51], a[80:83], v[100:103], a[48:51]\n"
    "ds_read_b64_tr_b16 a[80:81], v13 offset:9216\n"
    "ds_read_b64_tr_b16 a[82:83], v13 offset:13312\n"
    "v_mfma_f32_16x16x32_bf16 a[20:23], a[84:87], v[96:99], a[20:23]\n"
    "s_add_u32 s59, s59, 4096\n"
    "v_mfma_f32_16x16x32_bf16 a[52:55], a[84:87], v[100:103], a[52:55]\n"
    "ds_read_b64_tr_b16 a[84:85], v14 offset:9216\n"
    "ds_read_b64_tr_b16 a[86:87], v14 offset:13312\n"
    "v_mfma_f32_16x16x32_bf16 a[24:27], a[88:91], v[96:99], a[24:27]\n"
    "ds_read_b128 v[44:47], v15 offset:560\n"
    "v_mfma_f32_16x16x32_bf16 a[56:59], a[88:91], v[100:103], a[56:59]\n"
    "ds_read_b64_tr_b16 a[88:89], v13 offset:9728\n"
    "ds_read_b64_tr_b16 a[90:91], v13 offset:13824\n"
    "v_mfma_f32_16x16x32_bf16 a[28:31], a[92:95], v[96:99], a[28:31]\n"
    "buffer_load_dwordx4 v[64:67], v2, s[20:23], s62 offen offset:0 sc1\n"
    "v_mfma_f32_16x16x32_bf16 a[60:63], a[92:95], v[100:103], a[60:63]\n"
    "ds_read_b64_tr_b16 a[92:93], v14 offset:9728\n"
    "ds_read_b64_tr_b16 a[94:95], v14 offset:13824\n"
    "s_waitcnt lgkmcnt(10)\n"
    "v_mfma_f32_16x16x32_bf16 a[0:3], a[64:67], v[104:107], a[0:3]\n"
    "ds_read_b128 v[48:51], v15 offset:768\n"
    "v_mfma_f32_16x16x32_bf16 a[32:35], a[64:67], v[108:111], a[32:35]\n"
    "ds_read_b64_tr_b16 a[64:65], v13 offset:16384\n"
    "ds_read_b64_tr_b16 a[66:67], v13 offset:20480\n"
    "v_mfma_f32_16x16x32_bf16 a[4:7], a[68:71], v[104:107], a[4:7]\n"
    "buffer_load_dwordx4 v[68:71], v2, s[20:23], s59 offen offset:0 sc1\n"
    "v_mfma_f32_16x16x32_bf16 a[36:39], a[68:71], v[108:111], a[36:39]\n"
    "ds_read_b64_tr_b16 a[68:69], v14 offset:16384\n"
    "ds_read_b64_tr_b16 a[70:71], v14 offset:20480\n"
    "v_mfma_f32_16x16x32_bf16 a[8:11], a[72:75], v[104:107], a[8:11]\n"
    "ds_read_b128 v[52:55], v15 offset:784\n"
    "v_mfma_f32_16x16x32_bf16 a[40:43], a[72:75], v[108:111], a[40:43]\n"
    "ds_read_b64_tr_b16 a[72:73], v13 offset:16896\n"
    "ds_read_b64_tr_b16 a[74:75], v13 offset:20992\n"
    "v_mfma_f32_16x16x32_bf16 a[12:15], a[76:79], v[104:107], a[12:15]\n"
    "buffer_load_dwordx4 v[72:75], v2, s[20:23], s62 offen offset:1024 sc1\n"
    "v_mfma_f32_16x16x32_bf16 a[44:47], a[76:79], v[108:111], a[44:47]\n"
    "ds_read_b64_tr_b16 a[76:77], v14 offset:16896\n"
    "ds_read_b64_tr_b16 a[78:79], v14 offset:20992\n"
    "s_waitcnt lgkmcnt(10)\n"
    "v_mfma_f32_16x16x32_bf16 a[16:19], a[80:83], v[104:107], a[16:19]\n"
    "ds_read_b128 v[56:59], v15 offset:800\n"
    "v_mfma_f32_16x16x32_bf16 a[48:51], a[80:83], v[108:111], a[48:51]\n"
    "ds_read_b64_tr_b16 a[80:81], v13 offset:17408\n"
    "ds_read_b64_tr_b16 a[82:83], v13 offset:21504\n"
    "v_mfma_f32_16x16x32_bf16 a[20:23], a[84:87], v[104:107], a[20:23]\n"
    "buffer_load_dwordx4 v[76:79], v2, s[20:23], s59 offen offset:1024 sc1\n"
    "v_mfma_f32_16x16x32_bf16 a[52:55], a[84:87], v[108:111], a[52:55]\n"
    "ds_read_b64_tr_b16 a[84:85], v14 offset:17408\n"
    "ds_read_b64_tr_b16 a[86:87], v14 offset:21504\n"
    "v_mfma_f32_16x16x32_bf16 a[24:27], a[88:91], v[104:107], a[24:27]\n"
    "ds_read_b128 v[60:63], v15 offset:816\n"
    "v_mfma_f32_16x16x32_bf16 a[56:59], a[88:91], v[108:111], a[56:59]\n"
    "ds_read_b64_tr_b16 a[88:89], v13 offset:17920\n"
    "ds_read_b64_tr_b16 a[90:91], v13 offset:22016\n"
    "s_cbranch_vccnz L_qb2_0_%=\n"
    "s_barrier\n"
    "L_qb2_0_%=:\n"
    "v_mfma_f32_16x16x32_bf16 a[28:31], a[92:95], v[104:107], a[28:31]\n"
    "v_mfma_f32_16x16x32_bf16 a[60:63], a[92:95], v[108:111], a[60:63]\n"
    "ds_read_b64_tr_b16 a[92:93], v14 offset:17920\n"
    "ds_read_b64_tr_b16 a[94:95], v14 offset:22016\n"
    "s_waitcnt vmcnt(4)\n"
    "s_waitcnt lgkmcnt(4)\n"
    "v_fma_mix_f32 v112, v80, v32, v40 op_sel:[0,0,0] op_sel_hi:[1,0,0]\n"
    "v_fma_mix_f32 v113, v80, v33, v41 op_sel:[1,0,0] op_sel_hi:[1,0,0]\n"
    "v_fma_mix_f32 v114, v81, v34, v42 op_sel:[0,0,0] op_sel_hi:[1,0,0]\n"
    "v_fma_mix_f32 v115, v81, v35, v43 op_sel:[1,0,0] op_sel_hi:[1,0,0]\n"
    "v_fma_mix_f32 v116, v82, v36, v44 op_sel:[0,0,0] op_sel_hi:[1,0,0]\n"
    "v_fma_mix_f32 v117, v82, v37, v45 op_sel:[1,0,0] op_sel_hi:[1,0,0]\n"
    "v_fma_mix_f32 v118, v83, v38, v46 op_sel:[0,0,0] op_sel_hi:[1,0,0]\n"
    "v_fma_mix_f32 v119, v83, v39, v47 op_sel:[1,0,0] op_sel_hi:[1,0,0]\n"
    "v_cvt_pk_bf16_f32 v96, v112, v113\n"
    "v_cvt_pk_bf16_f32 v97, v114, v115\n"
    "v_cvt_pk_bf16_f32 v98, v116, v117\n"
    "v_cvt_pk_bf16_f32 v99, v118, v119\n"
    "v_fma_mix_f32 v120, v84, v32, v40 op_sel:[0,0,0] op_sel_hi:[1,0,0]\n"
    "v_fma_mix_f32 v121, v84, v33, v41 op_sel:[1,0,0] op_sel_hi:[1,0,0]\n"
    "v_fma_mix_f32 v122, v85, v34, v42 op_sel:[0,0,0] op_sel_hi:[1,0,0]\n"
    "v_fma_mix_f32 v123, v85, v35, v43 op_sel:[1,0,0] op_sel_hi:[1,0,0]\n"
    "v_fma_mix_f32 v124, v86, v36, v44 op_sel:[0,0,0] op_sel_hi:[1,0,0]\n"
    "v_fma_mix_f32 v125, v86, v37, v45 op_sel:[1,0,0] op_sel_hi:[1,0,0]\n"
    "v_fma_mix_f32 v126, v87, v38, v46 op_sel:[0,0,0] op_sel_hi:[1,0,0]\n"
    "v_fma_mix_f32 v127, v87, v39, v47 op_sel:[1,0,0] op_sel_hi:[1,0,0]\n"
    "v_cvt_pk_bf16_f32 v100, v120, v121\n"
    "v_cvt_pk_bf16_f32 v101, v122, v123\n"
    "v_cvt_pk_bf16_f32 v102, v124, v125\n"
    "v_cvt_pk_bf16_f32 v103, v126, v127\n"
    "v_fma_mix_f32 v112, v88, v48, v56 op_sel:[0,0,0] op_sel_hi:[1,0,0]\n"
    "v_fma_mix_f32 v113, v88, v49, v57 op_sel:[1,0,0] op_sel_hi:[1,0,0]\n"
    "v_fma_mix_f32 v114, v89, v50, v58 op_sel:[0,0,0] op_sel_hi:[1,0,0]\n"
    "v_fma_mix_f32 v115, v89, v51, v59 op_sel:[1,0,0] op_sel_hi:[1,0,0]\n"
    "v_fma_mix_f32 v116, v90, v52, v60 op_sel:[0,0,0] op_sel_hi:[1,0,0]\n"
    "v_fma_mix_f32 v117, v90, v53, v61 op_sel:[1,0,0] op_sel_hi:[1,0,0]\n"
    "v_fma_mix_f32 v118, v91, v54, v62 op_sel:[0,0,0] op_sel_hi:[1,0,0]\n"
    "v_fma_mix_f32 v119, v91, v55, v63 op_sel:[1,0,0] op_sel_hi:[1,0,0]\n"
    "v_cvt_pk_bf16_f32 v104, v112, v113\n"
    "v_cvt_pk_bf16_f32 v105, v114, v115\n"
    "v_cvt_pk_bf16_f32 v106, v116, v117\n"
    "v_cvt_pk_bf16_f32 v107, v118, v119\n"
    "v_fma_mix_f32 v120, v92, v48, v56 op_sel:[0,0,0] op_sel_hi:[1,0,0]\n"
    "v_fma_mix_f32 v121, v92, v49, v57 op_sel:[1,0,0] op_sel_hi:[1,0,0]\n"
    "v_fma_mix_f32 v122, v93, v50, v58 op_sel:[0,0,0] op_sel_hi:[1,0,0]\n"
    "v_fma_mix_f32 v123, v93, v51, v59 op_sel:[1,0,0] op_sel_hi:[1,0,0]\n"
    "v_fma_mix_f32 v124, v94, v52, v60 op_sel:[0,0,0] op_sel_hi:[1,0,0]\n"
    "v_fma_mix_f32 v125, v94, v53, v61 op_sel:[1,0,0] op_sel_hi:[1,0,0]\n"
    "v_fma_mix_f32 v126, v95, v54, v62 op_sel:[0,0,0] op_sel_hi:[1,0,0]\n"
    "v_fma_mix_f32 v127, v95, v55, v63 op_sel:[1,0,0] op_sel_hi:[1,0,0]\n"
    "v_cvt_pk_bf16_f32 v108, v120, v121\n"
    "v_cvt_pk_bf16_f32 v109, v122, v123\n"
    "v_cvt_pk_bf16_f32 v110, v124, v125\n"
    "v_cvt_pk_bf16_f32 v111, v126, v127\n"
    "s_waitcnt lgkmcnt(0)\n"
    "s_cbranch_vccz L_qa2_1_%=\n"
    "s_barrier\n"
    "L_qa2_1_%=:\n"
    "v_add_u32 v15, 1024, v15\n"
    "v_mfma_f32_16x16x32_bf16 a[0:3], a[64:67], v[96:99], a[0:3]\n"
    "ds_read_b128 v[32:35], v15 offset:0\n"
    "v_mfma_f32_16x16x32_bf16 a[32:35], a[64:67], v[100:103], a[32:35]\n"
    "ds_read_b64_tr_b16 a[64:65], v13 offset:24576\n"
    "ds_read_b64_tr_b16 a[66:67], v13 offset:28672\n"
    "v_mfma_f32_16x16x32_bf16 a[4:7], a[68:71], v[96:99], a[4:7]\n"
    "s_add_u32 m0, s64, s61\n"
    "s_nop 0\n"
    "buffer_load_dwordx4 v19, s[16:19], s61 offen sc1 lds\n"
    "v_mfma_f32_16x16x32_bf16 a[36:39], a[68:71], v[100:103], a[36:39]\n"
    "ds_read_b64_tr_b16 a[68:69], v14 offset:24576\n"
    "ds_read_b64_tr_b16 a[70:71], v14 offset:28672\n"
    "v_mfma_f32_16x16x32_bf16 a[8:11], a[72:75], v[96:99], a[8:11]\n"
    "ds_read_b128 v[36:39], v15 offset:16\n"
    "v_mfma_f32_16x16x32_bf16 a[40:43], a[72:75], v[100:103], a[40:43]\n"
    "ds_read_b64_tr_b16 a[72:73], v13 offset:25088\n"
    "ds_read_b64_tr_b16 a[74:75], v13 offset:29184\n"
    "v_mfma_f32_16x16x32_bf16 a[12:15], a[76:79], v[96:99], a[12:15]\n"
    "buffer_load_dwordx4 v19, s[16:19], s61 offen offset:1024 sc1 lds\n"
    "s_add_u32 s61, s61, 0x4000\n"
    "v_mfma_f32_16x16x32_bf16 a[44:47], a[76:79], v[100:103], a[44:47]\n"
    "ds_read_b64_tr_b16 a[76:77], v14 offset:25088\n"
    "ds_read_b64_tr_b16 a[78:79], v14 offset:29184\n"
    "v_mfma_f32_16x16x32_bf16 a[16:19], a[80:83], v[96:99], a[16:19]\n"
    "ds_read_b128 v[40:43], v15 offset:32\n"
    "v_mfma_f32_16x16x32_bf16 a[48:51], a[80:83], v[100:103], a[48:51]\n"
    "ds_read_b64_tr_b16 a[80:81], v13 offset:25600\n"
    "ds_read_b64_tr_b16 a[82:83], v13 offset:29696\n"
    "v_mfma_f32_16x16x32_bf16 a[20:23], a[84:87], v[96:99], a[20:23]\n"
    "s_add_u32 m0, s64, s61\n"
    "s_nop 0\n"
    "buffer_load_dwordx4 v19, s[16:19], s61 offen sc1 lds\n"
    "v_mfma_f32_16x16x32_bf16 a[52:55], a[84:87], v[100:103], a[52:55]\n"
    "ds_read_b64_tr_b16 a[84:85], v14 offset:25600\n"
    "ds_read_b64_tr_b16 a[86:87], v14 offset:29696\n"
    "v_mfma_f32_16x16x32_bf16 a[24:27], a[88:91], v[96:99], a[24:27]\n"
    "ds_read_b128 v[44:47], v15 offset:48\n"
    "v_mfma_f32_16x16x32_bf16 a[56:59], a[88:91], v[100:103], a[56:59]\n"
    "ds_read_b64_tr_b16 a[88:89], v13 offset:26112\n"
    "ds_read_b64_tr_b16 a[90:91], v13 offset:30208\n"
    "v_mfma_f32_16x16x32_bf16 a[28:31], a[92:95], v[96:99], a[28:31]\n"
    "buffer_load_dwordx4 v19, s[16:19], s61 offen offset:1024 sc1 lds\n"
    "s_add_u32 s61, s61, 0x4000\n"
    "v_mfma_f32_16x16x32_bf16 a[60:63], a[92:95], v[100:103], a[60:63]\n"
    "ds_read_b64_tr_b16 a[92:93], v14 offset:26112\n"
    "ds_read_b64_tr_b16 a[94:95], v14 offset:30208\n"
    "s_waitcnt lgkmcnt(10)\n"
    "v_mfma_f32_16x16x32_bf16 a[0:3], a[64:67], v[104:107], a[0:3]\n"
    "ds_read_b128 v[48:51], v15 offset:256\n"
    "v_mfma_f32_16x16x32_bf16 a[32:35], a[64:67], v[108:111], a[32:35]\n"
    "ds_read_b64_tr_b16 a[64:65], v13 offset:32768\n"
    "ds_read_b64_tr_b16 a[66:67], v13 offset:36864\n"
    "v_mfma_f32_16x16x32_bf16 a[4:7], a[68:71], v[104:107], a[4:7]\n"
    "buffer_load_dwordx4 v[80:83], v2, s[20:23], s62 offen offset:2048 sc1\n"
    "v_mfma_f32_16x16x32_bf16 a[36:39], a[68:71], v[108:111], a[36:39]\n"
    "ds_read_b64_tr_b16 a[68:69], v14 offset:32768\n"
    "ds_read_b64_tr_b16 a[70:71], v14 offset:36864\n"
    "v_mfma_f32_16x16x32_bf16 a[8:11], a[72:75], v[104:107], a[8:11]\n"
    "ds_read_b128 v[52:55], v15 offset:272\n"
    "v_mfma_f32_16x16x32_bf16 a[40:43], a[72:75], v[108:111], a[40:43]\n"
    "ds_read_b64_tr_b16 a[72:73], v13 offset:33280\n"
    "ds_read_b64_tr_b16 a[74:75], v13 offset:37376\n"
    "v_mfma_f32_16x16x32_bf16 a[12:15], a[76:79], v[104:107], a[12:15]\n"
    "buffer_load_dwordx4 v[84:87], v2, s[20:23], s59 offen offset:2048 sc1\n"
    "v_mfma_f32_16x16x32_bf16 a[44:47], a[76:79], v[108:111], a[44:47]\n"
    "ds_read_b64_tr_b16 a[76:77], v14 offset:33280\n"
    "ds_read_b64_tr_b16 a[78:79], v14 offset:37376\n"
    "s_waitcnt lgkmcnt(10)\n"
    "v_mfma_f32_16x16x32_bf16 a[16:19], a[80:83], v[104:107], a[16:19]\n"
    "ds_read_b128 v[56:59], v15 offset:288\n"
    "v_mfma_f32_16x16x32_bf16 a[48:51], a[80:83], v[108:111], a[48:51]\n"
    "ds_read_b64_tr_b16 a[80:81], v13 offset:33792\n"
    "ds_read_b64_tr_b16 a[82:83], v13 offset:37888\n"
    "v_mfma_f32_16x16x32_bf16 a[20:23], a[84:87], v[104:107], a[20:23]\n"
    "buffer_load_dwordx4 v[88:91], v2, s[20:23], s62 offen offset:3072 sc1\n"
    "v_mfma_f32_16x16x32_bf16 a[52:55], a[84:87], v[108:111], a[52:55]\n"
    "ds_read_b64_tr_b16 a[84:85], v14 offset:33792\n"
    "ds_read_b64_tr_b16 a[86:87], v14 offset:37888\n"
    "v_mfma_f32_16x16x32_bf16 a[24:27], a[88:91], v[104:107], a[24:27]\n"
    "ds_read_b128 v[60:63], v15 offset:304\n"
    "v_mfma_f32_16x16x32_bf16 a[56:59], a[88:91], v[108:111], a[56:59]\n"
    "ds_read_b64_tr_b16 a[88:89], v13 offset:34304\n"
    "ds_read_b64_tr_b16 a[90:91], v13 offset:38400\n"
    "s_cbranch_vccnz L_qb2_1_%=\n"
    "s_barrier\n"
    "L_qb2_1_%=:\n"
    "v_mfma_f32_16x16x32_bf16 a[28:31], a[92:95], v[104:107], a[28:31]\n"
    "buffer_load_dwordx4 v[92:95], v2, s[20:23], s59 offen offset:3072 sc1\n"
    "v_mfma_f32_16x16x32_bf16 a[60:63], a[92:95], v[108:111], a[60:63]\n"
    "ds_read_b64_tr_b16 a[92:93], v14 offset:34304\n"
    "ds_read_b64_tr_b16 a[94:95], v14 offset:38400\n"
    "s_cmp_eq_u32 s63, 1\n"
    "s_cbranch_scc1 L_qdone_%=\n"
    "s_waitcnt vmcnt(4)\n"
    "s_waitcnt lgkmcnt(4)\n"
    "v_fma_mix_f32 v112, v64, v32, v40 op_sel:[0,0,0] op_sel_hi:[1,0,0]\n"
    "v_fma_mix_f32 v113, v64, v33, v41 op_sel:[1,0,0] op_sel_hi:[1,0,0]\n"
    "v_fma_mix_f32 v114, v65, v34, v42 op_sel:[0,0,0] op_sel_hi:[1,0,0]\n"
    "v_fma_mix_f32 v115, v65, v35, v43 op_sel:[1,0,0] op_sel_hi:[1,0,0]\n"
    "v_fma_mix_f32 v116, v66, v36, v44 op_sel:[0,0,0] op_sel_hi:[1,0,0]\n"
    "v_fma_mix_f32 v117, v66, v37, v45 op_sel:[1,0,0] op_sel_hi:[1,0,0]\n"
    "v_fma_mix_f32 v118, v67, v38, v46 op_sel:[0,0,0] op_sel_hi:[1,0,0]\n"
    "v_fma_mix_f32 v119, v67, v39, v47 op_sel:[1,0,0] op_sel_hi:[1,0,0]\n"
    "v_cvt_pk_bf16_f32 v96, v112, v113\n"
    "v_cvt_pk_bf16_f32 v97, v114, v115\n"
    "v_cvt_pk_bf16_f32 v98, v116, v117\n"
    "v_cvt_pk_bf16_f32 v99, v118, v119\n"
    "v_fma_mix_f32 v120, v68, v32, v40 op_sel:[0,0,0] op_sel_hi:[1,0,0]\n"
    "v_fma_mix_f32 v121, v68, v33, v41 op_sel:[1,0,0] op_sel_hi:[1,0,0]\n"
    "v_fma_mix_f32 v122, v69, v34, v42 op_sel:[0,0,0] op_sel_hi:[1,0,0]\n"
    "v_fma_mix_f32 v123, v69, v35, v43 op_sel:[1,0,0] op_sel_hi:[1,0,0]\n"
    "v_fma_mix_f32 v124, v70, v36, v44 op_sel:[0,0,0] op_sel_hi:[1,0,0]\n"
    "v_fma_mix_f32 v125, v70, v37, v45 op_sel:[1,0,0] op_sel_hi:[1,0,0]\n"
    "v_fma_mix_f32 v126, v71, v38, v46 op_sel:[0,0,0] op_sel_hi:[1,0,0]\n"
    "v_fma_mix_f32 v127, v71, v39, v47 op_sel:[1,0,0] op_sel_hi:[1,0,0]\n"
    "v_cvt_pk_bf16_f32 v100, v120, v121\n"
    "v_cvt_pk_bf16_f32 v101, v122, v123\n"
    "v_cvt_pk_bf16_f32 v102, v124, v125\n"
    "v_cvt_pk_bf16_f32 v103, v126, v127\n"
    "v_fma_mix_f32 v112, v72, v48, v56 op_sel:[0,0,0] op_sel_hi:[1,0,0]\n"
    "v_fma_mix_f32 v113, v72, v49, v57 op_sel:[1,0,0] op_sel_hi:[1,0,0]\n"
    "v_fma_mix_f32 v114, v73, v50, v58 op_sel:[0,0,0] op_sel_hi:[1,0,0]\n"
    "v_fma_mix_f32 v115, v73, v51, v59 op_sel:[1,0,0] op_sel_hi:[1,0,0]\n"
    "v_fma_mix_f32 v116, v74, v52, v60 op_sel:[0,0,0] op_sel_hi:[1,0,0]\n"
    "v_fma_mix_f32 v117, v74, v53, v61 op_sel:[1,0,0] op_sel_hi:[1,0,0]\n"
    "v_fma_mix_f32 v118, v75, v54, v62 op_sel:[0,0,0] op_sel_hi:[1,0,0]\n"
    "v_fma_mix_f32 v119, v75, v55, v63 op_sel:[1,0,0] op_sel_hi:[1,0,0]\n"
    "v_cvt_pk_bf16_f32 v104, v112, v113\n"
    "v_cvt_pk_bf16_f32 v105, v114, v115\n"
    "v_cvt_pk_bf16_f32 v106, v116, v117\n"
    "v_cvt_pk_bf16_f32 v107, v118, v119\n"
    "v_fma_mix_f32 v120, v76, v48, v56 op_sel:[0,0,0] op_sel_hi:[1,0,0]\n"
    "v_fma_mix_f32 v121, v76, v49, v57 op_sel:[1,0,0] op_sel_hi:[1,0,0]\n"
    "v_fma_mix_f32 v122, v77, v50, v58 op_sel:[0,0,0] op_sel_hi:[1,0,0]\n"
    "v_fma_mix_f32 v123, v77, v51, v59 op_sel:[1,0,0] op_sel_hi:[1,0,0]\n"
    "v_fma_mix_f32 v124, v78, v52, v60 op_sel:[0,0,0] op_sel_hi:[1,0,0]\n"
    "v_fma_mix_f32 v125, v78, v53, v61 op_sel:[1,0,0] op_sel_hi:[1,0,0]\n"
    "v_fma_mix_f32 v126, v79, v54, v62 op_sel:[0,0,0] op_sel_hi:[1,0,0]\n"
    "v_fma_mix_f32 v127, v79, v55, v63 op_sel:[1,0,0] op_sel_hi:[1,0,0]\n"
    "v_cvt_pk_bf16_f32 v108, v120, v121\n"
    "v_cvt_pk_bf16_f32 v109, v122, v123\n"
    "v_cvt_pk_bf16_f32 v110, v124, v125\n"
    "v_cvt_pk_bf16_f32 v111, v126, v127\n"
    "v_add_u32 v13, 0x8000, v13\n"
    "v_add_u32 v14, 0x8000, v14\n"
    "s_waitcnt lgkmcnt(0)\n"
    "s_sub_u32 s63, s63, 1\n"
    "s_cmp_lg_u32 s63, 0\n"
    "s_cbranch_scc1 L_qloop_%=\n"
    "L_qdone_%=:\n"
    "s_setprio 0\n"
    "s_lshl_b32 s44, s51, 1\n"
    "s_add_u32 s44, s44, 22\n"
    "s_waitcnt lgkmcnt(0)\n"
    "s_nop 7\n"
    "v_accvgpr_read_b32 v112, a0\n"
    "v_accvgpr_read_b32 v113, a1\n"
    "v_accvgpr_read_b32 v114, a2\n"
    "v_accvgpr_read_b32 v115, a3\n"
    "v_accvgpr_read_b32 v116, a4\n"
    "v_accvgpr_read_b32 v117, a5\n"
    "v_accvgpr_read_b32 v118, a6\n"
    "v_accvgpr_read_b32 v119, a7\n"
    "v_cvt_pk_bf16_f32 v32, v112, v113\n"
    "v_cvt_pk_bf16_f32 v33, v114, v115\n"
    "v_cvt_pk_bf16_f32 v34, v116, v117\n"
    "v_cvt_pk_bf16_f32 v35, v118, v119\n"
    "v_accvgpr_read_b32 v120, a8\n"
    "v_accvgpr_read_b32 v121, a9\n"
    "v_accvgpr_read_b32 v122, a10\n"
    "v_accvgpr_read_b32 v123, a11\n"
    "v_accvgpr_read_b32 v124, a12\n"
    "v_accvgpr_read_b32 v125, a13\n"
    "v_accvgpr_read_b32 v126, a14\n"
    "v_accvgpr_read_b32 v127, a15\n"
    "v_cvt_pk_bf16_f32 v36, v120, v121\n"
    "v_cvt_pk_bf16_f32 v37, v122, v123\n"
    "v_cvt_pk_bf16_f32 v38, v124, v125\n"
    "v_cvt_pk_bf16_f32 v39, v126, v127\n"
    "v_accvgpr_read_b32 v112, a16\n"
    "v_accvgpr_read_b32 v113, a17\n"
    "v_accvgpr_read_b32 v114, a18\n"
    "v_accvgpr_read_b32 v115, a19\n"
    "v_accvgpr_read_b32 v116, a20\n"
    "v_accvgpr_read_b32 v117, a21\n"
    "v_accvgpr_read_b32 v118, a22\n"
    "v_accvgpr_read_b32 v119, a23\n"
    "v_cvt_pk_bf16_f32 v40, v112, v113\n"
    "v_cvt_pk_bf16_f32 v41, v114, v115\n"
    "v_cvt_pk_bf16_f32 v42, v116, v117\n"
    "v_cvt_pk_bf16_f32 v43, v118, v119\n"
    "v_accvgpr_read_b32 v120, a24\n"
    "v_accvgpr_read_b32 v121, a25\n"
    "v_accvgpr_read_b32 v122, a26\n"
    "v_accvgpr_read_b32 v123, a27\n"
    "v_accvgpr_read_b32 v124, a28\n"
    "v_accvgpr_read_b32 v125, a29\n"
    "v_accvgpr_read_b32 v126, a30\n"
    "v_accvgpr_read_b32 v127, a31\n"
    "v_cvt_pk_bf16_f32 v44, v120, v121\n"
    "v_cvt_pk_bf16_f32 v45, v122, v123\n"
    "v_cvt_pk_bf16_f32 v46, v124, v125\n"
    "v_cvt_pk_bf16_f32 v47, v126, v127\n"
    "v_accvgpr_read_b32 v112, a32\n"
    "v_accvgpr_read_b32 v113, a33\n"
    "v_accvgpr_read_b32 v114, a34\n"
    "v_accvgpr_read_b32 v115, a35\n"
    "v_accvgpr_read_b32 v116, a36\n"
    "v_accvgpr_read_b32 v117, a37\n"
    "v_accvgpr_read_b32 v118, a38\n"
    "v_accvgpr_read_b32 v119, a39\n"
    "v_cvt_pk_bf16_f32 v48, v112, v113\n"
    "v_cvt_pk_bf16_f32 v49, v114, v115\n"
    "v_cvt_pk_bf16_f32 v50, v116, v117\n"
    "v_cvt_pk_bf16_f32 v51, v118, v119\n"
    "v_accvgpr_read_b32 v120, a40\n"
    "v_accvgpr_read_b32 v121, a41\n"
    "v_accvgpr_read_b32 v122, a42\n"
    "v_accvgpr_read_b32 v123, a43\n"
    "v_accvgpr_read_b32 v124, a44\n"
    "v_accvgpr_read_b32 v125, a45\n"
    "v_accvgpr_read_b32 v126, a46\n"
    "v_accvgpr_read_b32 v127, a47\n"
    "v_cvt_pk_bf16_f32 v52, v120, v121\n"
    "v_cvt_pk_bf16_f32 v53, v122, v123\n"
    "v_cvt_pk_bf16_f32 v54, v124, v125\n"
    "v_cvt_pk_bf16_f32 v55, v126, v127\n"
    "v_accvgpr_read_b32 v112, a48\n"
    "v_accvgpr_read_b32 v113, a49\n"
    "v_accvgpr_read_b32 v114, a50\n"
    "v_accvgpr_read_b32 v115, a51\n"
    "v_accvgpr_read_b32 v116, a52\n"
    "v_accvgpr_read_b32 v117, a53\n"
    "v_accvgpr_read_b32 v118, a54\n"
    "v_accvgpr_read_b32 v119, a55\n"
    "v_cvt_pk_bf16_f32 v56, v112, v113\n"
    "v_cvt_pk_bf16_f32 v57, v114, v115\n"
    "v_cvt_pk_bf16_f32 v58, v116, v117\n"
    "v_cvt_pk_bf16_f32 v59, v118, v119\n"
    "v_accvgpr_read_b32 v120, a56\n"
    "v_accvgpr_read_b32 v121, a57\n"
    "v_accvgpr_read_b32 v122, a58\n"
    "v_accvgpr_read_b32 v123, a59\n"
    "v_accvgpr_read_b32 v124, a60\n"
    "v_accvgpr_read_b32 v125, a61\n"
    "v_accvgpr_read_b32 v126, a62\n"
    "v_accvgpr_read_b32 v127, a63\n"
    "v_cvt_pk_bf16_f32 v60, v120, v121\n"
    "v_cvt_pk_bf16_f32 v61, v122, v123\n"
    "v_cvt_pk_bf16_f32 v62, v124, v125\n"
    "v_cvt_pk_bf16_f32 v63, v126, v127\n"
    "s_lshl_b32 s52, %1, 2\n"
    "s_add_u32 s52, s52, s51\n"
    "s_lshl_b32 s52, s52, 13\n"
    "s_waitcnt vmcnt(0)\n"
    "s_mov_b64 s[20:21], s[80:81]\n"
    "s_add_u32 s20, s20, s52\n"
    "s_addc_u32 s21, s21, 0\n"
    "s_and_b32 s21, s21, 0xffff\n"
    "s_mov_b32 s22, 0x2000\n"
    "s_mov_b32 s23, 0x20000\n"
    "s_mov_b32 s52, 4096\n"
    "s_mov_b32 s54, 0x1c0000\n"
    "s_cmp_eq_u32 s50, 1\n"
    "s_cbranch_scc1 L_nostash_%=\n"
    "buffer_store_dwordx4 v[32:35], v2, s[20:23], 0 offen offset:0\n"
    "buffer_store_dwordx4 v[36:39], v2, s[20:23], 0 offen offset:1024\n"
    "buffer_store_dwordx4 v[40:43], v2, s[20:23], 0 offen offset:2048\n"
    "buffer_store_dwordx4 v[44:47], v2, s[20:23], 0 offen offset:3072\n"
    "buffer_store_dwordx4 v[48:51], v2, s[20:23], s52 offen offset:0\n"
    "buffer_store_dwordx4 v[52:55], v2, s[20:23], s52 offen offset:1024\n"
    "buffer_store_dwordx4 v[56:59], v2, s[20:23], s52 offen offset:2048\n"
    "buffer_store_dwordx4 v[60:63], v2, s[20:23], s52 offen offset:3072\n"
    "s_branch L_stashed_%=\n"
    "L_nostash_%=:\n"
    "v_lshlrev_b32 v20, 3, v2\n"
    "s_lshl_b32 s52, %1, 13\n"
    "s_lshl_b32 s53, s51, 21\n"
    "s_add_u32 s53, s53, s52\n"
    "s_cmp_lt_u32 s51, 2\n"
    "s_cselect_b32 s52, s53, s52\n"
    "s_cselect_b64 s[24:25], s[88:89], s[90:91]\n"
    "s_cselect_b32 s26, 0x400000, s54\n"
    "s_cmp_eq_u32 s51, 3\n"
    "s_cselect_b64 s[24:25], s[98:99], s[24:25]\n"
    "s_and_b32 s25, s25, 0xffff\n"
    "buffer_load_dword v21, v20, s[24:27], s52 offen\n"
    "s_mov_b64 s[24:25], s[92:93]\n"
    "s_and_b32 s25, s25, 0xffff\n"
    "s_cmp_eq_u32 s51, 2\n"
    "s_cselect_b32 s26, s54, 0\n"
    "s_nop 0\n"
    "buffer_load_dword v22, v20, s[24:27], s52 offen\n"
    "L_stashed_%=:\n"
    "s_lshl_b32 s52, s51, 12\n"
    "v_add_u32 v19, s52, v2\n"
    "s_mov_b32 s46, 6\n"
    "s_add_u32 s47, s52, 98304\n"
    "s_mov_b32 s48, 98304\n"
    "s_mov_b32 s38, 0\n"
    "s_mov_b32 s45, 0\n"
    "s_mov_b32 s14, 0\n"
    "s_mov_b32 s15, 0\n"
    "s_mov_b32 s39, s37\n"
    "s_cmp_eq_u32 s50, 1\n"
    "s_cselect_b32 s42, 0x1000, s32\n"
    "s_cselect_b32 s49, 0, s49\n"
    "s_mov_b32 s67, 0\n"
    "L_run_%=:\n"
    "v_mov_b32 v6, 0\n"
    "v_mov_b32 v7, 0\n"
    "v_mov_b32 v80, 0\n"
    "v_mov_b32 v81, 0\n"
    "v_mov_b32 v82, 0\n"
    "v_mov_b32 v83, 0\n"
    "v_mov_b32 v84, 0\n"
    "v_mov_b32 v85, 0\n"
    "v_mov_b32 v86, 0\n"
    "v_mov_b32 v87, 0\n"
    "v_add_u32 v3, s45, v2\n"
    "s_cmp_eq_u32 s38, s39\n"
    "s_cbranch_scc1 L_pm0_%=\n"
    "v_mov_b32 v64, v80\n"
    "v_mov_b32 v65, v80\n"
    "v_mov_b32 v66, v80\n"
    "v_mov_b32 v67, v80\n"
    "v_mov_b32 v68, v80\n"
    "v_mov_b32 v69, v80\n"
    "v_mov_b32 v70, v80\n"
    "v_mov_b32 v71, v80\n"
    "v_mov_b32 v72, v84\n"
    "v_mov_b32 v73, v84\n"
    "v_mov_b32 v74, v84\n"
    "v_mov_b32 v75, v84\n"
    "v_mov_b32 v76, v84\n"
    "v_mov_b32 v77, v84\n"
    "v_mov_b32 v78, v84\n"
    "v_mov_b32 v79, v84\n"
    "s_branch L_b0_%=\n"
    "L_pm0_%=:\n"
    "v_mov_b32 v10, 0xff800000\n"
    "v_cmp_gt_i32 s[52:53], 0, v18\n"
    "v_cmp_gt_i32 s[54:55], 1, v18\n"
    "v_cmp_gt_i32 s[56:57], 2, v18\n"
    "v_cmp_gt_i32 s[58:59], 3, v18\n"
    "v_cndmask_b32 v64, v80, v10, s[52:53]\n"
    "v_cndmask_b32 v65, v80, v10, s[54:55]\n"
    "v_cndmask_b32 v66, v80, v10, s[56:57]\n"
    "v_cndmask_b32 v67, v80, v10, s[58:59]\n"
    "v_cmp_gt_i32 s[52:53], 16, v18\n"
    "v_cmp_gt_i32 s[54:55], 17, v18\n"
    "v_cmp_gt_i32 s[56:57], 18, v18\n"
    "v_cmp_gt_i32 s[58:59], 19, v18\n"
    "v_cndmask_b32 v68, v80, v10, s[52:53]\n"
    "v_cndmask_b32 v69, v80, v10, s[54:55]\n"
    "v_cndmask_b32 v70, v80, v10, s[56:57]\n"
    "v_cndmask_b32 v71, v80, v10, s[58:59]\n"
    "v_cmp_gt_i32 s[52:53], -16, v18\n"
    "v_cmp_gt_i32 s[54:55], -15, v18\n"
    "v_cmp_gt_i32 s[56:57], -14, v18\n"
    "v_cmp_gt_i32 s[58:59], -13, v18\n"
    "v_cndmask_b32 v72, v84, v10, s[52:53]\n"
    "v_cndmask_b32 v73, v84, v10, s[54:55]\n"
    "v_cndmask_b32 v74, v84, v10, s[56:57]\n"
    "v_cndmask_b32 v75, v84, v10, s[58:59]\n"
    "v_cmp_gt_i32 s[52:53], 0, v18\n"
    "v_cmp_gt_i32 s[54:55], 1, v18\n"
    "v_cmp_gt_i32 s[56:57], 2, v18\n"
    "v_cmp_gt_i32 s[58:59], 3, v18\n"
    "v_cndmask_b32 v76, v84, v10, s[52:53]\n"
    "v_cndmask_b32 v77, v84, v10, s[54:55]\n"
    "v_cndmask_b32 v78, v84, v10, s[56:57]\n"
    "v_cndmask_b32 v79, v84, v10, s[58:59]\n"
    "L_b0_%=:\n"
    "s_cmp_eq_u32 s50, 1\n"
    "s_cbranch_scc1 L_b0lo_%=\n"
    "ds_read_b128 a[64:67], v3 offset:0\n"
    "s_add_u32 s15, s15, 1\n"
    "ds_read_b128 a[68:71], v3 offset:4096\n"
    "s_cmp_ge_u32 s38, s42\n"
    "s_cselect_b32 s54, 0x4000, 0\n"
    "s_addc_u32 s66, s38, 1\n"
    "ds_read_b128 a[72:75], v3 offset:1024\n"
    "s_add_u32 s45, s45, s54\n"
    "s_add_u32 s45, s45, 0x4000\n"
    "ds_read_b128 a[76:79], v3 offset:5120\n"
    "s_sub_u32 s53, s45, 163840\n"
    "ds_read_b128 a[80:83], v3 offset:2048\n"
    "s_cmp_ge_u32 s45, 163840\n"
    "s_cselect_b32 s45, s53, s45\n"
    "ds_read_b128 a[84:87], v3 offset:6144\n"
    "ds_read_b128 a[88:91], v3 offset:3072\n"
    "ds_read_b128 a[92:95], v3 offset:7168\n"
    "s_waitcnt lgkmcnt(4)\n"
    "v_mfma_f32_16x16x32_bf16 v[64:67], a[64:67], v[32:35], v[64:67]\n"
    "s_sub_u32 s53, s14, s65\n"
    "s_cselect_b32 s53, 0, s53\n"
    "v_mfma_f32_16x16x32_bf16 v[72:75], a[64:67], v[48:51], v[72:75]\n"
    "ds_read_b128 a[64:67], v3 offset:8192\n"
    "v_mfma_f32_16x16x32_bf16 v[68:71], a[68:71], v[32:35], v[68:71]\n"
    "s_add_u32 s60, s14, s53\n"
    "s_add_u32 s60, s60, 10\n"
    "v_mfma_f32_16x16x32_bf16 v[76:79], a[68:71], v[48:51], v[76:79]\n"
    "ds_read_b128 a[68:71], v3 offset:9216\n"
    "v_mfma_f32_16x16x32_bf16 v[64:67], a[72:75], v[36:39], v[64:67]\n"
    "s_min_u32 s60, s60, s49\n"
    "v_mfma_f32_16x16x32_bf16 v[72:75], a[72:75], v[52:55], v[72:75]\n"
    "ds_read_b128 a[72:75], v3 offset:10240\n"
    "v_mfma_f32_16x16x32_bf16 v[68:71], a[76:79], v[36:39], v[68:71]\n"
    "s_add_u32 s14, s14, 1\n"
    "v_mfma_f32_16x16x32_bf16 v[76:79], a[76:79], v[52:55], v[76:79]\n"
    "ds_read_b128 a[76:79], v3 offset:11264\n"
    "s_waitcnt lgkmcnt(4)\n"
    "v_mfma_f32_16x16x32_bf16 v[64:67], a[80:83], v[40:43], v[64:67]\n"
    "v_mfma_f32_16x16x32_bf16 v[72:75], a[80:83], v[56:59], v[72:75]\n"
    "ds_read_b128 a[80:83], v3 offset:12288\n"
    "v_mfma_f32_16x16x32_bf16 v[68:71], a[84:87], v[40:43], v[68:71]\n"
    "v_mfma_f32_16x16x32_bf16 v[76:79], a[84:87], v[56:59], v[76:79]\n"
    "ds_read_b128 a[84:87], v3 offset:13312\n"
    "s_sub_u32 s56, s60, s46\n"
    "v_mfma_f32_16x16x32_bf16 v[64:67], a[88:91], v[44:47], v[64:67]\n"
    "s_cmp_gt_u32 s60, s46\n"
    "v_mfma_f32_16x16x32_bf16 v[72:75], a[88:91], v[60:63], v[72:75]\n"
    "ds_read_b128 a[88:91], v3 offset:14336\n"
    "s_cselect_b32 s56, s56, 0\n"
    "s_waitcnt vmcnt(0)\n"
    "s_barrier\n"
    "v_mfma_f32_16x16x32_bf16 v[68:71], a[92:95], v[44:47], v[68:71]\n"
    "v_mfma_f32_16x16x32_bf16 v[76:79], a[92:95], v[60:63], v[76:79]\n"
    "ds_read_b128 a[92:95], v3 offset:15360\n"
    "v_max3_f32 v8, v64, v65, v66\n"
    "v_max3_f32 v9, v72, v73, v74\n"
    "s_nop 3\n"
    "v_max3_f32 v8, v8, v67, v68\n"
    "v_max3_f32 v9, v9, v75, v76\n"
    "v_max3_f32 v8, v8, v69, v70\n"
    "v_max3_f32 v9, v9, v77, v78\n"
    "v_max_f32 v8, v8, v71\n"
    "v_max_f32 v9, v9, v79\n"
    "v_mov_b32 v10, v8\n"
    "s_nop 1\n"
    "v_permlane16_swap_b32 v8, v10\n"
    "v_max_f32 v8, v8, v10\n"
    "v_mov_b32 v10, v8\n"
    "s_nop 1\n"
    "v_permlane32_swap_b32 v8, v10\n"
    "v_max_f32 v8, v8, v10\n"
    "v_mov_b32 v10, v9\n"
    "s_nop 1\n"
    "v_permlane16_swap_b32 v9, v10\n"
    "v_max_f32 v9, v9, v10\n"
    "v_mov_b32 v10, v9\n"
    "s_nop 1\n"
    "v_permlane32_swap_b32 v9, v10\n"
    "v_max_f32 v9, v9, v10\n"
    "v_mov_b32 v4, v8\n"
    "v_sub_f32 v64, v64, v8\n"
    "v_sub_f32 v65, v65, v8\n"
    "v_sub_f32 v66, v66, v8\n"
    "v_sub_f32 v67, v67, v8\n"
    "v_sub_f32 v68, v68, v8\n"
    "v_sub_f32 v69, v69, v8\n"
    "v_sub_f32 v70, v70, v8\n"
    "v_sub_f32 v71, v71, v8\n"
    "v_sub_f32 v80, 0, v8\n"
    "v_sub_f32 v81, 0, v8\n"
    "v_sub_f32 v82, 0, v8\n"
    "v_sub_f32 v83, 0, v8\n"
    "v_mov_b32 v5, v9\n"
    "v_sub_f32 v72, v72, v9\n"
    "v_sub_f32 v73, v73, v9\n"
    "v_sub_f32 v74, v74, v9\n"
    "v_sub_f32 v75, v75, v9\n"
    "v_sub_f32 v76, v76, v9\n"
    "v_sub_f32 v77, v77, v9\n"
    "v_sub_f32 v78, v78, v9\n"
    "v_sub_f32 v79, v79, v9\n"
    "v_sub_f32 v84, 0, v9\n"
    "v_sub_f32 v85, 0, v9\n"
    "v_sub_f32 v86, 0, v9\n"
    "v_sub_f32 v87, 0, v9\n"
    "v_exp_f32 v96, v64\n"
    "v_exp_f32 v97, v65\n"
    "v_exp_f32 v98, v66\n"
    "v_add_f32 v8, v96, v97\n"
    "v_cvt_pk_bf16_f32 v88, v96, v97\n"
    "v_exp_f32 v99, v67\n"
    "v_exp_f32 v104, v72\n"
    "v_add_f32 v8, v8, v98\n"
    "v_add_f32 v8, v8, v99\n"
    "v_cvt_pk_bf16_f32 v89, v98, v99\n"
    "v_exp_f32 v105, v73\n"
    "v_exp_f32 v106, v74\n"
    "v_add_f32 v9, v104, v105\n"
    "v_cvt_pk_bf16_f32 v92, v104, v105\n"
    "v_exp_f32 v107, v75\n"
    "v_exp_f32 v100, v68\n"
    "v_add_f32 v9, v9, v106\n"
    "v_add_f32 v9, v9, v107\n"
    "v_cvt_pk_bf16_f32 v93, v106, v107\n"
    "v_exp_f32 v101, v69\n"
    "v_exp_f32 v102, v70\n"
    "v_add_f32 v8, v8, v100\n"
    "v_add_f32 v8, v8, v101\n"
    "v_cvt_pk_bf16_f32 v90, v100, v101\n"
    "v_exp_f32 v103, v71\n"
    "v_exp_f32 v108, v76\n"
    "v_add_f32 v8, v8, v102\n"
    "v_add_f32 v8, v8, v103\n"
    "v_cvt_pk_bf16_f32 v91, v102, v103\n"
    "v_exp_f32 v109, v77\n"
    "v_exp_f32 v110, v78\n"
    "v_add_f32 v9, v9, v108\n"
    "v_add_f32 v9, v9, v109\n"
    "v_cvt_pk_bf16_f32 v94, v108, v109\n"
    "v_exp_f32 v111, v79\n"
    "s_nop 0\n"
    "v_add_f32 v9, v9, v110\n"
    "v_add_f32 v9, v9, v111\n"
    "v_cvt_pk_bf16_f32 v95, v110, v111\n"
    "v_add_u32 v3, s45, v2\n"
    "s_mov_b32 s38, s66\n"
    "v_add_f32 v6, v6, v8\n"
    "v_add_f32 v7, v7, v9\n"
    "s_waitcnt lgkmcnt(0)\n"
    "s_cmp_lt_u32 s38, s39\n"
    "s_cbranch_scc0 L_zerohi_%=\n"
    "v_mfma_f32_16x16x32_bf16 a[0:3], a[64:67], v[88:91], 0\n"
    "v_mfma_f32_16x16x32_bf16 a[32:35], a[64:67], v[92:95], 0\n"
    "ds_read_b128 a[64:67], v3 offset:0\n"
    "s_add_u32 s15, s15, 1\n"
    "v_mfma_f32_16x16x32_bf16 a[4:7], a[68:71], v[88:91], 0\n"
    "s_mov_b32 m0, s47\n"
    "s_add_u32 s46, s46, 1\n"
    "v_mfma_f32_16x16x32_bf16 a[36:39], a[68:71], v[92:95], 0\n"
    "ds_read_b128 a[68:71], v3 offset:4096\n"
    "buffer_load_dwordx4 v19, s[16:19], s48 offen offset:0 sc1 lds\n"
    "v_mfma_f32_16x16x32_bf16 a[8:11], a[72:75], v[88:91], 0\n"
    "s_cmp_ge_u32 s38, s42\n"
    "s_cselect_b32 s54, 0x4000, 0\n"
    "s_addc_u32 s66, s38, 1\n"
    "v_mfma_f32_16x16x32_bf16 a[40:43], a[72:75], v[92:95], 0\n"
    "ds_read_b128 a[72:75], v3 offset:1024\n"
    "s_add_u32 s47, s47, 0x4000\n"
    "v_mfma_f32_16x16x32_bf16 a[12:15], a[76:79], v[88:91], 0\n"
    "s_add_u32 s45, s45, s54\n"
    "s_add_u32 s45, s45, 0x4000\n"
    "buffer_load_dwordx4 v19, s[16:19], s48 offen offset:1024 sc1 lds\n"
    "v_mfma_f32_16x16x32_bf16 a[44:47], a[76:79], v[92:95], 0\n"
    "ds_read_b128 a[76:79], v3 offset:5120\n"
    "s_sub_u32 s58, s47, 163840\n"
    "v_mfma_f32_16x16x32_bf16 a[16:19], a[80:83], v[88:91], 0\n"
    "s_sub_u32 s53, s45, 163840\n"
    "v_mfma_f32_16x16x32_bf16 a[48:51], a[80:83], v[92:95], 0\n"
    "ds_read_b128 a[80:83], v3 offset:2048\n"
    "buffer_load_dwordx4 v19, s[16:19], s48 offen offset:2048 sc1 lds\n"
    "v_mfma_f32_16x16x32_bf16 a[20:23], a[84:87], v[88:91], 0\n"
    "s_cmp_ge_u32 s45, 163840\n"
    "s_cselect_b32 s45, s53, s45\n"
    "v_mfma_f32_16x16x32_bf16 a[52:55], a[84:87], v[92:95], 0\n"
    "ds_read_b128 a[84:87], v3 offset:6144\n"
    "v_mfma_f32_16x16x32_bf16 a[24:27], a[88:91], v[88:91], 0\n"
    "buffer_load_dwordx4 v19, s[16:19], s48 offen offset:3072 sc1 lds\n"
    "s_cmp_ge_u32 s47, 163840\n"
    "s_cselect_b32 s47, s58, s47\n"
    "v_mfma_f32_16x16x32_bf16 a[56:59], a[88:91], v[92:95], 0\n"
    "ds_read_b128 a[88:91], v3 offset:3072\n"
    "s_add_u32 s48, s48, 0x4000\n"
    "v_mfma_f32_16x16x32_bf16 a[28:31], a[92:95], v[88:91], 0\n"
    "s_mov_b32 m0, s47\n"
    "s_add_u32 s46, s46, 1\n"
    "v_mfma_f32_16x16x32_bf16 a[60:63], a[92:95], v[92:95], 0\n"
    "ds_read_b128 a[92:95], v3 offset:7168\n"
    "buffer_load_dwordx4 v19, s[16:19], s48 offen offset:0 sc1 lds\n"
    "s_waitcnt lgkmcnt(4)\n"
    "v_mfma_f32_16x16x32_bf16 v[64:67], a[64:67], v[32:35], v[80:83]\n"
    "s_sub_u32 s53, s14, s65\n"
    "s_cselect_b32 s53, 0, s53\n"
    "v_mfma_f32_16x16x32_bf16 v[72:75], a[64:67], v[48:51], v[84:87]\n"
    "ds_read_b128 a[64:67], v3 offset:8192\n"
    "s_add_u32 s47, s47, 0x4000\n"
    "v_mfma_f32_16x16x32_bf16 v[68:71], a[68:71], v[32:35], v[80:83]\n"
    "s_add_u32 s60, s14, s53\n"
    "s_add_u32 s60, s60, 10\n"
    "buffer_load_dwordx4 v19, s[16:19], s48 offen offset:1024 sc1 lds\n"
    "v_mfma_f32_16x16x32_bf16 v[76:79], a[68:71], v[48:51], v[84:87]\n"
    "ds_read_b128 a[68:71], v3 offset:9216\n"
    "s_sub_u32 s58, s47, 163840\n"
    "v_mfma_f32_16x16x32_bf16 v[64:67], a[72:75], v[36:39], v[64:67]\n"
    "s_min_u32 s60, s60, s49\n"
    "v_mfma_f32_16x16x32_bf16 v[72:75], a[72:75], v[52:55], v[72:75]\n"
    "ds_read_b128 a[72:75], v3 offset:10240\n"
    "buffer_load_dwordx4 v19, s[16:19], s48 offen offset:2048 sc1 lds\n"
    "v_mfma_f32_16x16x32_bf16 v[68:71], a[76:79], v[36:39], v[68:71]\n"
    "s_add_u32 s14, s14, 1\n"
    "v_mfma_f32_16x16x32_bf16 v[76:79], a[76:79], v[52:55], v[76:79]\n"
    "ds_read_b128 a[76:79], v3 offset:11264\n"
    "s_waitcnt lgkmcnt(4)\n"
    "v_mfma_f32_16x16x32_bf16 v[64:67], a[80:83], v[40:43], v[64:67]\n"
    "buffer_load_dwordx4 v19, s[16:19], s48 offen offset:3072 sc1 lds\n"
    "s_cmp_ge_u32 s47, 163840\n"
    "s_cselect_b32 s47, s58, s47\n"
    "v_mfma_f32_16x16x32_bf16 v[72:75], a[80:83], v[56:59], v[72:75]\n"
    "ds_read_b128 a[80:83], v3 offset:12288\n"
    "s_add_u32 s48, s48, 0x4000\n"
    "v_mfma_f32_16x16x32_bf16 v[68:71], a[84:87], v[40:43], v[68:71]\n"
    "v_mfma_f32_16x16x32_bf16 v[76:79], a[84:87], v[56:59], v[76:79]\n"
    "ds_read_b128 a[84:87], v3 offset:13312\n"
    "s_sub_u32 s56, s60, s46\n"
    "v_mfma_f32_16x16x32_bf16 v[64:67], a[88:91], v[44:47], v[64:67]\n"
    "s_cmp_gt_u32 s60, s46\n"
    "v_mfma_f32_16x16x32_bf16 v[72:75], a[88:91], v[60:63], v[72:75]\n"
    "ds_read_b128 a[88:91], v3 offset:14336\n"
    "s_cselect_b32 s56, s56, 0\n"
    "s_waitcnt vmcnt(8)\n"
    "s_barrier\n"
    "v_mfma_f32_16x16x32_bf16 v[68:71], a[92:95], v[44:47], v[68:71]\n"
    "v_mfma_f32_16x16x32_bf16 v[76:79], a[92:95], v[60:63], v[76:79]\n"
    "ds_read_b128 a[92:95], v3 offset:15360\n"
    "s_branch L_smhi_%=\n"
    "L_zerohi_%=:\n"
    "v_accvgpr_write_b32 a0, 0\n"
    "v_accvgpr_write_b32 a1, 0\n"
    "v_accvgpr_write_b32 a2, 0\n"
    "v_accvgpr_write_b32 a3, 0\n"
    "v_accvgpr_write_b32 a4, 0\n"
    "v_accvgpr_write_b32 a5, 0\n"
    "v_accvgpr_write_b32 a6, 0\n"
    "v_accvgpr_write_b32 a7, 0\n"
    "v_accvgpr_write_b32 a8, 0\n"
    "v_accvgpr_write_b32 a9, 0\n"
    "v_accvgpr_write_b32 a10, 0\n"
    "v_accvgpr_write_b32 a11, 0\n"
    "v_accvgpr_write_b32 a12, 0\n"
    "v_accvgpr_write_b32 a13, 0\n"
    "v_accvgpr_write_b32 a14, 0\n"
    "v_accvgpr_write_b32 a15, 0\n"
    "v_accvgpr_write_b32 a16, 0\n"
    "v_accvgpr_write_b32 a17, 0\n"
    "v_accvgpr_write_b32 a18, 0\n"
    "v_accvgpr_write_b32 a19, 0\n"
    "v_accvgpr_write_b32 a20, 0\n"
    "v_accvgpr_write_b32 a21, 0\n"
    "v_accvgpr_write_b32 a22, 0\n"
    "v_accvgpr_write_b32 a23, 0\n"
    "v_accvgpr_write_b32 a24, 0\n"
    "v_accvgpr_write_b32 a25, 0\n"
    "v_accvgpr_write_b32 a26, 0\n"
    "v_accvgpr_write_b32 a27, 0\n"
    "v_accvgpr_write_b32 a28, 0\n"
    "v_accvgpr_write_b32 a29, 0\n"
    "v_accvgpr_write_b32 a30, 0\n"
    "v_accvgpr_write_b32 a31, 0\n"
    "v_accvgpr_write_b32 a32, 0\n"
    "v_accvgpr_write_b32 a33, 0\n"
    "v_accvgpr_write_b32 a34, 0\n"
    "v_accvgpr_write_b32 a35, 0\n"
    "v_accvgpr_write_b32 a36, 0\n"
    "v_accvgpr_write_b32 a37, 0\n"
    "v_accvgpr_write_b32 a38, 0\n"
    "v_accvgpr_write_b32 a39, 0\n"
    "v_accvgpr_write_b32 a40, 0\n"
    "v_accvgpr_write_b32 a41, 0\n"
    "v_accvgpr_write_b32 a42, 0\n"
    "v_accvgpr_write_b32 a43, 0\n"
    "v_accvgpr_write_b32 a44, 0\n"
    "v_accvgpr_write_b32 a45, 0\n"
    "v_accvgpr_write_b32 a46, 0\n"
    "v_accvgpr_write_b32 a47, 0\n"
    "v_accvgpr_write_b32 a48, 0\n"
    "v_accvgpr_write_b32 a49, 0\n"
    "v_accvgpr_write_b32 a50, 0\n"
    "v_accvgpr_write_b32 a51, 0\n"
    "v_accvgpr_write_b32 a52, 0\n"
    "v_accvgpr_write_b32 a53, 0\n"
    "v_accvgpr_write_b32 a54, 0\n"
    "v_accvgpr_write_b32 a55, 0\n"
    "v_accvgpr_write_b32 a56, 0\n"
    "v_accvgpr_write_b32 a57, 0\n"
    "v_accvgpr_write_b32 a58, 0\n"
    "v_accvgpr_write_b32 a59, 0\n"
    "v_accvgpr_write_b32 a60, 0\n"
    "v_accvgpr_write_b32 a61, 0\n"
    "v_accvgpr_write_b32 a62, 0\n"
    "v_accvgpr_write_b32 a63, 0\n"
    "s_branch L_nexthi_%=\n"
    "L_bn0_%=:\n"
    "v_mfma_f32_16x16x32_bf16 a[0:3], a[64:67], v[88:91], a[0:3]\n"
    "v_mfma_f32_16x16x32_bf16 a[32:35], a[64:67], v[92:95], a[32:35]\n"
    "ds_read_b128 a[64:67], v3 offset:0\n"
    "s_add_u32 s15, s15, 1\n"
    "v_mfma_f32_16x16x32_bf16 a[4:7], a[68:71], v[88:91], a[4:7]\n"
    "v_mfma_f32_16x16x32_bf16 a[36:39], a[68:71], v[92:95], a[36:39]\n"
    "ds_read_b128 a[68:71], v3 offset:4096\n"
    "v_mfma_f32_16x16x32_bf16 a[8:11], a[72:75], v[88:91], a[8:11]\n"
    "s_cmp_ge_u32 s38, s42\n"
    "s_cselect_b32 s54, 0x4000, 0\n"
    "s_addc_u32 s66, s38, 1\n"
    "v_mfma_f32_16x16x32_bf16 a[40:43], a[72:75], v[92:95], a[40:43]\n"
    "ds_read_b128 a[72:75], v3 offset:1024\n"
    "v_mfma_f32_16x16x32_bf16 a[12:15], a[76:79], v[88:91], a[12:15]\n"
    "s_add_u32 s45, s45, s54\n"
    "s_add_u32 s45, s45, 0x4000\n"
    "v_mfma_f32_16x16x32_bf16 a[44:47], a[76:79], v[92:95], a[44:47]\n"
    "ds_read_b128 a[76:79], v3 offset:5120\n"
    "v_mfma_f32_16x16x32_bf16 a[16:19], a[80:83], v[88:91], a[16:19]\n"
    "s_sub_u32 s53, s45, 163840\n"
    "v_mfma_f32_16x16x32_bf16 a[48:51], a[80:83], v[92:95], a[48:51]\n"
    "ds_read_b128 a[80:83], v3 offset:2048\n"
    "v_mfma_f32_16x16x32_bf16 a[20:23], a[84:87], v[88:91], a[20:23]\n"
    "s_cmp_ge_u32 s45, 163840\n"
    "s_cselect_b32 s45, s53, s45\n"
    "v_mfma_f32_16x16x32_bf16 a[52:55], a[84:87], v[92:95], a[52:55]\n"
    "ds_read_b128 a[84:87], v3 offset:6144\n"
    "v_mfma_f32_16x16x32_bf16 a[24:27], a[88:91], v[88:91], a[24:27]\n"
    "v_mfma_f32_16x16x32_bf16 a[56:59], a[88:91], v[92:95], a[56:59]\n"
    "ds_read_b128 a[88:91], v3 offset:3072\n"
    "v_mfma_f32_16x16x32_bf16 a[28:31], a[92:95], v[88:91], a[28:31]\n"
    "v_mfma_f32_16x16x32_bf16 a[60:63], a[92:95], v[92:95], a[60:63]\n"
    "ds_read_b128 a[92:95], v3 offset:7168\n"
    "s_waitcnt lgkmcnt(4)\n"
    "v_mfma_f32_16x16x32_bf16 v[64:67], a[64:67], v[32:35], v[80:83]\n"
    "s_sub_u32 s53, s14, s65\n"
    "s_cselect_b32 s53, 0, s53\n"
    "v_mfma_f32_16x16x32_bf16 v[72:75], a[64:67], v[48:51], v[84:87]\n"
    "ds_read_b128 a[64:67], v3 offset:8192\n"
    "v_mfma_f32_16x16x32_bf16 v[68:71], a[68:71], v[32:35], v[80:83]\n"
    "s_add_u32 s60, s14, s53\n"
    "s_add_u32 s60, s60, 10\n"
    "v_mfma_f32_16x16x32_bf16 v[76:79], a[68:71], v[48:51], v[84:87]\n"
    "ds_read_b128 a[68:71], v3 offset:9216\n"
    "v_mfma_f32_16x16x32_bf16 v[64:67], a[72:75], v[36:39], v[64:67]\n"
    "s_min_u32 s60, s60, s49\n"
    "v_mfma_f32_16x16x32_bf16 v[72:75], a[72:75], v[52:55], v[72:75]\n"
    "ds_read_b128 a[72:75], v3 offset:10240\n"
    "v_mfma_f32_16x16x32_bf16 v[68:71], a[76:79], v[36:39], v[68:71]\n"
    "s_add_u32 s14, s14, 1\n"
    "v_mfma_f32_16x16x32_bf16 v[76:79], a[76:79], v[52:55], v[76:79]\n"
    "ds_read_b128 a[76:79], v3 offset:11264\n"
    "s_waitcnt lgkmcnt(4)\n"
    "v_mfma_f32_16x16x32_bf16 v[64:67], a[80:83], v[40:43], v[64:67]\n"
    "v_mfma_f32_16x16x32_bf16 v[72:75], a[80:83], v[56:59], v[72:75]\n"
    "ds_read_b128 a[80:83], v3 offset:12288\n"
    "v_mfma_f32_16x16x32_bf16 v[68:71], a[84:87], v[40:43], v[68:71]\n"
    "v_mfma_f32_16x16x32_bf16 v[76:79], a[84:87], v[56:59], v[76:79]\n"
    "ds_read_b128 a[84:87], v3 offset:13312\n"
    "s_sub_u32 s56, s60, s46\n"
    "v_mfma_f32_16x16x32_bf16 v[64:67], a[88:91], v[44:47], v[64:67]\n"
    "s_cmp_gt_u32 s60, s46\n"
    "v_mfma_f32_16x16x32_bf16 v[72:75], a[88:91], v[60:63], v[72:75]\n"
    "ds_read_b128 a[88:91], v3 offset:14336\n"
    "s_cselect_b32 s56, s56, 0\n"
    "s_waitcnt vmcnt(0)\n"
    "s_barrier\n"
    "v_mfma_f32_16x16x32_bf16 v[68:71], a[92:95], v[44:47], v[68:71]\n"
    "v_mfma_f32_16x16x32_bf16 v[76:79], a[92:95], v[60:63], v[76:79]\n"
    "ds_read_b128 a[92:95], v3 offset:15360\n"
    "s_branch L_smhi_%=\n"
    "L_bn1_%=:\n"
    "v_mfma_f32_16x16x32_bf16 a[0:3], a[64:67], v[88:91], a[0:3]\n"
    "v_mfma_f32_16x16x32_bf16 a[32:35], a[64:67], v[92:95], a[32:35]\n"
    "ds_read_b128 a[64:67], v3 offset:0\n"
    "s_add_u32 s15, s15, 1\n"
    "v_mfma_f32_16x16x32_bf16 a[4:7], a[68:71], v[88:91], a[4:7]\n"
    "s_mov_b32 m0, s47\n"
    "s_add_u32 s46, s46, 1\n"
    "v_mfma_f32_16x16x32_bf16 a[36:39], a[68:71], v[92:95], a[36:39]\n"
    "ds_read_b128 a[68:71], v3 offset:4096\n"
    "buffer_load_dwordx4 v19, s[16:19], s48 offen offset:0 sc1 lds\n"
    "v_mfma_f32_16x16x32_bf16 a[8:11], a[72:75], v[88:91], a[8:11]\n"
    "s_cmp_ge_u32 s38, s42\n"
    "s_cselect_b32 s54, 0x4000, 0\n"
    "s_addc_u32 s66, s38, 1\n"
    "v_mfma_f32_16x16x32_bf16 a[40:43], a[72:75], v[92:95], a[40:43]\n"
    "ds_read_b128 a[72:75], v3 offset:1024\n"
    "s_add_u32 s47, s47, 0x4000\n"
    "v_mfma_f32_16x16x32_bf16 a[12:15], a[76:79], v[88:91], a[12:15]\n"
    "s_add_u32 s45, s45, s54\n"
    "s_add_u32 s45, s45, 0x4000\n"
    "buffer_load_dwordx4 v19, s[16:19], s48 offen offset:1024 sc1 lds\n"
    "v_mfma_f32_16x16x32_bf16 a[44:47], a[76:79], v[92:95], a[44:47]\n"
    "ds_read_b128 a[76:79], v3 offset:5120\n"
    "s_sub_u32 s58, s47, 163840\n"
    "v_mfma_f32_16x16x32_bf16 a[16:19], a[80:83], v[88:91], a[16:19]\n"
    "s_sub_u32 s53, s45, 163840\n"
    "v_mfma_f32_16x16x32_bf16 a[48:51], a[80:83], v[92:95], a[48:51]\n"
    "ds_read_b128 a[80:83], v3 offset:2048\n"
    "buffer_load_dwordx4 v19, s[16:19], s48 offen offset:2048 sc1 lds\n"
    "v_mfma_f32_16x16x32_bf16 a[20:23], a[84:87], v[88:91], a[20:23]\n"
    "s_cmp_ge_u32 s45, 163840\n"
    "s_cselect_b32 s45, s53, s45\n"
    "v_mfma_f32_16x16x32_bf16 a[52:55], a[84:87], v[92:95], a[52:55]\n"
    "ds_read_b128 a[84:87], v3 offset:6144\n"
    "v_mfma_f32_16x16x32_bf16 a[24:27], a[88:91], v[88:91], a[24:27]\n"
    "buffer_load_dwordx4 v19, s[16:19], s48 offen offset:3072 sc1 lds\n"
    "s_cmp_ge_u32 s47, 163840\n"
    "s_cselect_b32 s47, s58, s47\n"
    "v_mfma_f32_16x16x32_bf16 a[56:59], a[88:91], v[92:95], a[56:59]\n"
    "ds_read_b128 a[88:91], v3 offset:3072\n"
    "s_add_u32 s48, s48, 0x4000\n"
    "v_mfma_f32_16x16x32_bf16 a[28:31], a[92:95], v[88:91], a[28:31]\n"
    "v_mfma_f32_16x16x32_bf16 a[60:63], a[92:95], v[92:95], a[60:63]\n"
    "ds_read_b128 a[92:95], v3 offset:7168\n"
    "s_waitcnt lgkmcnt(4)\n"
    "v_mfma_f32_16x16x32_bf16 v[64:67], a[64:67], v[32:35], v[80:83]\n"
    "s_sub_u32 s53, s14, s65\n"
    "s_cselect_b32 s53, 0, s53\n"
    "v_mfma_f32_16x16x32_bf16 v[72:75], a[64:67], v[48:51], v[84:87]\n"
    "ds_read_b128 a[64:67], v3 offset:8192\n"
    "v_mfma_f32_16x16x32_bf16 v[68:71], a[68:71], v[32:35], v[80:83]\n"
    "s_add_u32 s60, s14, s53\n"
    "s_add_u32 s60, s60, 10\n"
    "v_mfma_f32_16x16x32_bf16 v[76:79], a[68:71], v[48:51], v[84:87]\n"
    "ds_read_b128 a[68:71], v3 offset:9216\n"
    "v_mfma_f32_16x16x32_bf16 v[64:67], a[72:75], v[36:39], v[64:67]\n"
    "s_min_u32 s60, s60, s49\n"
    "v_mfma_f32_16x16x32_bf16 v[72:75], a[72:75], v[52:55], v[72:75]\n"
    "ds_read_b128 a[72:75], v3 offset:10240\n"
    "v_mfma_f32_16x16x32_bf16 v[68:71], a[76:79], v[36:39], v[68:71]\n"
    "s_add_u32 s14, s14, 1\n"
    "v_mfma_f32_16x16x32_bf16 v[76:79], a[76:79], v[52:55], v[76:79]\n"
    "ds_read_b128 a[76:79], v3 offset:11264\n"
    "s_waitcnt lgkmcnt(4)\n"
    "v_mfma_f32_16x16x32_bf16 v[64:67], a[80:83], v[40:43], v[64:67]\n"
    "v_mfma_f32_16x16x32_bf16 v[72:75], a[80:83], v[56:59], v[72:75]\n"
    "ds_read_b128 a[80:83], v3 offset:12288\n"
    "v_mfma_f32_16x16x32_bf16 v[68:71], a[84:87], v[40:43], v[68:71]\n"
    "v_mfma_f32_16x16x32_bf16 v[76:79], a[84:87], v[56:59], v[76:79]\n"
    "ds_read_b128 a[84:87], v3 offset:13312\n"
    "s_sub_u32 s56, s60, s46\n"
    "v_mfma_f32_16x16x32_bf16 v[64:67], a[88:91], v[44:47], v[64:67]\n"
    "s_cmp_gt_u32 s60, s46\n"
    "v_mfma_f32_16x16x32_bf16 v[72:75], a[88:91], v[60:63], v[72:75]\n"
    "ds_read_b128 a[88:91], v3 offset:14336\n"
    "s_cselect_b32 s56, s56, 0\n"
    "s_waitcnt vmcnt(4)\n"
    "s_barrier\n"
    "v_mfma_f32_16x16x32_bf16 v[68:71], a[92:95], v[44:47], v[68:71]\n"
    "v_mfma_f32_16x16x32_bf16 v[76:79], a[92:95], v[60:63], v[76:79]\n"
    "ds_read_b128 a[92:95], v3 offset:15360\n"
    "s_branch L_smhi_%=\n"
    "L_loophi_%=:\n"
    "v_mfma_f32_16x16x32_bf16 a[0:3], a[64:67], v[88:91], a[0:3]\n"
    "v_mfma_f32_16x16x32_bf16 a[32:35], a[64:67], v[92:95], a[32:35]\n"
    "ds_read_b128 a[64:67], v3 offset:0\n"
    "s_add_u32 s15, s15, 1\n"
    "v_mfma_f32_16x16x32_bf16 a[4:7], a[68:71], v[88:91], a[4:7]\n"
    "s_mov_b32 m0, s47\n"
    "s_add_u32 s46, s46, 1\n"
    "v_mfma_f32_16x16x32_bf16 a[36:39], a[68:71], v[92:95], a[36:39]\n"
    "ds_read_b128 a[68:71], v3 offset:4096\n"
    "buffer_load_dwordx4 v19, s[16:19], s48 offen offset:0 sc1 lds\n"
    "v_mfma_f32_16x16x32_bf16 a[8:11], a[72:75], v[88:91], a[8:11]\n"
    "s_cmp_ge_u32 s38, s42\n"
    "s_cselect_b32 s54, 0x4000, 0\n"
    "s_addc_u32 s66, s38, 1\n"
    "v_mfma_f32_16x16x32_bf16 a[40:43], a[72:75], v[92:95], a[40:43]\n"
    "ds_read_b128 a[72:75], v3 offset:1024\n"
    "s_add_u32 s47, s47, 0x4000\n"
    "v_mfma_f32_16x16x32_bf16 a[12:15], a[76:79], v[88:91], a[12:15]\n"
    "s_add_u32 s45, s45, s54\n"
    "s_add_u32 s45, s45, 0x4000\n"
    "buffer_load_dwordx4 v19, s[16:19], s48 offen offset:1024 sc1 lds\n"
    "v_mfma_f32_16x16x32_bf16 a[44:47], a[76:79], v[92:95], a[44:47]\n"
    "ds_read_b128 a[76:79], v3 offset:5120\n"
    "s_sub_u32 s58, s47, 163840\n"
    "v_mfma_f32_16x16x32_bf16 a[16:19], a[80:83], v[88:91], a[16:19]\n"
    "s_sub_u32 s53, s45, 163840\n"
    "v_mfma_f32_16x16x32_bf16 a[48:51], a[80:83], v[92:95], a[48:51]\n"
    "ds_read_b128 a[80:83], v3 offset:2048\n"
    "buffer_load_dwordx4 v19, s[16:19], s48 offen offset:2048 sc1 lds\n"
    "v_mfma_f32_16x16x32_bf16 a[20:23], a[84:87], v[88:91], a[20:23]\n"
    "s_cmp_ge_u32 s45, 163840\n"
    "s_cselect_b32 s45, s53, s45\n"
    "v_mfma_f32_16x16x32_bf16 a[52:55], a[84:87], v[92:95], a[52:55]\n"
    "ds_read_b128 a[84:87], v3 offset:6144\n"
    "v_mfma_f32_16x16x32_bf16 a[24:27], a[88:91], v[88:91], a[24:27]\n"
    "buffer_load_dwordx4 v19, s[16:19], s48 offen offset:3072 sc1 lds\n"
    "s_cmp_ge_u32 s47, 163840\n"
    "s_cselect_b32 s47, s58, s47\n"
    "v_mfma_f32_16x16x32_bf16 a[56:59], a[88:91], v[92:95], a[56:59]\n"
    "ds_read_b128 a[88:91], v3 offset:3072\n"
    "s_add_u32 s48, s48, 0x4000\n"
    "v_mfma_f32_16x16x32_bf16 a[28:31], a[92:95], v[88:91], a[28:31]\n"
    "s_mov_b32 m0, s47\n"
    "s_add_u32 s46, s46, 1\n"
    "v_mfma_f32_16x16x32_bf16 a[60:63], a[92:95], v[92:95], a[60:63]\n"
    "ds_read_b128 a[92:95], v3 offset:7168\n"
    "buffer_load_dwordx4 v19, s[16:19], s48 offen offset:0 sc1 lds\n"
    "s_waitcnt lgkmcnt(4)\n"
    "v_mfma_f32_16x16x32_bf16 v[64:67], a[64:67], v[32:35], v[80:83]\n"
    "s_sub_u32 s53, s14, s65\n"
    "s_cselect_b32 s53, 0, s53\n"
    "v_mfma_f32_16x16x32_bf16 v[72:75], a[64:67], v[48:51], v[84:87]\n"
    "ds_read_b128 a[64:67], v3 offset:8192\n"
    "s_add_u32 s47, s47, 0x4000\n"
    "v_mfma_f32_16x16x32_bf16 v[68:71], a[68:71], v[32:35], v[80:83]\n"
    "s_add_u32 s60, s14, s53\n"
    "s_add_u32 s60, s60, 10\n"
    "buffer_load_dwordx4 v19, s[16:19], s48 offen offset:1024 sc1 lds\n"
    "v_mfma_f32_16x16x32_bf16 v[76:79], a[68:71], v[48:51], v[84:87]\n"
    "ds_read_b128 a[68:71], v3 offset:9216\n"
    "s_sub_u32 s58, s47, 163840\n"
    "v_mfma_f32_16x16x32_bf16 v[64:67], a[72:75], v[36:39], v[64:67]\n"
    "s_min_u32 s60, s60, s49\n"
    "v_mfma_f32_16x16x32_bf16 v[72:75], a[72:75], v[52:55], v[72:75]\n"
    "ds_read_b128 a[72:75], v3 offset:10240\n"
    "buffer_load_dwordx4 v19, s[16:19], s48 offen offset:2048 sc1 lds\n"
    "v_mfma_f32_16x16x32_bf16 v[68:71], a[76:79], v[36:39], v[68:71]\n"
    "s_add_u32 s14, s14, 1\n"
    "v_mfma_f32_16x16x32_bf16 v[76:79], a[76:79], v[52:55], v[76:79]\n"
    "ds_read_b128 a[76:79], v3 offset:11264\n"
    "s_waitcnt lgkmcnt(4)\n"
    "v_mfma_f32_16x16x32_bf16 v[64:67], a[80:83], v[40:43], v[64:67]\n"
    "buffer_load_dwordx4 v19, s[16:19], s48 offen offset:3072 sc1 lds\n"
    "s_cmp_ge_u32 s47, 163840\n"
    "s_cselect_b32 s47, s58, s47\n"
    "v_mfma_f32_16x16x32_bf16 v[72:75], a[80:83], v[56:59], v[72:75]\n"
    "ds_read_b128 a[80:83], v3 offset:12288\n"
    "s_add_u32 s48, s48, 0x4000\n"
    "v_mfma_f32_16x16x32_bf16 v[68:71], a[84:87], v[40:43], v[68:71]\n"
    "v_mfma_f32_16x16x32_bf16 v[76:79], a[84:87], v[56:59], v[76:79]\n"
    "ds_read_b128 a[84:87], v3 offset:13312\n"
    "s_sub_u32 s56, s60, s46\n"
    "v_mfma_f32_16x16x32_bf16 v[64:67], a[88:91], v[44:47], v[64:67]\n"
    "s_cmp_gt_u32 s60, s46\n"
    "v_mfma_f32_16x16x32_bf16 v[72:75], a[88:91], v[60:63], v[72:75]\n"
    "ds_read_b128 a[88:91], v3 offset:14336\n"
    "s_cselect_b32 s56, s56, 0\n"
    "s_waitcnt vmcnt(8)\n"
    "s_barrier\n"
    "v_mfma_f32_16x16x32_bf16 v[68:71], a[92:95], v[44:47], v[68:71]\n"
    "v_mfma_f32_16x16x32_bf16 v[76:79], a[92:95], v[60:63], v[76:79]\n"
    "ds_read_b128 a[92:95], v3 offset:15360\n"
    "L_smhi_%=:\n"
    "v_exp_f32 v96, v64\n"
    "v_exp_f32 v97, v65\n"
    "v_exp_f32 v98, v66\n"
    "v_add_f32 v8, v96, v97\n"
    "v_cvt_pk_bf16_f32 v88, v96, v97\n"
    "v_exp_f32 v99, v67\n"
    "v_exp_f32 v104, v72\n"
    "v_add_f32 v8, v8, v98\n"
    "v_add_f32 v8, v8, v99\n"
    "v_cvt_pk_bf16_f32 v89, v98, v99\n"
    "v_exp_f32 v105, v73\n"
    "v_exp_f32 v106, v74\n"
    "v_add_f32 v9, v104, v105\n"
    "v_cvt_pk_bf16_f32 v92, v104, v105\n"
    "v_exp_f32 v107, v75\n"
    "v_exp_f32 v100, v68\n"
    "v_add_f32 v9, v9, v106\n"
    "v_add_f32 v9, v9, v107\n"
    "v_cvt_pk_bf16_f32 v93, v106, v107\n"
    "v_exp_f32 v101, v69\n"
    "v_exp_f32 v102, v70\n"
    "v_add_f32 v8, v8, v100\n"
    "v_add_f32 v8, v8, v101\n"
    "v_cvt_pk_bf16_f32 v90, v100, v101\n"
    "v_exp_f32 v103, v71\n"
    "v_exp_f32 v108, v76\n"
    "v_add_f32 v8, v8, v102\n"
    "v_add_f32 v8, v8, v103\n"
    "v_cvt_pk_bf16_f32 v91, v102, v103\n"
    "v_exp_f32 v109, v77\n"
    "v_exp_f32 v110, v78\n"
    "v_add_f32 v9, v9, v108\n"
    "v_add_f32 v9, v9, v109\n"
    "v_cvt_pk_bf16_f32 v94, v108, v109\n"
    "v_exp_f32 v111, v79\n"
    "s_nop 0\n"
    "v_add_f32 v9, v9, v110\n"
    "v_add_f32 v9, v9, v111\n"
    "v_cvt_pk_bf16_f32 v95, v110, v111\n"
    "v_max_f32 v10, v8, v9\n"
    "v_cmp_lt_f32 vcc, s43, v10\n"
    "v_add_u32 v3, s45, v2\n"
    "s_mov_b32 s38, s66\n"
    "s_cbranch_vccnz L_rare_%=\n"
    "L_ret_hi_%=:\n"
    "v_add_f32 v6, v6, v8\n"
    "v_add_f32 v7, v7, v9\n"
    "s_waitcnt lgkmcnt(0)\n"
    "L_nexthi_%=:\n"
    "s_cmp_ge_u32 s38, s39\n"
    "s_cbranch_scc1 L_lasthi_%=\n"
    "s_cmp_eq_u32 s56, 1\n"
    "s_cbranch_scc1 L_bn1_%=\n"
    "s_cmp_gt_u32 s56, 1\n"
    "s_cbranch_scc1 L_loophi_%=\n"
    "s_branch L_bn0_%=\n"
    "L_lasthi_%=:\n"
    "s_cmp_gt_u32 s38, s39\n"
    "s_cbranch_scc1 L_finhi_%=\n"
    "v_mov_b32 v10, 0xff800000\n"
    "v_cmp_gt_i32 s[52:53], 0, v18\n"
    "v_cmp_gt_i32 s[54:55], 1, v18\n"
    "v_cmp_gt_i32 s[56:57], 2, v18\n"
    "v_cmp_gt_i32 s[58:59], 3, v18\n"
    "v_cndmask_b32 v64, v80, v10, s[52:53]\n"
    "v_cndmask_b32 v65, v80, v10, s[54:55]\n"
    "v_cndmask_b32 v66, v80, v10, s[56:57]\n"
    "v_cndmask_b32 v67, v80, v10, s[58:59]\n"
    "v_cmp_gt_i32 s[52:53], 16, v18\n"
    "v_cmp_gt_i32 s[54:55], 17, v18\n"
    "v_cmp_gt_i32 s[56:57], 18, v18\n"
    "v_cmp_gt_i32 s[58:59], 19, v18\n"
    "v_cndmask_b32 v68, v80, v10, s[52:53]\n"
    "v_cndmask_b32 v69, v80, v10, s[54:55]\n"
    "v_cndmask_b32 v70, v80, v10, s[56:57]\n"
    "v_cndmask_b32 v71, v80, v10, s[58:59]\n"
    "v_cmp_gt_i32 s[52:53], -16, v18\n"
    "v_cmp_gt_i32 s[54:55], -15, v18\n"
    "v_cmp_gt_i32 s[56:57], -14, v18\n"
    "v_cmp_gt_i32 s[58:59], -13, v18\n"
    "v_cndmask_b32 v72, v84, v10, s[52:53]\n"
    "v_cndmask_b32 v73, v84, v10, s[54:55]\n"
    "v_cndmask_b32 v74, v84, v10, s[56:57]\n"
    "v_cndmask_b32 v75, v84, v10, s[58:59]\n"
    "v_cmp_gt_i32 s[52:53], 0, v18\n"
    "v_cmp_gt_i32 s[54:55], 1, v18\n"
    "v_cmp_gt_i32 s[56:57], 2, v18\n"
    "v_cmp_gt_i32 s[58:59], 3, v18\n"
    "v_cndmask_b32 v76, v84, v10, s[52:53]\n"
    "v_cndmask_b32 v77, v84, v10, s[54:55]\n"
    "v_cndmask_b32 v78, v84, v10, s[56:57]\n"
    "v_cndmask_b32 v79, v84, v10, s[58:59]\n"
    "v_mfma_f32_16x16x32_bf16 a[0:3], a[64:67], v[88:91], a[0:3]\n"
    "v_mfma_f32_16x16x32_bf16 a[32:35], a[64:67], v[92:95], a[32:35]\n"
    "ds_read_b128 a[64:67], v3 offset:0\n"
    "s_add_u32 s15, s15, 1\n"
    "v_mfma_f32_16x16x32_bf16 a[4:7], a[68:71], v[88:91], a[4:7]\n"
    "v_mfma_f32_16x16x32_bf16 a[36:39], a[68:71], v[92:95], a[36:39]\n"
    "ds_read_b128 a[68:71], v3 offset:4096\n"
    "v_mfma_f32_16x16x32_bf16 a[8:11], a[72:75], v[88:91], a[8:11]\n"
    "s_cmp_ge_u32 s38, s42\n"
    "s_cselect_b32 s54, 0x4000, 0\n"
    "s_addc_u32 s66, s38, 1\n"
    "v_mfma_f32_16x16x32_bf16 a[40:43], a[72:75], v[92:95], a[40:43]\n"
    "ds_read_b128 a[72:75], v3 offset:1024\n"
    "v_mfma_f32_16x16x32_bf16 a[12:15], a[76:79], v[88:91], a[12:15]\n"
    "s_add_u32 s45, s45, s54\n"
    "s_add_u32 s45, s45, 0x4000\n"
    "v_mfma_f32_16x16x32_bf16 a[44:47], a[76:79], v[92:95], a[44:47]\n"
    "ds_read_b128 a[76:79], v3 offset:5120\n"
    "v_mfma_f32_16x16x32_bf16 a[16:19], a[80:83], v[88:91], a[16:19]\n"
    "s_sub_u32 s53, s45, 163840\n"
    "v_mfma_f32_16x16x32_bf16 a[48:51], a[80:83], v[92:95], a[48:51]\n"
    "ds_read_b128 a[80:83], v3 offset:2048\n"
    "v_mfma_f32_16x16x32_bf16 a[20:23], a[84:87], v[88:91], a[20:23]\n"
    "s_cmp_ge_u32 s45, 163840\n"
    "s_cselect_b32 s45, s53, s45\n"
    "v_mfma_f32_16x16x32_bf16 a[52:55], a[84:87], v[92:95], a[52:55]\n"
    "ds_read_b128 a[84:87], v3 offset:6144\n"
    "v_mfma_f32_16x16x32_bf16 a[24:27], a[88:91], v[88:91], a[24:27]\n"
    "v_mfma_f32_16x16x32_bf16 a[56:59], a[88:91], v[92:95], a[56:59]\n"
    "ds_read_b128 a[88:91], v3 offset:3072\n"
    "v_mfma_f32_16x16x32_bf16 a[28:31], a[92:95], v[88:91], a[28:31]\n"
    "v_mfma_f32_16x16x32_bf16 a[60:63], a[92:95], v[92:95], a[60:63]\n"
    "ds_read_b128 a[92:95], v3 offset:7168\n"
    "s_waitcnt lgkmcnt(4)\n"
    "v_mfma_f32_16x16x32_bf16 v[64:67], a[64:67], v[32:35], v[64:67]\n"
    "s_sub_u32 s53, s14, s65\n"
    "s_cselect_b32 s53, 0, s53\n"
    "v_mfma_f32_16x16x32_bf16 v[72:75], a[64:67], v[48:51], v[72:75]\n"
    "ds_read_b128 a[64:67], v3 offset:8192\n"
    "v_mfma_f32_16x16x32_bf16 v[68:71], a[68:71], v[32:35], v[68:71]\n"
    "s_add_u32 s60, s14, s53\n"
    "s_add_u32 s60, s60, 10\n"
    "v_mfma_f32_16x16x32_bf16 v[76:79], a[68:71], v[48:51], v[76:79]\n"
    "ds_read_b128 a[68:71], v3 offset:9216\n"
    "v_mfma_f32_16x16x32_bf16 v[64:67], a[72:75], v[36:39], v[64:67]\n"
    "s_min_u32 s60, s60, s49\n"
    "v_mfma_f32_16x16x32_bf16 v[72:75], a[72:75], v[52:55], v[72:75]\n"
    "ds_read_b128 a[72:75], v3 offset:10240\n"
    "v_mfma_f32_16x16x32_bf16 v[68:71], a[76:79], v[36:39], v[68:71]\n"
    "s_add_u32 s14, s14, 1\n"
    "v_mfma_f32_16x16x32_bf16 v[76:79], a[76:79], v[52:55], v[76:79]\n"
    "ds_read_b128 a[76:79], v3 offset:11264\n"
    "s_waitcnt lgkmcnt(4)\n"
    "v_mfma_f32_16x16x32_bf16 v[64:67], a[80:83], v[40:43], v[64:67]\n"
    "v_mfma_f32_16x16x32_bf16 v[72:75], a[80:83], v[56:59], v[72:75]\n"
    "ds_read_b128 a[80:83], v3 offset:12288\n"
    "v_mfma_f32_16x16x32_bf16 v[68:71], a[84:87], v[40:43], v[68:71]\n"
    "v_mfma_f32_16x16x32_bf16 v[76:79], a[84:87], v[56:59], v[76:79]\n"
    "ds_read_b128 a[84:87], v3 offset:13312\n"
    "s_sub_u32 s56, s60, s46\n"
    "v_mfma_f32_16x16x32_bf16 v[64:67], a[88:91], v[44:47], v[64:67]\n"
    "s_cmp_gt_u32 s60, s46\n"
    "v_mfma_f32_16x16x32_bf16 v[72:75], a[88:91], v[60:63], v[72:75]\n"
    "ds_read_b128 a[88:91], v3 offset:14336\n"
    "s_cselect_b32 s56, s56, 0\n"
    "s_waitcnt vmcnt(0)\n"
    "s_barrier\n"
    "v_mfma_f32_16x16x32_bf16 v[68:71], a[92:95], v[44:47], v[68:71]\n"
    "v_mfma_f32_16x16x32_bf16 v[76:79], a[92:95], v[60:63], v[76:79]\n"
    "ds_read_b128 a[92:95], v3 offset:15360\n"
    "s_branch L_smhi_%=\n"
    "L_finhi_%=:\n"
    "v_mfma_f32_16x16x32_bf16 a[0:3], a[64:67], v[88:91], a[0:3]\n"
    "v_mfma_f32_16x16x32_bf16 a[32:35], a[64:67], v[92:95], a[32:35]\n"
    "v_mfma_f32_16x16x32_bf16 a[4:7], a[68:71], v[88:91], a[4:7]\n"
    "v_mfma_f32_16x16x32_bf16 a[36:39], a[68:71], v[92:95], a[36:39]\n"
    "v_mfma_f32_16x16x32_bf16 a[8:11], a[72:75], v[88:91], a[8:11]\n"
    "v_mfma_f32_16x16x32_bf16 a[40:43], a[72:75], v[92:95], a[40:43]\n"
    "v_mfma_f32_16x16x32_bf16 a[12:15], a[76:79], v[88:91], a[12:15]\n"
    "v_mfma_f32_16x16x32_bf16 a[44:47], a[76:79], v[92:95], a[44:47]\n"
    "v_mfma_f32_16x16x32_bf16 a[16:19], a[80:83], v[88:91], a[16:19]\n"
    "v_mfma_f32_16x16x32_bf16 a[48:51], a[80:83], v[92:95], a[48:51]\n"
    "v_mfma_f32_16x16x32_bf16 a[20:23], a[84:87], v[88:91], a[20:23]\n"
    "v_mfma_f32_16x16x32_bf16 a[52:55], a[84:87], v[92:95], a[52:55]\n"
    "v_mfma_f32_16x16x32_bf16 a[24:27], a[88:91], v[88:91], a[24:27]\n"
    "v_mfma_f32_16x16x32_bf16 a[56:59], a[88:91], v[92:95], a[56:59]\n"
    "v_mfma_f32_16x16x32_bf16 a[28:31], a[92:95], v[88:91], a[28:31]\n"
    "v_mfma_f32_16x16x32_bf16 a[60:63], a[92:95], v[92:95], a[60:63]\n"
    "s_branch L_fin_%=\n"
    "L_b0lo_%=:\n"
    "s_barrier\n"
    "ds_read_b128 a[64:67], v3 offset:0\n"
    "s_add_u32 s15, s15, 1\n"
    "ds_read_b128 a[68:71], v3 offset:4096\n"
    "s_cmp_ge_u32 s38, s42\n"
    "s_cselect_b32 s54, 0x4000, 0\n"
    "s_addc_u32 s66, s38, 1\n"
    "ds_read_b128 a[72:75], v3 offset:1024\n"
    "s_add_u32 s45, s45, s54\n"
    "s_add_u32 s45, s45, 0x4000\n"
    "ds_read_b128 a[76:79], v3 offset:5120\n"
    "s_sub_u32 s53, s45, 163840\n"
    "ds_read_b128 a[80:83], v3 offset:2048\n"
    "s_cmp_ge_u32 s45, 163840\n"
    "s_cselect_b32 s45, s53, s45\n"
    "ds_read_b128 a[84:87], v3 offset:6144\n"
    "ds_read_b128 a[88:91], v3 offset:3072\n"
    "ds_read_b128 a[92:95], v3 offset:7168\n"
    "s_waitcnt lgkmcnt(4)\n"
    "v_mfma_f32_16x16x32_bf16 v[64:67], a[64:67], v[32:35], v[64:67]\n"
    "s_add_u32 s14, s14, 1\n"
    "v_mfma_f32_16x16x32_bf16 v[72:75], a[64:67], v[48:51], v[72:75]\n"
    "ds_read_b128 a[64:67], v3 offset:8192\n"
    "v_mfma_f32_16x16x32_bf16 v[68:71], a[68:71], v[32:35], v[68:71]\n"
    "v_mfma_f32_16x16x32_bf16 v[76:79], a[68:71], v[48:51], v[76:79]\n"
    "ds_read_b128 a[68:71], v3 offset:9216\n"
    "v_mfma_f32_16x16x32_bf16 v[64:67], a[72:75], v[36:39], v[64:67]\n"
    "v_mfma_f32_16x16x32_bf16 v[72:75], a[72:75], v[52:55], v[72:75]\n"
    "ds_read_b128 a[72:75], v3 offset:10240\n"
    "v_mfma_f32_16x16x32_bf16 v[68:71], a[76:79], v[36:39], v[68:71]\n"
    "v_mfma_f32_16x16x32_bf16 v[76:79], a[76:79], v[52:55], v[76:79]\n"
    "ds_read_b128 a[76:79], v3 offset:11264\n"
    "s_waitcnt lgkmcnt(4)\n"
    "v_mfma_f32_16x16x32_bf16 v[64:67], a[80:83], v[40:43], v[64:67]\n"
    "v_mfma_f32_16x16x32_bf16 v[72:75], a[80:83], v[56:59], v[72:75]\n"
    "ds_read_b128 a[80:83], v3 offset:12288\n"
    "v_mfma_f32_16x16x32_bf16 v[68:71], a[84:87], v[40:43], v[68:71]\n"
    "v_mfma_f32_16x16x32_bf16 v[76:79], a[84:87], v[56:59], v[76:79]\n"
    "ds_read_b128 a[84:87], v3 offset:13312\n"
    "v_mfma_f32_16x16x32_bf16 v[64:67], a[88:91], v[44:47], v[64:67]\n"
    "v_mfma_f32_16x16x32_bf16 v[72:75], a[88:91], v[60:63], v[72:75]\n"
    "ds_read_b128 a[88:91], v3 offset:14336\n"
    "v_mfma_f32_16x16x32_bf16 v[68:71], a[92:95], v[44:47], v[68:71]\n"
    "v_mfma_f32_16x16x32_bf16 v[76:79], a[92:95], v[60:63], v[76:79]\n"
    "ds_read_b128 a[92:95], v3 offset:15360\n"
    "s_nop 2\n"
    "v_max3_f32 v8, v64, v65, v66\n"
    "v_max3_f32 v9, v72, v73, v74\n"
    "s_nop 0\n"
    "v_max3_f32 v8, v8, v67, v68\n"
    "v_max3_f32 v9, v9, v75, v76\n"
    "v_max3_f32 v8, v8, v69, v70\n"
    "v_max3_f32 v9, v9, v77, v78\n"
    "v_max_f32 v8, v8, v71\n"
    "v_max_f32 v9, v9, v79\n"
    "v_mov_b32 v10, v8\n"
    "s_nop 1\n"
    "v_permlane16_swap_b32 v8, v10\n"
    "v_max_f32 v8, v8, v10\n"
    "v_mov_b32 v10, v8\n"
    "s_nop 1\n"
    "v_permlane32_swap_b32 v8, v10\n"
    "v_max_f32 v8, v8, v10\n"
    "v_mov_b32 v10, v9\n"
    "s_nop 1\n"
    "v_permlane16_swap_b32 v9, v10\n"
    "v_max_f32 v9, v9, v10\n"
    "v_mov_b32 v10, v9\n"
    "s_nop 1\n"
    "v_permlane32_swap_b32 v9, v10\n"
    "v_max_f32 v9, v9, v10\n"
    "v_mov_b32 v4, v8\n"
    "v_sub_f32 v64, v64, v8\n"
    "v_sub_f32 v65, v65, v8\n"
    "v_sub_f32 v66, v66, v8\n"
    "v_sub_f32 v67, v67, v8\n"
    "v_sub_f32 v68, v68, v8\n"
    "v_sub_f32 v69, v69, v8\n"
    "v_sub_f32 v70, v70, v8\n"
    "v_sub_f32 v71, v71, v8\n"
    "v_sub_f32 v80, 0, v8\n"
    "v_sub_f32 v81, 0, v8\n"
    "v_sub_f32 v82, 0, v8\n"
    "v_sub_f32 v83, 0, v8\n"
    "v_mov_b32 v5, v9\n"
    "v_sub_f32 v72, v72, v9\n"
    "v_sub_f32 v73, v73, v9\n"
    "v_sub_f32 v74, v74, v9\n"
    "v_sub_f32 v75, v75, v9\n"
    "v_sub_f32 v76, v76, v9\n"
    "v_sub_f32 v77, v77, v9\n"
    "v_sub_f32 v78, v78, v9\n"
    "v_sub_f32 v79, v79, v9\n"
    "v_sub_f32 v84, 0, v9\n"
    "v_sub_f32 v85, 0, v9\n"
    "v_sub_f32 v86, 0, v9\n"
    "v_sub_f32 v87, 0, v9\n"
    "v_exp_f32 v96, v64\n"
    "v_exp_f32 v97, v65\n"
    "v_exp_f32 v98, v66\n"
    "v_add_f32 v8, v96, v97\n"
    "v_cvt_pk_bf16_f32 v88, v96, v97\n"
    "v_exp_f32 v99, v67\n"
    "v_exp_f32 v104, v72\n"
    "v_add_f32 v8, v8, v98\n"
    "v_add_f32 v8, v8, v99\n"
    "v_cvt_pk_bf16_f32 v89, v98, v99\n"
    "v_exp_f32 v105, v73\n"
    "v_exp_f32 v106, v74\n"
    "v_add_f32 v9, v104, v105\n"
    "v_cvt_pk_bf16_f32 v92, v104, v105\n"
    "v_exp_f32 v107, v75\n"
    "v_exp_f32 v100, v68\n"
    "v_add_f32 v9, v9, v106\n"
    "v_add_f32 v9, v9, v107\n"
    "v_cvt_pk_bf16_f32 v93, v106, v107\n"
    "v_exp_f32 v101, v69\n"
    "v_exp_f32 v102, v70\n"
    "v_add_f32 v8, v8, v100\n"
    "v_add_f32 v8, v8, v101\n"
    "v_cvt_pk_bf16_f32 v90, v100, v101\n"
    "v_exp_f32 v103, v71\n"
    "v_exp_f32 v108, v76\n"
    "v_add_f32 v8, v8, v102\n"
    "v_add_f32 v8, v8, v103\n"
    "v_cvt_pk_bf16_f32 v91, v102, v103\n"
    "v_exp_f32 v109, v77\n"
    "v_exp_f32 v110, v78\n"
    "v_add_f32 v9, v9, v108\n"
    "v_add_f32 v9, v9, v109\n"
    "v_cvt_pk_bf16_f32 v94, v108, v109\n"
    "v_exp_f32 v111, v79\n"
    "s_nop 0\n"
    "v_add_f32 v9, v9, v110\n"
    "v_add_f32 v9, v9, v111\n"
    "v_cvt_pk_bf16_f32 v95, v110, v111\n"
    "v_add_u32 v3, s45, v2\n"
    "s_mov_b32 s38, s66\n"
    "v_add_f32 v6, v6, v8\n"
    "v_add_f32 v7, v7, v9\n"
    "s_waitcnt lgkmcnt(0)\n"
    "s_cmp_lt_u32 s38, s39\n"
    "s_cbranch_scc0 L_zerolo_%=\n"
    "s_barrier\n"
    "v_mfma_f32_16x16x32_bf16 a[0:3], a[64:67], v[88:91], 0\n"
    "v_mfma_f32_16x16x32_bf16 a[32:35], a[64:67], v[92:95], 0\n"
    "ds_read_b128 a[64:67], v3 offset:0\n"
    "s_add_u32 s15, s15, 1\n"
    "v_mfma_f32_16x16x32_bf16 a[4:7], a[68:71], v[88:91], 0\n"
    "v_mfma_f32_16x16x32_bf16 a[36:39], a[68:71], v[92:95], 0\n"
    "ds_read_b128 a[68:71], v3 offset:4096\n"
    "v_mfma_f32_16x16x32_bf16 a[8:11], a[72:75], v[88:91], 0\n"
    "s_cmp_ge_u32 s38, s42\n"
    "s_cselect_b32 s54, 0x4000, 0\n"
    "s_addc_u32 s66, s38, 1\n"
    "v_mfma_f32_16x16x32_bf16 a[40:43], a[72:75], v[92:95], 0\n"
    "ds_read_b128 a[72:75], v3 offset:1024\n"
    "v_mfma_f32_16x16x32_bf16 a[12:15], a[76:79], v[88:91], 0\n"
    "s_add_u32 s45, s45, s54\n"
    "s_add_u32 s45, s45, 0x4000\n"
    "v_mfma_f32_16x16x32_bf16 a[44:47], a[76:79], v[92:95], 0\n"
    "ds_read_b128 a[76:79], v3 offset:5120\n"
    "v_mfma_f32_16x16x32_bf16 a[16:19], a[80:83], v[88:91], 0\n"
    "s_sub_u32 s53, s45, 163840\n"
    "v_mfma_f32_16x16x32_bf16 a[48:51], a[80:83], v[92:95], 0\n"
    "ds_read_b128 a[80:83], v3 offset:2048\n"
    "v_mfma_f32_16x16x32_bf16 a[20:23], a[84:87], v[88:91], 0\n"
    "s_cmp_ge_u32 s45, 163840\n"
    "s_cselect_b32 s45, s53, s45\n"
    "v_mfma_f32_16x16x32_bf16 a[52:55], a[84:87], v[92:95], 0\n"
    "ds_read_b128 a[84:87], v3 offset:6144\n"
    "v_mfma_f32_16x16x32_bf16 a[24:27], a[88:91], v[88:91], 0\n"
    "v_mfma_f32_16x16x32_bf16 a[56:59], a[88:91], v[92:95], 0\n"
    "ds_read_b128 a[88:91], v3 offset:3072\n"
    "v_mfma_f32_16x16x32_bf16 a[28:31], a[92:95], v[88:91], 0\n"
    "v_mfma_f32_16x16x32_bf16 a[60:63], a[92:95], v[92:95], 0\n"
    "ds_read_b128 a[92:95], v3 offset:7168\n"
    "s_waitcnt lgkmcnt(4)\n"
    "v_mfma_f32_16x16x32_bf16 v[64:67], a[64:67], v[32:35], v[80:83]\n"
    "s_add_u32 s14, s14, 1\n"
    "v_mfma_f32_16x16x32_bf16 v[72:75], a[64:67], v[48:51], v[84:87]\n"
    "ds_read_b128 a[64:67], v3 offset:8192\n"
    "v_mfma_f32_16x16x32_bf16 v[68:71], a[68:71], v[32:35], v[80:83]\n"
    "v_mfma_f32_16x16x32_bf16 v[76:79], a[68:71], v[48:51], v[84:87]\n"
    "ds_read_b128 a[68:71], v3 offset:9216\n"
    "v_mfma_f32_16x16x32_bf16 v[64:67], a[72:75], v[36:39], v[64:67]\n"
    "v_mfma_f32_16x16x32_bf16 v[72:75], a[72:75], v[52:55], v[72:75]\n"
    "ds_read_b128 a[72:75], v3 offset:10240\n"
    "v_mfma_f32_16x16x32_bf16 v[68:71], a[76:79], v[36:39], v[68:71]\n"
    "v_mfma_f32_16x16x32_bf16 v[76:79], a[76:79], v[52:55], v[76:79]\n"
    "ds_read_b128 a[76:79], v3 offset:11264\n"
    "s_waitcnt lgkmcnt(4)\n"
    "v_mfma_f32_16x16x32_bf16 v[64:67], a[80:83], v[40:43], v[64:67]\n"
    "v_mfma_f32_16x16x32_bf16 v[72:75], a[80:83], v[56:59], v[72:75]\n"
    "ds_read_b128 a[80:83], v3 offset:12288\n"
    "v_mfma_f32_16x16x32_bf16 v[68:71], a[84:87], v[40:43], v[68:71]\n"
    "v_mfma_f32_16x16x32_bf16 v[76:79], a[84:87], v[56:59], v[76:79]\n"
    "ds_read_b128 a[84:87], v3 offset:13312\n"
    "v_mfma_f32_16x16x32_bf16 v[64:67], a[88:91], v[44:47], v[64:67]\n"
    "v_mfma_f32_16x16x32_bf16 v[72:75], a[88:91], v[60:63], v[72:75]\n"
    "ds_read_b128 a[88:91], v3 offset:14336\n"
    "v_mfma_f32_16x16x32_bf16 v[68:71], a[92:95], v[44:47], v[68:71]\n"
    "v_mfma_f32_16x16x32_bf16 v[76:79], a[92:95], v[60:63], v[76:79]\n"
    "ds_read_b128 a[92:95], v3 offset:15360\n"
    "s_branch L_smlo_%=\n"
    "L_zerolo_%=:\n"
    "v_accvgpr_write_b32 a0, 0\n"
    "v_accvgpr_write_b32 a1, 0\n"
    "v_accvgpr_write_b32 a2, 0\n"
    "v_accvgpr_write_b32 a3, 0\n"
    "v_accvgpr_write_b32 a4, 0\n"
    "v_accvgpr_write_b32 a5, 0\n"
    "v_accvgpr_write_b32 a6, 0\n"
    "v_accvgpr_write_b32 a7, 0\n"
    "v_accvgpr_write_b32 a8, 0\n"
    "v_accvgpr_write_b32 a9, 0\n"
    "v_accvgpr_write_b32 a10, 0\n"
    "v_accvgpr_write_b32 a11, 0\n"
    "v_accvgpr_write_b32 a12, 0\n"
    "v_accvgpr_write_b32 a13, 0\n"
    "v_accvgpr_write_b32 a14, 0\n"
    "v_accvgpr_write_b32 a15, 0\n"
    "v_accvgpr_write_b32 a16, 0\n"
    "v_accvgpr_write_b32 a17, 0\n"
    "v_accvgpr_write_b32 a18, 0\n"
    "v_accvgpr_write_b32 a19, 0\n"
    "v_accvgpr_write_b32 a20, 0\n"
    "v_accvgpr_write_b32 a21, 0\n"
    "v_accvgpr_write_b32 a22, 0\n"
    "v_accvgpr_write_b32 a23, 0\n"
    "v_accvgpr_write_b32 a24, 0\n"
    "v_accvgpr_write_b32 a25, 0\n"
    "v_accvgpr_write_b32 a26, 0\n"
    "v_accvgpr_write_b32 a27, 0\n"
    "v_accvgpr_write_b32 a28, 0\n"
    "v_accvgpr_write_b32 a29, 0\n"
    "v_accvgpr_write_b32 a30, 0\n"
    "v_accvgpr_write_b32 a31, 0\n"
    "v_accvgpr_write_b32 a32, 0\n"
    "v_accvgpr_write_b32 a33, 0\n"
    "v_accvgpr_write_b32 a34, 0\n"
    "v_accvgpr_write_b32 a35, 0\n"
    "v_accvgpr_write_b32 a36, 0\n"
    "v_accvgpr_write_b32 a37, 0\n"
    "v_accvgpr_write_b32 a38, 0\n"
    "v_accvgpr_write_b32 a39, 0\n"
    "v_accvgpr_write_b32 a40, 0\n"
    "v_accvgpr_write_b32 a41, 0\n"
    "v_accvgpr_write_b32 a42, 0\n"
    "v_accvgpr_write_b32 a43, 0\n"
    "v_accvgpr_write_b32 a44, 0\n"
    "v_accvgpr_write_b32 a45, 0\n"
    "v_accvgpr_write_b32 a46, 0\n"
    "v_accvgpr_write_b32 a47, 0\n"
    "v_accvgpr_write_b32 a48, 0\n"
    "v_accvgpr_write_b32 a49, 0\n"
    "v_accvgpr_write_b32 a50, 0\n"
    "v_accvgpr_write_b32 a51, 0\n"
    "v_accvgpr_write_b32 a52, 0\n"
    "v_accvgpr_write_b32 a53, 0\n"
    "v_accvgpr_write_b32 a54, 0\n"
    "v_accvgpr_write_b32 a55, 0\n"
    "v_accvgpr_write_b32 a56, 0\n"
    "v_accvgpr_write_b32 a57, 0\n"
    "v_accvgpr_write_b32 a58, 0\n"
    "v_accvgpr_write_b32 a59, 0\n"
    "v_accvgpr_write_b32 a60, 0\n"
    "v_accvgpr_write_b32 a61, 0\n"
    "v_accvgpr_write_b32 a62, 0\n"
    "v_accvgpr_write_b32 a63, 0\n"
    "s_branch L_nextlo_%=\n"
    "L_looplo_%=:\n"
    "s_barrier\n"
    "v_mfma_f32_16x16x32_bf16 a[0:3], a[64:67], v[88:91], a[0:3]\n"
    "v_mfma_f32_16x16x32_bf16 a[32:35], a[64:67], v[92:95], a[32:35]\n"
    "ds_read_b128 a[64:67], v3 offset:0\n"
    "s_add_u32 s15, s15, 1\n"
    "v_mfma_f32_16x16x32_bf16 a[4:7], a[68:71], v[88:91], a[4:7]\n"
    "v_mfma_f32_16x16x32_bf16 a[36:39], a[68:71], v[92:95], a[36:39]\n"
    "ds_read_b128 a[68:71], v3 offset:4096\n"
    "v_mfma_f32_16x16x32_bf16 a[8:11], a[72:75], v[88:91], a[8:11]\n"
    "s_cmp_ge_u32 s38, s42\n"
    "s_cselect_b32 s54, 0x4000, 0\n"
    "s_addc_u32 s66, s38, 1\n"
    "v_mfma_f32_16x16x32_bf16 a[40:43], a[72:75], v[92:95], a[40:43]\n"
    "ds_read_b128 a[72:75], v3 offset:1024\n"
    "v_mfma_f32_16x16x32_bf16 a[12:15], a[76:79], v[88:91], a[12:15]\n"
    "s_add_u32 s45, s45, s54\n"
    "s_add_u32 s45, s45, 0x4000\n"
    "v_mfma_f32_16x16x32_bf16 a[44:47], a[76:79], v[92:95], a[44:47]\n"
    "ds_read_b128 a[76:79], v3 offset:5120\n"
    "v_mfma_f32_16x16x32_bf16 a[16:19], a[80:83], v[88:91], a[16:19]\n"
    "s_sub_u32 s53, s45, 163840\n"
    "v_mfma_f32_16x16x32_bf16 a[48:51], a[80:83], v[92:95], a[48:51]\n"
    "ds_read_b128 a[80:83], v3 offset:2048\n"
    "v_mfma_f32_16x16x32_bf16 a[20:23], a[84:87], v[88:91], a[20:23]\n"
    "s_cmp_ge_u32 s45, 163840\n"
    "s_cselect_b32 s45, s53, s45\n"
    "v_mfma_f32_16x16x32_bf16 a[52:55], a[84:87], v[92:95], a[52:55]\n"
    "ds_read_b128 a[84:87], v3 offset:6144\n"
    "v_mfma_f32_16x16x32_bf16 a[24:27], a[88:91], v[88:91], a[24:27]\n"
    "v_mfma_f32_16x16x32_bf16 a[56:59], a[88:91], v[92:95], a[56:59]\n"
    "ds_read_b128 a[88:91], v3 offset:3072\n"
    "v_mfma_f32_16x16x32_bf16 a[28:31], a[92:95], v[88:91], a[28:31]\n"
    "v_mfma_f32_16x16x32_bf16 a[60:63], a[92:95], v[92:95], a[60:63]\n"
    "ds_read_b128 a[92:95], v3 offset:7168\n"
    "s_waitcnt lgkmcnt(4)\n"
    "v_mfma_f32_16x16x32_bf16 v[64:67], a[64:67], v[32:35], v[80:83]\n"
    "s_add_u32 s14, s14, 1\n"
    "v_mfma_f32_16x16x32_bf16 v[72:75], a[64:67], v[48:51], v[84:87]\n"
    "ds_read_b128 a[64:67], v3 offset:8192\n"
    "v_mfma_f32_16x16x32_bf16 v[68:71], a[68:71], v[32:35], v[80:83]\n"
    "v_mfma_f32_16x16x32_bf16 v[76:79], a[68:71], v[48:51], v[84:87]\n"
    "ds_read_b128 a[68:71], v3 offset:9216\n"
    "v_mfma_f32_16x16x32_bf16 v[64:67], a[72:75], v[36:39], v[64:67]\n"
    "v_mfma_f32_16x16x32_bf16 v[72:75], a[72:75], v[52:55], v[72:75]\n"
    "ds_read_b128 a[72:75], v3 offset:10240\n"
    "v_mfma_f32_16x16x32_bf16 v[68:71], a[76:79], v[36:39], v[68:71]\n"
    "v_mfma_f32_16x16x32_bf16 v[76:79], a[76:79], v[52:55], v[76:79]\n"
    "ds_read_b128 a[76:79], v3 offset:11264\n"
    "s_waitcnt lgkmcnt(4)\n"
    "v_mfma_f32_16x16x32_bf16 v[64:67], a[80:83], v[40:43], v[64:67]\n"
    "v_mfma_f32_16x16x32_bf16 v[72:75], a[80:83], v[56:59], v[72:75]\n"
    "ds_read_b128 a[80:83], v3 offset:12288\n"
    "v_mfma_f32_16x16x32_bf16 v[68:71], a[84:87], v[40:43], v[68:71]\n"
    "v_mfma_f32_16x16x32_bf16 v[76:79], a[84:87], v[56:59], v[76:79]\n"
    "ds_read_b128 a[84:87], v3 offset:13312\n"
    "v_mfma_f32_16x16x32_bf16 v[64:67], a[88:91], v[44:47], v[64:67]\n"
    "v_mfma_f32_16x16x32_bf16 v[72:75], a[88:91], v[60:63], v[72:75]\n"
    "ds_read_b128 a[88:91], v3 offset:14336\n"
    "v_mfma_f32_16x16x32_bf16 v[68:71], a[92:95], v[44:47], v[68:71]\n"
    "v_mfma_f32_16x16x32_bf16 v[76:79], a[92:95], v[60:63], v[76:79]\n"
    "ds_read_b128 a[92:95], v3 offset:15360\n"
    "L_smlo_%=:\n"
    "s_nop 2\n"
    "v_exp_f32 v96, v64\n"
    "v_exp_f32 v97, v65\n"
    "v_exp_f32 v98, v66\n"
    "v_add_f32 v8, v96, v97\n"
    "v_cvt_pk_bf16_f32 v88, v96, v97\n"
    "v_exp_f32 v99, v67\n"
    "v_exp_f32 v104, v72\n"
    "v_add_f32 v8, v8, v98\n"
    "v_add_f32 v8, v8, v99\n"
    "v_cvt_pk_bf16_f32 v89, v98, v99\n"
    "v_exp_f32 v105, v73\n"
    "v_exp_f32 v106, v74\n"
    "v_add_f32 v9, v104, v105\n"
    "v_cvt_pk_bf16_f32 v92, v104, v105\n"
    "v_exp_f32 v107, v75\n"
    "v_exp_f32 v100, v68\n"
    "v_add_f32 v9, v9, v106\n"
    "v_add_f32 v9, v9, v107\n"
    "v_cvt_pk_bf16_f32 v93, v106, v107\n"
    "v_exp_f32 v101, v69\n"
    "v_exp_f32 v102, v70\n"
    "v_add_f32 v8, v8, v100\n"
    "v_add_f32 v8, v8, v101\n"
    "v_cvt_pk_bf16_f32 v90, v100, v101\n"
    "v_exp_f32 v103, v71\n"
    "v_exp_f32 v108, v76\n"
    "v_add_f32 v8, v8, v102\n"
    "v_add_f32 v8, v8, v103\n"
    "v_cvt_pk_bf16_f32 v91, v102, v103\n"
    "v_exp_f32 v109, v77\n"
    "v_exp_f32 v110, v78\n"
    "v_add_f32 v9, v9, v108\n"
    "v_add_f32 v9, v9, v109\n"
    "v_cvt_pk_bf16_f32 v94, v108, v109\n"
    "v_exp_f32 v111, v79\n"
    "s_nop 0\n"
    "v_add_f32 v9, v9, v110\n"
    "v_add_f32 v9, v9, v111\n"
    "v_cvt_pk_bf16_f32 v95, v110, v111\n"
    "v_max_f32 v10, v8, v9\n"
    "v_cmp_lt_f32 vcc, s43, v10\n"
    "v_add_u32 v3, s45, v2\n"
    "s_mov_b32 s38, s66\n"
    "s_cbranch_vccnz L_rare_%=\n"
    "L_ret_lo_%=:\n"
    "v_add_f32 v6, v6, v8\n"
    "v_add_f32 v7, v7, v9\n"
    "s_waitcnt lgkmcnt(0)\n"
    "L_nextlo_%=:\n"
    "s_cmp_eq_u32 s15, s44\n"
    "s_cbranch_scc1 L_wopf_%=\n"
    "L_wopfr_%=:\n"
    "s_cmp_lt_u32 s38, s39\n"
    "s_cbranch_scc1 L_looplo_%=\n"
    "s_cmp_gt_u32 s38, s39\n"
    "s_cbranch_scc1 L_finlo_%=\n"
    "v_mov_b32 v10, 0xff800000\n"
    "v_cmp_gt_i32 s[52:53], 0, v18\n"
    "v_cmp_gt_i32 s[54:55], 1, v18\n"
    "v_cmp_gt_i32 s[56:57], 2, v18\n"
    "v_cmp_gt_i32 s[58:59], 3, v18\n"
    "v_cndmask_b32 v64, v80, v10, s[52:53]\n"
    "v_cndmask_b32 v65, v80, v10, s[54:55]\n"
    "v_cndmask_b32 v66, v80, v10, s[56:57]\n"
    "v_cndmask_b32 v67, v80, v10, s[58:59]\n"
    "v_cmp_gt_i32 s[52:53], 16, v18\n"
    "v_cmp_gt_i32 s[54:55], 17, v18\n"
    "v_cmp_gt_i32 s[56:57], 18, v18\n"
    "v_cmp_gt_i32 s[58:59], 19, v18\n"
    "v_cndmask_b32 v68, v80, v10, s[52:53]\n"
    "v_cndmask_b32 v69, v80, v10, s[54:55]\n"
    "v_cndmask_b32 v70, v80, v10, s[56:57]\n"
    "v_cndmask_b32 v71, v80, v10, s[58:59]\n"
    "v_cmp_gt_i32 s[52:53], -16, v18\n"
    "v_cmp_gt_i32 s[54:55], -15, v18\n"
    "v_cmp_gt_i32 s[56:57], -14, v18\n"
    "v_cmp_gt_i32 s[58:59], -13, v18\n"
    "v_cndmask_b32 v72, v84, v10, s[52:53]\n"
    "v_cndmask_b32 v73, v84, v10, s[54:55]\n"
    "v_cndmask_b32 v74, v84, v10, s[56:57]\n"
    "v_cndmask_b32 v75, v84, v10, s[58:59]\n"
    "v_cmp_gt_i32 s[52:53], 0, v18\n"
    "v_cmp_gt_i32 s[54:55], 1, v18\n"
    "v_cmp_gt_i32 s[56:57], 2, v18\n"
    "v_cmp_gt_i32 s[58:59], 3, v18\n"
    "v_cndmask_b32 v76, v84, v10, s[52:53]\n"
    "v_cndmask_b32 v77, v84, v10, s[54:55]\n"
    "v_cndmask_b32 v78, v84, v10, s[56:57]\n"
    "v_cndmask_b32 v79, v84, v10, s[58:59]\n"
    "s_barrier\n"
    "v_mfma_f32_16x16x32_bf16 a[0:3], a[64:67], v[88:91], a[0:3]\n"
    "v_mfma_f32_16x16x32_bf16 a[32:35], a[64:67], v[92:95], a[32:35]\n"
    "ds_read_b128 a[64:67], v3 offset:0\n"
    "s_add_u32 s15, s15, 1\n"
    "v_mfma_f32_16x16x32_bf16 a[4:7], a[68:71], v[88:91], a[4:7]\n"
    "v_mfma_f32_16x16x32_bf16 a[36:39], a[68:71], v[92:95], a[36:39]\n"
    "ds_read_b128 a[68:71], v3 offset:4096\n"
    "v_mfma_f32_16x16x32_bf16 a[8:11], a[72:75], v[88:91], a[8:11]\n"
    "s_cmp_ge_u32 s38, s42\n"
    "s_cselect_b32 s54, 0x4000, 0\n"
    "s_addc_u32 s66, s38, 1\n"
    "v_mfma_f32_16x16x32_bf16 a[40:43], a[72:75], v[92:95], a[40:43]\n"
    "ds_read_b128 a[72:75], v3 offset:1024\n"
    "v_mfma_f32_16x16x32_bf16 a[12:15], a[76:79], v[88:91], a[12:15]\n"
    "s_add_u32 s45, s45, s54\n"
    "s_add_u32 s45, s45, 0x4000\n"
    "v_mfma_f32_16x16x32_bf16 a[44:47], a[76:79], v[92:95], a[44:47]\n"
    "ds_read_b128 a[76:79], v3 offset:5120\n"
    "v_mfma_f32_16x16x32_bf16 a[16:19], a[80:83], v[88:91], a[16:19]\n"
    "s_sub_u32 s53, s45, 163840\n"
    "v_mfma_f32_16x16x32_bf16 a[48:51], a[80:83], v[92:95], a[48:51]\n"
    "ds_read_b128 a[80:83], v3 offset:2048\n"
    "v_mfma_f32_16x16x32_bf16 a[20:23], a[84:87], v[88:91], a[20:23]\n"
    "s_cmp_ge_u32 s45, 163840\n"
    "s_cselect_b32 s45, s53, s45\n"
    "v_mfma_f32_16x16x32_bf16 a[52:55], a[84:87], v[92:95], a[52:55]\n"
    "ds_read_b128 a[84:87], v3 offset:6144\n"
    "v_mfma_f32_16x16x32_bf16 a[24:27], a[88:91], v[88:91], a[24:27]\n"
    "v_mfma_f32_16x16x32_bf16 a[56:59], a[88:91], v[92:95], a[56:59]\n"
    "ds_read_b128 a[88:91], v3 offset:3072\n"
    "v_mfma_f32_16x16x32_bf16 a[28:31], a[92:95], v[88:91], a[28:31]\n"
    "v_mfma_f32_16x16x32_bf16 a[60:63], a[92:95], v[92:95], a[60:63]\n"
    "ds_read_b128 a[92:95], v3 offset:7168\n"
    "s_waitcnt lgkmcnt(4)\n"
    "v_mfma_f32_16x16x32_bf16 v[64:67], a[64:67], v[32:35], v[64:67]\n"
    "s_add_u32 s14, s14, 1\n"
    "v_mfma_f32_16x16x32_bf16 v[72:75], a[64:67], v[48:51], v[72:75]\n"
    "ds_read_b128 a[64:67], v3 offset:8192\n"
    "v_mfma_f32_16x16x32_bf16 v[68:71], a[68:71], v[32:35], v[68:71]\n"
    "v_mfma_f32_16x16x32_bf16 v[76:79], a[68:71], v[48:51], v[76:79]\n"
    "ds_read_b128 a[68:71], v3 offset:9216\n"
    "v_mfma_f32_16x16x32_bf16 v[64:67], a[72:75], v[36:39], v[64:67]\n"
    "v_mfma_f32_16x16x32_bf16 v[72:75], a[72:75], v[52:55], v[72:75]\n"
    "ds_read_b128 a[72:75], v3 offset:10240\n"
    "v_mfma_f32_16x16x32_bf16 v[68:71], a[76:79], v[36:39], v[68:71]\n"
    "v_mfma_f32_16x16x32_bf16 v[76:79], a[76:79], v[52:55], v[76:79]\n"
    "ds_read_b128 a[76:79], v3 offset:11264\n"
    "s_waitcnt lgkmcnt(4)\n"
    "v_mfma_f32_16x16x32_bf16 v[64:67], a[80:83], v[40:43], v[64:67]\n"
    "v_mfma_f32_16x16x32_bf16 v[72:75], a[80:83], v[56:59], v[72:75]\n"
    "ds_read_b128 a[80:83], v3 offset:12288\n"
    "v_mfma_f32_16x16x32_bf16 v[68:71], a[84:87], v[40:43], v[68:71]\n"
    "v_mfma_f32_16x16x32_bf16 v[76:79], a[84:87], v[56:59], v[76:79]\n"
    "ds_read_b128 a[84:87], v3 offset:13312\n"
    "v_mfma_f32_16x16x32_bf16 v[64:67], a[88:91], v[44:47], v[64:67]\n"
    "v_mfma_f32_16x16x32_bf16 v[72:75], a[88:91], v[60:63], v[72:75]\n"
    "ds_read_b128 a[88:91], v3 offset:14336\n"
    "v_mfma_f32_16x16x32_bf16 v[68:71], a[92:95], v[44:47], v[68:71]\n"
    "v_mfma_f32_16x16x32_bf16 v[76:79], a[92:95], v[60:63], v[76:79]\n"
    "ds_read_b128 a[92:95], v3 offset:15360\n"
    "s_branch L_smlo_%=\n"
    "L_finlo_%=:\n"
    "s_barrier\n"
    "v_mfma_f32_16x16x32_bf16 a[0:3], a[64:67], v[88:91], a[0:3]\n"
    "v_mfma_f32_16x16x32_bf16 a[32:35], a[64:67], v[92:95], a[32:35]\n"
    "s_add_u32 s15, s15, 1\n"
    "v_mfma_f32_16x16x32_bf16 a[4:7], a[68:71], v[88:91], a[4:7]\n"
    "v_mfma_f32_16x16x32_bf16 a[36:39], a[68:71], v[92:95], a[36:39]\n"
    "v_mfma_f32_16x16x32_bf16 a[8:11], a[72:75], v[88:91], a[8:11]\n"
    "v_mfma_f32_16x16x32_bf16 a[40:43], a[72:75], v[92:95], a[40:43]\n"
    "v_mfma_f32_16x16x32_bf16 a[12:15], a[76:79], v[88:91], a[12:15]\n"
    "v_mfma_f32_16x16x32_bf16 a[44:47], a[76:79], v[92:95], a[44:47]\n"
    "v_mfma_f32_16x16x32_bf16 a[16:19], a[80:83], v[88:91], a[16:19]\n"
    "v_mfma_f32_16x16x32_bf16 a[48:51], a[80:83], v[92:95], a[48:51]\n"
    "v_mfma_f32_16x16x32_bf16 a[20:23], a[84:87], v[88:91], a[20:23]\n"
    "v_mfma_f32_16x16x32_bf16 a[52:55], a[84:87], v[92:95], a[52:55]\n"
    "v_mfma_f32_16x16x32_bf16 a[24:27], a[88:91], v[88:91], a[24:27]\n"
    "v_mfma_f32_16x16x32_bf16 a[56:59], a[88:91], v[92:95], a[56:59]\n"
    "v_mfma_f32_16x16x32_bf16 a[28:31], a[92:95], v[88:91], a[28:31]\n"
    "v_mfma_f32_16x16x32_bf16 a[60:63], a[92:95], v[92:95], a[60:63]\n"
    "s_branch L_fin_%=\n"
    "L_rare_%=:\n"
    "v_max3_f32 v8, v64, v65, v66\n"
    "v_max3_f32 v9, v72, v73, v74\n"
    "v_max3_f32 v8, v8, v67, v68\n"
    "v_max3_f32 v9, v9, v75, v76\n"
    "v_max3_f32 v8, v8, v69, v70\n"
    "v_max3_f32 v9, v9, v77, v78\n"
    "v_max_f32 v8, v8, v71\n"
    "v_max_f32 v9, v9, v79\n"
    "v_mov_b32 v10, v8\n"
    "s_nop 1\n"
    "v_permlane16_swap_b32 v8, v10\n"
    "v_max_f32 v8, v8, v10\n"
    "v_mov_b32 v10, v8\n"
    "s_nop 1\n"
    "v_permlane32_swap_b32 v8, v10\n"
    "v_max_f32 v8, v8, v10\n"
    "v_mov_b32 v10, v9\n"
    "s_nop 1\n"
    "v_permlane16_swap_b32 v9, v10\n"
    "v_max_f32 v9, v9, v10\n"
    "v_mov_b32 v10, v9\n"
    "s_nop 1\n"
    "v_permlane32_swap_b32 v9, v10\n"
    "v_max_f32 v9, v9, v10\n"
    "v_max_f32 v8, 0, v8\n"
    "v_ceil_f32 v8, v8\n"
    "v_add_f32 v4, v4, v8\n"
    "v_min_f32 v10, 0x43800000, v8\n"
    "v_cvt_i32_f32 v10, v10\n"
    "v_sub_u32 v10, 0, v10\n"
    "v_ldexp_f32 v11, 1.0, v10\n"
    "v_sub_f32 v64, v64, v8\n"
    "v_sub_f32 v65, v65, v8\n"
    "v_sub_f32 v66, v66, v8\n"
    "v_sub_f32 v67, v67, v8\n"
    "v_sub_f32 v68, v68, v8\n"
    "v_sub_f32 v69, v69, v8\n"
    "v_sub_f32 v70, v70, v8\n"
    "v_sub_f32 v71, v71, v8\n"
    "v_sub_f32 v80, 0, v4\n"
    "v_sub_f32 v81, 0, v4\n"
    "v_sub_f32 v82, 0, v4\n"
    "v_sub_f32 v83, 0, v4\n"
    "v_mul_f32 v6, v6, v11\n"
    "v_max_f32 v9, 0, v9\n"
    "v_ceil_f32 v9, v9\n"
    "v_add_f32 v5, v5, v9\n"
    "v_min_f32 v10, 0x43800000, v9\n"
    "v_cvt_i32_f32 v10, v10\n"
    "v_sub_u32 v10, 0, v10\n"
    "v_ldexp_f32 v12, 1.0, v10\n"
    "v_sub_f32 v72, v72, v9\n"
    "v_sub_f32 v73, v73, v9\n"
    "v_sub_f32 v74, v74, v9\n"
    "v_sub_f32 v75, v75, v9\n"
    "v_sub_f32 v76, v76, v9\n"
    "v_sub_f32 v77, v77, v9\n"
    "v_sub_f32 v78, v78, v9\n"
    "v_sub_f32 v79, v79, v9\n"
    "v_sub_f32 v84, 0, v5\n"
    "v_sub_f32 v85, 0, v5\n"
    "v_sub_f32 v86, 0, v5\n"
    "v_sub_f32 v87, 0, v5\n"
    "v_mul_f32 v7, v7, v12\n"
    "v_accvgpr_read_b32 v8, a0\n"
    "v_accvgpr_read_b32 v9, a1\n"
    "v_mul_f32 v8, v8, v11\n"
    "v_mul_f32 v9, v9, v11\n"
    "v_accvgpr_write_b32 a0, v8\n"
    "v_accvgpr_write_b32 a1, v9\n"
    "v_accvgpr_read_b32 v8, a2\n"
    "v_accvgpr_read_b32 v9, a3\n"
    "v_mul_f32 v8, v8, v11\n"
    "v_mul_f32 v9, v9, v11\n"
    "v_accvgpr_write_b32 a2, v8\n"
    "v_accvgpr_write_b32 a3, v9\n"
    "v_accvgpr_read_b32 v8, a4\n"
    "v_accvgpr_read_b32 v9, a5\n"
    "v_mul_f32 v8, v8, v11\n"
    "v_mul_f32 v9, v9, v11\n"
    "v_accvgpr_write_b32 a4, v8\n"
    "v_accvgpr_write_b32 a5, v9\n"
    "v_accvgpr_read_b32 v8, a6\n"
    "v_accvgpr_read_b32 v9, a7\n"
    "v_mul_f32 v8, v8, v11\n"
    "v_mul_f32 v9, v9, v11\n"
    "v_accvgpr_write_b32 a6, v8\n"
    "v_accvgpr_write_b32 a7, v9\n"
    "v_accvgpr_read_b32 v8, a8\n"
    "v_accvgpr_read_b32 v9, a9\n"
    "v_mul_f32 v8, v8, v11\n"
    "v_mul_f32 v9, v9, v11\n"
    "v_accvgpr_write_b32 a8, v8\n"
    "v_accvgpr_write_b32 a9, v9\n"
    "v_accvgpr_read_b32 v8, a10\n"
    "v_accvgpr_read_b32 v9, a11\n"
    "v_mul_f32 v8, v8, v11\n"
    "v_mul_f32 v9, v9, v11\n"
    "v_accvgpr_write_b32 a10, v8\n"
    "v_accvgpr_write_b32 a11, v9\n"
    "v_accvgpr_read_b32 v8, a12\n"
    "v_accvgpr_read_b32 v9, a13\n"
    "v_mul_f32 v8, v8, v11\n"
    "v_mul_f32 v9, v9, v11\n"
    "v_accvgpr_write_b32 a12, v8\n"
    "v_accvgpr_write_b32 a13, v9\n"
    "v_accvgpr_read_b32 v8, a14\n"
    "v_accvgpr_read_b32 v9, a15\n"
    "v_mul_f32 v8, v8, v11\n"
    "v_mul_f32 v9, v9, v11\n"
    "v_accvgpr_write_b32 a14, v8\n"
    "v_accvgpr_write_b32 a15, v9\n"
    "v_accvgpr_read_b32 v8, a16\n"
    "v_accvgpr_read_b32 v9, a17\n"
    "v_mul_f32 v8, v8, v11\n"
    "v_mul_f32 v9, v9, v11\n"
    "v_accvgpr_write_b32 a16, v8\n"
    "v_accvgpr_write_b32 a17, v9\n"
    "v_accvgpr_read_b32 v8, a18\n"
    "v_accvgpr_read_b32 v9, a19\n"
    "v_mul_f32 v8, v8, v11\n"
    "v_mul_f32 v9, v9, v11\n"
    "v_accvgpr_write_b32 a18, v8\n"
    "v_accvgpr_write_b32 a19, v9\n"
    "v_accvgpr_read_b32 v8, a20\n"
    "v_accvgpr_read_b32 v9, a21\n"
    "v_mul_f32 v8, v8, v11\n"
    "v_mul_f32 v9, v9, v11\n"
    "v_accvgpr_write_b32 a20, v8\n"
    "v_accvgpr_write_b32 a21, v9\n"
    "v_accvgpr_read_b32 v8, a22\n"
    "v_accvgpr_read_b32 v9, a23\n"
    "v_mul_f32 v8, v8, v11\n"
    "v_mul_f32 v9, v9, v11\n"
    "v_accvgpr_write_b32 a22, v8\n"
    "v_accvgpr_write_b32 a23, v9\n"
    "v_accvgpr_read_b32 v8, a24\n"
    "v_accvgpr_read_b32 v9, a25\n"
    "v_mul_f32 v8, v8, v11\n"
    "v_mul_f32 v9, v9, v11\n"
    "v_accvgpr_write_b32 a24, v8\n"
    "v_accvgpr_write_b32 a25, v9\n"
    "v_accvgpr_read_b32 v8, a26\n"
    "v_accvgpr_read_b32 v9, a27\n"
    "v_mul_f32 v8, v8, v11\n"
    "v_mul_f32 v9, v9, v11\n"
    "v_accvgpr_write_b32 a26, v8\n"
    "v_accvgpr_write_b32 a27, v9\n"
    "v_accvgpr_read_b32 v8, a28\n"
    "v_accvgpr_read_b32 v9, a29\n"
    "v_mul_f32 v8, v8, v11\n"
    "v_mul_f32 v9, v9, v11\n"
    "v_accvgpr_write_b32 a28, v8\n"
    "v_accvgpr_write_b32 a29, v9\n"
    "v_accvgpr_read_b32 v8, a30\n"
    "v_accvgpr_read_b32 v9, a31\n"
    "v_mul_f32 v8, v8, v11\n"
    "v_mul_f32 v9, v9, v11\n"
    "v_accvgpr_write_b32 a30, v8\n"
    "v_accvgpr_write_b32 a31, v9\n"
    "v_accvgpr_read_b32 v8, a32\n"
    "v_accvgpr_read_b32 v9, a33\n"
    "v_mul_f32 v8, v8, v12\n"
    "v_mul_f32 v9, v9, v12\n"
    "v_accvgpr_write_b32 a32, v8\n"
    "v_accvgpr_write_b32 a33, v9\n"
    "v_accvgpr_read_b32 v8, a34\n"
    "v_accvgpr_read_b32 v9, a35\n"
    "v_mul_f32 v8, v8, v12\n"
    "v_mul_f32 v9, v9, v12\n"
    "v_accvgpr_write_b32 a34, v8\n"
    "v_accvgpr_write_b32 a35, v9\n"
    "v_accvgpr_read_b32 v8, a36\n"
    "v_accvgpr_read_b32 v9, a37\n"
    "v_mul_f32 v8, v8, v12\n"
    "v_mul_f32 v9, v9, v12\n"
    "v_accvgpr_write_b32 a36, v8\n"
    "v_accvgpr_write_b32 a37, v9\n"
    "v_accvgpr_read_b32 v8, a38\n"
    "v_accvgpr_read_b32 v9, a39\n"
    "v_mul_f32 v8, v8, v12\n"
    "v_mul_f32 v9, v9, v12\n"
    "v_accvgpr_write_b32 a38, v8\n"
    "v_accvgpr_write_b32 a39, v9\n"
    "v_accvgpr_read_b32 v8, a40\n"
    "v_accvgpr_read_b32 v9, a41\n"
    "v_mul_f32 v8, v8, v12\n"
    "v_mul_f32 v9, v9, v12\n"
    "v_accvgpr_write_b32 a40, v8\n"
    "v_accvgpr_write_b32 a41, v9\n"
    "v_accvgpr_read_b32 v8, a42\n"
    "v_accvgpr_read_b32 v9, a43\n"
    "v_mul_f32 v8, v8, v12\n"
    "v_mul_f32 v9, v9, v12\n"
    "v_accvgpr_write_b32 a42, v8\n"
    "v_accvgpr_write_b32 a43, v9\n"
    "v_accvgpr_read_b32 v8, a44\n"
    "v_accvgpr_read_b32 v9, a45\n"
    "v_mul_f32 v8, v8, v12\n"
    "v_mul_f32 v9, v9, v12\n"
    "v_accvgpr_write_b32 a44, v8\n"
    "v_accvgpr_write_b32 a45, v9\n"
    "v_accvgpr_read_b32 v8, a46\n"
    "v_accvgpr_read_b32 v9, a47\n"
    "v_mul_f32 v8, v8, v12\n"
    "v_mul_f32 v9, v9, v12\n"
    "v_accvgpr_write_b32 a46, v8\n"
    "v_accvgpr_write_b32 a47, v9\n"
    "v_accvgpr_read_b32 v8, a48\n"
    "v_accvgpr_read_b32 v9, a49\n"
    "v_mul_f32 v8, v8, v12\n"
    "v_mul_f32 v9, v9, v12\n"
    "v_accvgpr_write_b32 a48, v8\n"
    "v_accvgpr_write_b32 a49, v9\n"
    "v_accvgpr_read_b32 v8, a50\n"
    "v_accvgpr_read_b32 v9, a51\n"
    "v_mul_f32 v8, v8, v12\n"
    "v_mul_f32 v9, v9, v12\n"
    "v_accvgpr_write_b32 a50, v8\n"
    "v_accvgpr_write_b32 a51, v9\n"
    "v_accvgpr_read_b32 v8, a52\n"
    "v_accvgpr_read_b32 v9, a53\n"
    "v_mul_f32 v8, v8, v12\n"
    "v_mul_f32 v9, v9, v12\n"
    "v_accvgpr_write_b32 a52, v8\n"
    "v_accvgpr_write_b32 a53, v9\n"
    "v_accvgpr_read_b32 v8, a54\n"
    "v_accvgpr_read_b32 v9, a55\n"
    "v_mul_f32 v8, v8, v12\n"
    "v_mul_f32 v9, v9, v12\n"
    "v_accvgpr_write_b32 a54, v8\n"
    "v_accvgpr_write_b32 a55, v9\n"
    "v_accvgpr_read_b32 v8, a56\n"
    "v_accvgpr_read_b32 v9, a57\n"
    "v_mul_f32 v8, v8, v12\n"
    "v_mul_f32 v9, v9, v12\n"
    "v_accvgpr_write_b32 a56, v8\n"
    "v_accvgpr_write_b32 a57, v9\n"
    "v_accvgpr_read_b32 v8, a58\n"
    "v_accvgpr_read_b32 v9, a59\n"
    "v_mul_f32 v8, v8, v12\n"
    "v_mul_f32 v9, v9, v12\n"
    "v_accvgpr_write_b32 a58, v8\n"
    "v_accvgpr_write_b32 a59, v9\n"
    "v_accvgpr_read_b32 v8, a60\n"
    "v_accvgpr_read_b32 v9, a61\n"
    "v_mul_f32 v8, v8, v12\n"
    "v_mul_f32 v9, v9, v12\n"
    "v_accvgpr_write_b32 a60, v8\n"
    "v_accvgpr_write_b32 a61, v9\n"
    "v_accvgpr_read_b32 v8, a62\n"
    "v_accvgpr_read_b32 v9, a63\n"
    "v_mul_f32 v8, v8, v12\n"
    "v_mul_f32 v9, v9, v12\n"
    "v_accvgpr_write_b32 a62, v8\n"
    "v_accvgpr_write_b32 a63, v9\n"
    "v_exp_f32 v96, v64\n"
    "v_exp_f32 v97, v65\n"
    "v_exp_f32 v98, v66\n"
    "v_add_f32 v8, v96, v97\n"
    "v_cvt_pk_bf16_f32 v88, v96, v97\n"
    "v_exp_f32 v99, v67\n"
    "v_exp_f32 v104, v72\n"
    "v_add_f32 v8, v8, v98\n"
    "v_add_f32 v8, v8, v99\n"
    "v_cvt_pk_bf16_f32 v89, v98, v99\n"
    "v_exp_f32 v105, v73\n"
    "v_exp_f32 v106, v74\n"
    "v_add_f32 v9, v104, v105\n"
    "v_cvt_pk_bf16_f32 v92, v104, v105\n"
    "v_exp_f32 v107, v75\n"
    "v_exp_f32 v100, v68\n"
    "v_add_f32 v9, v9, v106\n"
    "v_add_f32 v9, v9, v107\n"
    "v_cvt_pk_bf16_f32 v93, v106, v107\n"
    "v_exp_f32 v101, v69\n"
    "v_exp_f32 v102, v70\n"
    "v_add_f32 v8, v8, v100\n"
    "v_add_f32 v8, v8, v101\n"
    "v_cvt_pk_bf16_f32 v90, v100, v101\n"
    "v_exp_f32 v103, v71\n"
    "v_exp_f32 v108, v76\n"
    "v_add_f32 v8, v8, v102\n"
    "v_add_f32 v8, v8, v103\n"
    "v_cvt_pk_bf16_f32 v91, v102, v103\n"
    "v_exp_f32 v109, v77\n"
    "v_exp_f32 v110, v78\n"
    "v_add_f32 v9, v9, v108\n"
    "v_add_f32 v9, v9, v109\n"
    "v_cvt_pk_bf16_f32 v94, v108, v109\n"
    "v_exp_f32 v111, v79\n"
    "s_nop 0\n"
    "v_add_f32 v9, v9, v110\n"
    "v_add_f32 v9, v9, v111\n"
    "v_cvt_pk_bf16_f32 v95, v110, v111\n"
    "s_cmp_eq_u32 s50, 1\n"
    "s_cbranch_scc1 L_ret_lo_%=\n"
    "s_branch L_ret_hi_%=\n"
    "L_wopf_%=:\n"
    "s_mov_b64 s[24:25], s[88:89]\n"
    "s_and_b32 s25, s25, 0xffff\n"
    "s_mov_b32 s26, 0x400000\n"
    "s_lshr_b32 s52, %1, 3\n"
    "s_lshl_b32 s52, s52, 2\n"
    "s_add_u32 s52, s52, s51\n"
    "s_lshl_b32 s52, s52, 15\n"
    "s_and_b32 s53, %1, 1\n"
    "s_lshl_b32 s53, s53, 9\n"
    "s_add_u32 s52, s52, s53\n"
    "s_add_u32 s53, s52, 0x4000\n"
    "v_lshrrev_b32 v20, 6, v2\n"
    "v_lshlrev_b32 v20, 10, v20\n"
    "v_bfe_u32 v23, v2, 4, 2\n"
    "v_lshl_add_u32 v20, v23, 7, v20\n"
    "buffer_load_dword v21, v20, s[24:27], s52 offen\n"
    "buffer_load_dword v22, v20, s[24:27], s53 offen\n"
    "s_branch L_wopfr_%=\n"
    "L_fin_%=:\n"
    "s_cmp_eq_u32 s67, 1\n"
    "s_cbranch_scc1 L_mg1_%=\n"
    "s_cmp_eq_u32 s50, 1\n"
    "s_cbranch_scc1 L_loepi_%=\n"
    "s_cmp_lt_u32 s32, 0x1000\n"
    "s_cbranch_scc1 L_mg0_%=\n"
    "s_branch L_plain_%=\n"
    "L_loepi_%=:\n"
    "s_mov_b32 s52, 4096\n"
    "buffer_load_dwordx4 v[32:35], v2, s[20:23], 0 offen offset:0 sc1\n"
    "buffer_load_dwordx4 v[36:39], v2, s[20:23], 0 offen offset:1024 sc1\n"
    "buffer_load_dwordx4 v[40:43], v2, s[20:23], 0 offen offset:2048 sc1\n"
    "buffer_load_dwordx4 v[44:47], v2, s[20:23], 0 offen offset:3072 sc1\n"
    "buffer_load_dwordx4 v[48:51], v2, s[20:23], s52 offen offset:0 sc1\n"
    "buffer_load_dwordx4 v[52:55], v2, s[20:23], s52 offen offset:1024 sc1\n"
    "buffer_load_dwordx4 v[56:59], v2, s[20:23], s52 offen offset:2048 sc1\n"
    "buffer_load_dwordx4 v[60:63], v2, s[20:23], s52 offen offset:3072 sc1\n"
    "L_plain_%=:\n"
    "v_mov_b32 v10, v6\n"
    "s_nop 1\n"
    "v_permlane16_swap_b32 v6, v10\n"
    "v_add_f32 v6, v6, v10\n"
    "v_mov_b32 v10, v6\n"
    "s_nop 1\n"
    "v_permlane32_swap_b32 v6, v10\n"
    "v_add_f32 v6, v6, v10\n"
    "v_rcp_f32 v11, v6\n"
    "v_mov_b32 v10, v7\n"
    "s_nop 1\n"
    "v_permlane16_swap_b32 v7, v10\n"
    "v_add_f32 v7, v7, v10\n"
    "v_mov_b32 v10, v7\n"
    "s_nop 1\n"
    "v_permlane32_swap_b32 v7, v10\n"
    "v_add_f32 v7, v7, v10\n"
    "v_rcp_f32 v12, v7\n"
    "s_mov_b32 s55, 0x20000\n"
    "v_accvgpr_read_b32 v64, a0\n"
    "v_accvgpr_read_b32 v65, a1\n"
    "v_accvgpr_read_b32 v66, a2\n"
    "v_accvgpr_read_b32 v67, a3\n"
    "v_accvgpr_read_b32 v68, a4\n"
    "v_accvgpr_read_b32 v69, a5\n"
    "v_accvgpr_read_b32 v70, a6\n"
    "v_accvgpr_read_b32 v71, a7\n"
    "v_mul_f32 v64, v64, v11\n"
    "v_mul_f32 v65, v65, v11\n"
    "v_mul_f32 v66, v66, v11\n"
    "v_mul_f32 v67, v67, v11\n"
    "v_mul_f32 v68, v68, v11\n"
    "v_mul_f32 v69, v69, v11\n"
    "v_mul_f32 v70, v70, v11\n"
    "v_mul_f32 v71, v71, v11\n"
    "v_cvt_pk_bf16_f32 v112, v64, v65\n"
    "v_cvt_pk_bf16_f32 v113, v66, v67\n"
    "v_cvt_pk_bf16_f32 v114, v68, v69\n"
    "v_cvt_pk_bf16_f32 v115, v70, v71\n"
    "buffer_store_dwordx4 v[112:115], v2, s[28:31], 0 offen offset:0 sc1\n"
    "v_accvgpr_read_b32 v72, a8\n"
    "v_accvgpr_read_b32 v73, a9\n"
    "v_accvgpr_read_b32 v74, a10\n"
    "v_accvgpr_read_b32 v75, a11\n"
    "v_accvgpr_read_b32 v76, a12\n"
    "v_accvgpr_read_b32 v77, a13\n"
    "v_accvgpr_read_b32 v78, a14\n"
    "v_accvgpr_read_b32 v79, a15\n"
    "v_mul_f32 v72, v72, v11\n"
    "v_mul_f32 v73, v73, v11\n"
    "v_mul_f32 v74, v74, v11\n"
    "v_mul_f32 v75, v75, v11\n"
    "v_mul_f32 v76, v76, v11\n"
    "v_mul_f32 v77, v77, v11\n"
    "v_mul_f32 v78, v78, v11\n"
    "v_mul_f32 v79, v79, v11\n"
    "v_cvt_pk_bf16_f32 v116, v72, v73\n"
    "v_cvt_pk_bf16_f32 v117, v74, v75\n"
    "v_cvt_pk_bf16_f32 v118, v76, v77\n"
    "v_cvt_pk_bf16_f32 v119, v78, v79\n"
    "buffer_store_dwordx4 v[116:119], v2, s[28:31], 0 offen offset:1024 sc1\n"
    "v_accvgpr_read_b32 v80, a16\n"
    "v_accvgpr_read_b32 v81, a17\n"
    "v_accvgpr_read_b32 v82, a18\n"
    "v_accvgpr_read_b32 v83, a19\n"
    "v_accvgpr_read_b32 v84, a20\n"
    "v_accvgpr_read_b32 v85, a21\n"
    "v_accvgpr_read_b32 v86, a22\n"
    "v_accvgpr_read_b32 v87, a23\n"
    "v_mul_f32 v80, v80, v11\n"
    "v_mul_f32 v81, v81, v11\n"
    "v_mul_f32 v82, v82, v11\n"
    "v_mul_f32 v83, v83, v11\n"
    "v_mul_f32 v84, v84, v11\n"
    "v_mul_f32 v85, v85, v11\n"
    "v_mul_f32 v86, v86, v11\n"
    "v_mul_f32 v87, v87, v11\n"
    "v_cvt_pk_bf16_f32 v120, v80, v81\n"
    "v_cvt_pk_bf16_f32 v121, v82, v83\n"
    "v_cvt_pk_bf16_f32 v122, v84, v85\n"
    "v_cvt_pk_bf16_f32 v123, v86, v87\n"
    "buffer_store_dwordx4 v[120:123], v2, s[28:31], 0 offen offset:2048 sc1\n"
    "v_accvgpr_read_b32 v88, a24\n"
    "v_accvgpr_read_b32 v89, a25\n"
    "v_accvgpr_read_b32 v90, a26\n"
    "v_accvgpr_read_b32 v91, a27\n"
    "v_accvgpr_read_b32 v92, a28\n"
    "v_accvgpr_read_b32 v93, a29\n"
    "v_accvgpr_read_b32 v94, a30\n"
    "v_accvgpr_read_b32 v95, a31\n"
    "v_mul_f32 v88, v88, v11\n"
    "v_mul_f32 v89, v89, v11\n"
    "v_mul_f32 v90, v90, v11\n"
    "v_mul_f32 v91, v91, v11\n"
    "v_mul_f32 v92, v92, v11\n"
    "v_mul_f32 v93, v93, v11\n"
    "v_mul_f32 v94, v94, v11\n"
    "v_mul_f32 v95, v95, v11\n"
    "v_cvt_pk_bf16_f32 v124, v88, v89\n"
    "v_cvt_pk_bf16_f32 v125, v90, v91\n"
    "v_cvt_pk_bf16_f32 v126, v92, v93\n"
    "v_cvt_pk_bf16_f32 v127, v94, v95\n"
    "buffer_store_dwordx4 v[124:127], v2, s[28:31], 0 offen offset:3072 sc1\n"
    "s_barrier\n"
    "s_add_u32 s15, s15, 1\n"
    "v_accvgpr_read_b32 v64, a32\n"
    "v_accvgpr_read_b32 v65, a33\n"
    "v_accvgpr_read_b32 v66, a34\n"
    "v_accvgpr_read_b32 v67, a35\n"
    "v_accvgpr_read_b32 v68, a36\n"
    "v_accvgpr_read_b32 v69, a37\n"
    "v_accvgpr_read_b32 v70, a38\n"
    "v_accvgpr_read_b32 v71, a39\n"
    "v_mul_f32 v64, v64, v12\n"
    "v_mul_f32 v65, v65, v12\n"
    "v_mul_f32 v66, v66, v12\n"
    "v_mul_f32 v67, v67, v12\n"
    "v_mul_f32 v68, v68, v12\n"
    "v_mul_f32 v69, v69, v12\n"
    "v_mul_f32 v70, v70, v12\n"
    "v_mul_f32 v71, v71, v12\n"
    "v_cvt_pk_bf16_f32 v112, v64, v65\n"
    "v_cvt_pk_bf16_f32 v113, v66, v67\n"
    "v_cvt_pk_bf16_f32 v114, v68, v69\n"
    "v_cvt_pk_bf16_f32 v115, v70, v71\n"
    "buffer_store_dwordx4 v[112:115], v2, s[28:31], s55 offen offset:0 sc1\n"
    "v_accvgpr_read_b32 v72, a40\n"
    "v_accvgpr_read_b32 v73, a41\n"
    "v_accvgpr_read_b32 v74, a42\n"
    "v_accvgpr_read_b32 v75, a43\n"
    "v_accvgpr_read_b32 v76, a44\n"
    "v_accvgpr_read_b32 v77, a45\n"
    "v_accvgpr_read_b32 v78, a46\n"
    "v_accvgpr_read_b32 v79, a47\n"
    "v_mul_f32 v72, v72, v12\n"
    "v_mul_f32 v73, v73, v12\n"
    "v_mul_f32 v74, v74, v12\n"
    "v_mul_f32 v75, v75, v12\n"
    "v_mul_f32 v76, v76, v12\n"
    "v_mul_f32 v77, v77, v12\n"
    "v_mul_f32 v78, v78, v12\n"
    "v_mul_f32 v79, v79, v12\n"
    "v_cvt_pk_bf16_f32 v116, v72, v73\n"
    "v_cvt_pk_bf16_f32 v117, v74, v75\n"
    "v_cvt_pk_bf16_f32 v118, v76, v77\n"
    "v_cvt_pk_bf16_f32 v119, v78, v79\n"
    "buffer_store_dwordx4 v[116:119], v2, s[28:31], s55 offen offset:1024 sc1\n"
    "v_accvgpr_read_b32 v80, a48\n"
    "v_accvgpr_read_b32 v81, a49\n"
    "v_accvgpr_read_b32 v82, a50\n"
    "v_accvgpr_read_b32 v83, a51\n"
    "v_accvgpr_read_b32 v84, a52\n"
    "v_accvgpr_read_b32 v85, a53\n"
    "v_accvgpr_read_b32 v86, a54\n"
    "v_accvgpr_read_b32 v87, a55\n"
    "v_mul_f32 v80, v80, v12\n"
    "v_mul_f32 v81, v81, v12\n"
    "v_mul_f32 v82, v82, v12\n"
    "v_mul_f32 v83, v83, v12\n"
    "v_mul_f32 v84, v84, v12\n"
    "v_mul_f32 v85, v85, v12\n"
    "v_mul_f32 v86, v86, v12\n"
    "v_mul_f32 v87, v87, v12\n"
    "v_cvt_pk_bf16_f32 v120, v80, v81\n"
    "v_cvt_pk_bf16_f32 v121, v82, v83\n"
    "v_cvt_pk_bf16_f32 v122, v84, v85\n"
    "v_cvt_pk_bf16_f32 v123, v86, v87\n"
    "buffer_store_dwordx4 v[120:123], v2, s[28:31], s55 offen offset:2048 sc1\n"
    "v_accvgpr_read_b32 v88, a56\n"
    "v_accvgpr_read_b32 v89, a57\n"
    "v_accvgpr_read_b32 v90, a58\n"
    "v_accvgpr_read_b32 v91, a59\n"
    "v_accvgpr_read_b32 v92, a60\n"
    "v_accvgpr_read_b32 v93, a61\n"
    "v_accvgpr_read_b32 v94, a62\n"
    "v_accvgpr_read_b32 v95, a63\n"
    "v_mul_f32 v88, v88, v12\n"
    "v_mul_f32 v89, v89, v12\n"
    "v_mul_f32 v90, v90, v12\n"
    "v_mul_f32 v91, v91, v12\n"
    "v_mul_f32 v92, v92, v12\n"
    "v_mul_f32 v93, v93, v12\n"
    "v_mul_f32 v94, v94, v12\n"
    "v_mul_f32 v95, v95, v12\n"
    "v_cvt_pk_bf16_f32 v124, v88, v89\n"
    "v_cvt_pk_bf16_f32 v125, v90, v91\n"
    "v_cvt_pk_bf16_f32 v126, v92, v93\n"
    "v_cvt_pk_bf16_f32 v127, v94, v95\n"
    "buffer_store_dwordx4 v[124:127], v2, s[28:31], s55 offen offset:3072 sc1\n"
    "s_cmp_eq_u32 s50, 0\n"
    "s_cbranch_scc1 L_done_%=\n"
    "s_cmp_ge_u32 s32, 0x1000\n"
    "s_cbranch_scc1 L_done_%=\n"
    "s_mov_b32 s67, 1\n"
    "s_add_u32 s38, s32, 1\n"
    "s_mov_b32 s42, 0\n"
    "s_mov_b32 s39, s33\n"
    "s_mul_i32 s52, s38, 205\n"
    "s_lshr_b32 s52, s52, 11\n"
    "s_mul_i32 s52, s52, 10\n"
    "s_sub_u32 s52, s38, s52\n"
    "s_lshl_b32 s45, s52, 14\n"
    "s_lshl_b32 s52, s33, 18\n"
    "s_lshl_b32 s53, s36, 12\n"
    "s_add_u32 s52, s52, s53\n"
    "s_mov_b64 s[28:29], s[78:79]\n"
    "s_add_u32 s28, s28, s52\n"
    "s_addc_u32 s29, s29, 0\n"
    "s_and_b32 s29, s29, 0xffff\n"
    "s_mov_b32 s30, 0x21000\n"
    "s_mov_b32 s31, 0x20000\n"
    "s_waitcnt vmcnt(8)\n"
    "s_branch L_run_%=\n"
    "L_mg0_%=:\n"
    "L_mg0_w_%=:\n"
    "s_cmp_ge_u32 s15, 34\n"
    "s_cbranch_scc1 L_mg0_go_%=\n"
    "s_barrier\n"
    "s_add_u32 s15, s15, 1\n"
    "s_branch L_mg0_w_%=\n"
    "L_mg0_go_%=:\n"
    "s_sub_u32 s56, s33, 5\n"
    "s_mul_i32 s52, s56, 205\n"
    "s_lshr_b32 s52, s52, 11\n"
    "s_mul_i32 s52, s52, 10\n"
    "s_sub_u32 s52, s56, s52\n"
    "s_lshl_b32 s52, s52, 14\n"
    "v_add_u32 v3, s52, v2\n"
    "s_nop 7\n"
    "ds_write_b128 v3, a[16:19] offset:0\n"
    "ds_write_b128 v3, a[20:23] offset:1024\n"
    "ds_write_b128 v3, a[24:27] offset:2048\n"
    "ds_write_b128 v3, a[28:31] offset:3072\n"
    "ds_write_b128 v3, a[48:51] offset:4096\n"
    "ds_write_b128 v3, a[52:55] offset:5120\n"
    "ds_write_b128 v3, a[56:59] offset:6144\n"
    "ds_write_b128 v3, a[60:63] offset:7168\n"
    "v_mov_b32 v10, v6\n"
    "s_nop 1\n"
    "v_permlane16_swap_b32 v6, v10\n"
    "v_add_f32 v6, v6, v10\n"
    "v_mov_b32 v10, v6\n"
    "s_nop 1\n"
    "v_permlane32_swap_b32 v6, v10\n"
    "v_add_f32 v6, v6, v10\n"
    "v_mov_b32 v10, v7\n"
    "s_nop 1\n"
    "v_permlane16_swap_b32 v7, v10\n"
    "v_add_f32 v7, v7, v10\n"
    "v_mov_b32 v10, v7\n"
    "s_nop 1\n"
    "v_permlane32_swap_b32 v7, v10\n"
    "v_add_f32 v7, v7, v10\n"
    "s_sub_u32 s56, s33, s51\n"
    "s_sub_u32 s56, s56, 6\n"
    "s_mul_i32 s52, s56, 205\n"
    "s_lshr_b32 s52, s52, 11\n"
    "s_mul_i32 s52, s52, 10\n"
    "s_sub_u32 s56, s56, s52\n"
    "s_lshl_b32 s56, s56, 14\n"
    "s_lshl_b32 s52, s34, 10\n"
    "s_xor_b32 s53, s52, 4096\n"
    "s_add_u32 s52, s52, s56\n"
    "s_add_u32 s53, s53, s56\n"
    "v_add_u32 v8, s52, v2\n"
    "v_add_u32 v9, s53, v2\n"
    "ds_write_b128 v8, v[4:7]\n"
    "v_accvgpr_read_b32 v96, a0\n"
    "v_accvgpr_read_b32 v97, a1\n"
    "v_accvgpr_read_b32 v98, a2\n"
    "v_accvgpr_read_b32 v99, a3\n"
    "v_accvgpr_read_b32 v100, a4\n"
    "v_accvgpr_read_b32 v101, a5\n"
    "v_accvgpr_read_b32 v102, a6\n"
    "v_accvgpr_read_b32 v103, a7\n"
    "v_accvgpr_read_b32 v104, a8\n"
    "v_accvgpr_read_b32 v105, a9\n"
    "v_accvgpr_read_b32 v106, a10\n"
    "v_accvgpr_read_b32 v107, a11\n"
    "v_accvgpr_read_b32 v108, a12\n"
    "v_accvgpr_read_b32 v109, a13\n"
    "v_accvgpr_read_b32 v110, a14\n"
    "v_accvgpr_read_b32 v111, a15\n"
    "v_accvgpr_read_b32 v112, a32\n"
    "v_accvgpr_read_b32 v113, a33\n"
    "v_accvgpr_read_b32 v114, a34\n"
    "v_accvgpr_read_b32 v115, a35\n"
    "v_accvgpr_read_b32 v116, a36\n"
    "v_accvgpr_read_b32 v117, a37\n"
    "v_accvgpr_read_b32 v118, a38\n"
    "v_accvgpr_read_b32 v119, a39\n"
    "v_accvgpr_read_b32 v120, a40\n"
    "v_accvgpr_read_b32 v121, a41\n"
    "v_accvgpr_read_b32 v122, a42\n"
    "v_accvgpr_read_b32 v123, a43\n"
    "v_accvgpr_read_b32 v124, a44\n"
    "v_accvgpr_read_b32 v125, a45\n"
    "v_accvgpr_read_b32 v126, a46\n"
    "v_accvgpr_read_b32 v127, a47\n"
    "s_waitcnt vmcnt(0) lgkmcnt(0)\n"
    "s_barrier\n"
    "s_add_u32 s15, s15, 1\n"
    "ds_read_b128 v[20:23], v9\n"
    "ds_read_b128 v[64:67], v3 offset:8192\n"
    "ds_read_b128 v[68:71], v3 offset:9216\n"
    "ds_read_b128 v[72:75], v3 offset:10240\n"
    "ds_read_b128 v[76:79], v3 offset:11264\n"
    "ds_read_b128 v[80:83], v3 offset:12288\n"
    "ds_read_b128 v[84:87], v3 offset:13312\n"
    "ds_read_b128 v[88:91], v3 offset:14336\n"
    "ds_read_b128 v[92:95], v3 offset:15360\n"
    "s_mov_b32 s55, 0x20000\n"
    "s_waitcnt lgkmcnt(8)\n"
    "v_max_f32 v8, v4, v20\n"
    "v_sub_f32 v9, v4, v8\n"
    "v_sub_f32 v10, v20, v8\n"
    "v_exp_f32 v9, v9\n"
    "v_exp_f32 v10, v10\n"
    "v_mul_f32 v8, v9, v6\n"
    "v_fma_f32 v8, v10, v22, v8\n"
    "v_rcp_f32 v8, v8\n"
    "s_nop 0\n"
    "v_mul_f32 v11, v9, v8\n"
    "v_mul_f32 v24, v10, v8\n"
    "v_max_f32 v8, v5, v21\n"
    "v_sub_f32 v9, v5, v8\n"
    "v_sub_f32 v10, v21, v8\n"
    "v_exp_f32 v9, v9\n"
    "v_exp_f32 v10, v10\n"
    "v_mul_f32 v8, v9, v7\n"
    "v_fma_f32 v8, v10, v23, v8\n"
    "v_rcp_f32 v8, v8\n"
    "s_nop 0\n"
    "v_mul_f32 v12, v9, v8\n"
    "v_mul_f32 v25, v10, v8\n"
    "s_waitcnt lgkmcnt(0)\n"
    "v_mul_f32 v96, v96, v11\n"
    "v_mul_f32 v97, v97, v11\n"
    "v_mul_f32 v98, v98, v11\n"
    "v_mul_f32 v99, v99, v11\n"
    "v_mul_f32 v100, v100, v11\n"
    "v_mul_f32 v101, v101, v11\n"
    "v_mul_f32 v102, v102, v11\n"
    "v_mul_f32 v103, v103, v11\n"
    "v_fma_f32 v96, v64, v24, v96\n"
    "v_fma_f32 v97, v65, v24, v97\n"
    "v_fma_f32 v98, v66, v24, v98\n"
    "v_fma_f32 v99, v67, v24, v99\n"
    "v_fma_f32 v100, v68, v24, v100\n"
    "v_fma_f32 v101, v69, v24, v101\n"
    "v_fma_f32 v102, v70, v24, v102\n"
    "v_fma_f32 v103, v71, v24, v103\n"
    "v_cvt_pk_bf16_f32 v32, v96, v97\n"
    "v_cvt_pk_bf16_f32 v33, v98, v99\n"
    "v_cvt_pk_bf16_f32 v34, v100, v101\n"
    "v_cvt_pk_bf16_f32 v35, v102, v103\n"
    "buffer_store_dwordx4 v[32:35], v2, s[28:31], 0 offen offset:0 sc1\n"
    "v_mul_f32 v104, v104, v11\n"
    "v_mul_f32 v105, v105, v11\n"
    "v_mul_f32 v106, v106, v11\n"
    "v_mul_f32 v107, v107, v11\n"
    "v_mul_f32 v108, v108, v11\n"
    "v_mul_f32 v109, v109, v11\n"
    "v_mul_f32 v110, v110, v11\n"
    "v_mul_f32 v111, v111, v11\n"
    "v_fma_f32 v104, v72, v24, v104\n"
    "v_fma_f32 v105, v73, v24, v105\n"
    "v_fma_f32 v106, v74, v24, v106\n"
    "v_fma_f32 v107, v75, v24, v107\n"
    "v_fma_f32 v108, v76, v24, v108\n"
    "v_fma_f32 v109, v77, v24, v109\n"
    "v_fma_f32 v110, v78, v24, v110\n"
    "v_fma_f32 v111, v79, v24, v111\n"
    "v_cvt_pk_bf16_f32 v36, v104, v105\n"
    "v_cvt_pk_bf16_f32 v37, v106, v107\n"
    "v_cvt_pk_bf16_f32 v38, v108, v109\n"
    "v_cvt_pk_bf16_f32 v39, v110, v111\n"
    "buffer_store_dwordx4 v[36:39], v2, s[28:31], 0 offen offset:1024 sc1\n"
    "v_mul_f32 v112, v112, v12\n"
    "v_mul_f32 v113, v113, v12\n"
    "v_mul_f32 v114, v114, v12\n"
    "v_mul_f32 v115, v115, v12\n"
    "v_mul_f32 v116, v116, v12\n"
    "v_mul_f32 v117, v117, v12\n"
    "v_mul_f32 v118, v118, v12\n"
    "v_mul_f32 v119, v119, v12\n"
    "v_fma_f32 v112, v80, v25, v112\n"
    "v_fma_f32 v113, v81, v25, v113\n"
    "v_fma_f32 v114, v82, v25, v114\n"
    "v_fma_f32 v115, v83, v25, v115\n"
    "v_fma_f32 v116, v84, v25, v116\n"
    "v_fma_f32 v117, v85, v25, v117\n"
    "v_fma_f32 v118, v86, v25, v118\n"
    "v_fma_f32 v119, v87, v25, v119\n"
    "v_cvt_pk_bf16_f32 v40, v112, v113\n"
    "v_cvt_pk_bf16_f32 v41, v114, v115\n"
    "v_cvt_pk_bf16_f32 v42, v116, v117\n"
    "v_cvt_pk_bf16_f32 v43, v118, v119\n"
    "buffer_store_dwordx4 v[40:43], v2, s[28:31], s55 offen offset:0 sc1\n"
    "v_mul_f32 v120, v120, v12\n"
    "v_mul_f32 v121, v121, v12\n"
    "v_mul_f32 v122, v122, v12\n"
    "v_mul_f32 v123, v123, v12\n"
    "v_mul_f32 v124, v124, v12\n"
    "v_mul_f32 v125, v125, v12\n"
    "v_mul_f32 v126, v126, v12\n"
    "v_mul_f32 v127, v127, v12\n"
    "v_fma_f32 v120, v88, v25, v120\n"
    "v_fma_f32 v121, v89, v25, v121\n"
    "v_fma_f32 v122, v90, v25, v122\n"
    "v_fma_f32 v123, v91, v25, v123\n"
    "v_fma_f32 v124, v92, v25, v124\n"
    "v_fma_f32 v125, v93, v25, v125\n"
    "v_fma_f32 v126, v94, v25, v126\n"
    "v_fma_f32 v127, v95, v25, v127\n"
    "v_cvt_pk_bf16_f32 v44, v120, v121\n"
    "v_cvt_pk_bf16_f32 v45, v122, v123\n"
    "v_cvt_pk_bf16_f32 v46, v124, v125\n"
    "v_cvt_pk_bf16_f32 v47, v126, v127\n"
    "buffer_store_dwordx4 v[44:47], v2, s[28:31], s55 offen offset:1024 sc1\n"
    "s_branch L_done_%=\n"
    "L_mg1_%=:\n"
    "L_mg1_w_%=:\n"
    "s_cmp_ge_u32 s15, 34\n"
    "s_cbranch_scc1 L_mg1_go_%=\n"
    "s_barrier\n"
    "s_add_u32 s15, s15, 1\n"
    "s_branch L_mg1_w_%=\n"
    "L_mg1_go_%=:\n"
    "s_sub_u32 s56, s33, 5\n"
    "s_mul_i32 s52, s56, 205\n"
    "s_lshr_b32 s52, s52, 11\n"
    "s_mul_i32 s52, s52, 10\n"
    "s_sub_u32 s52, s56, s52\n"
    "s_lshl_b32 s52, s52, 14\n"
    "v_add_u32 v3, s52, v2\n"
    "s_nop 7\n"
    "ds_write_b128 v3, a[0:3] offset:8192\n"
    "ds_write_b128 v3, a[4:7] offset:9216\n"
    "ds_write_b128 v3, a[8:11] offset:10240\n"
    "ds_write_b128 v3, a[12:15] offset:11264\n"
    "ds_write_b128 v3, a[32:35] offset:12288\n"
    "ds_write_b128 v3, a[36:39] offset:13312\n"
    "ds_write_b128 v3, a[40:43] offset:14336\n"
    "ds_write_b128 v3, a[44:47] offset:15360\n"
    "v_mov_b32 v10, v6\n"
    "s_nop 1\n"
    "v_permlane16_swap_b32 v6, v10\n"
    "v_add_f32 v6, v6, v10\n"
    "v_mov_b32 v10, v6\n"
    "s_nop 1\n"
    "v_permlane32_swap_b32 v6, v10\n"
    "v_add_f32 v6, v6, v10\n"
    "v_mov_b32 v10, v7\n"
    "s_nop 1\n"
    "v_permlane16_swap_b32 v7, v10\n"
    "v_add_f32 v7, v7, v10\n"
    "v_mov_b32 v10, v7\n"
    "s_nop 1\n"
    "v_permlane32_swap_b32 v7, v10\n"
    "v_add_f32 v7, v7, v10\n"
    "s_sub_u32 s56, s33, s51\n"
    "s_sub_u32 s56, s56, 6\n"
    "s_mul_i32 s52, s56, 205\n"
    "s_lshr_b32 s52, s52, 11\n"
    "s_mul_i32 s52, s52, 10\n"
    "s_sub_u32 s56, s56, s52\n"
    "s_lshl_b32 s56, s56, 14\n"
    "s_lshl_b32 s52, s34, 10\n"
    "s_xor_b32 s53, s52, 4096\n"
    "s_add_u32 s52, s52, s56\n"
    "s_add_u32 s53, s53, s56\n"
    "v_add_u32 v8, s52, v2\n"
    "v_add_u32 v9, s53, v2\n"
    "ds_write_b128 v8, v[4:7]\n"
    "v_accvgpr_read_b32 v96, a16\n"
    "v_accvgpr_read_b32 v97, a17\n"
    "v_accvgpr_read_b32 v98, a18\n"
    "v_accvgpr_read_b32 v99, a19\n"
    "v_accvgpr_read_b32 v100, a20\n"
    "v_accvgpr_read_b32 v101, a21\n"
    "v_accvgpr_read_b32 v102, a22\n"
    "v_accvgpr_read_b32 v103, a23\n"
    "v_accvgpr_read_b32 v104, a24\n"
    "v_accvgpr_read_b32 v105, a25\n"
    "v_accvgpr_read_b32 v106, a26\n"
    "v_accvgpr_read_b32 v107, a27\n"
    "v_accvgpr_read_b32 v108, a28\n"
    "v_accvgpr_read_b32 v109, a29\n"
    "v_accvgpr_read_b32 v110, a30\n"
    "v_accvgpr_read_b32 v111, a31\n"
    "v_accvgpr_read_b32 v112, a48\n"
    "v_accvgpr_read_b32 v113, a49\n"
    "v_accvgpr_read_b32 v114, a50\n"
    "v_accvgpr_read_b32 v115, a51\n"
    "v_accvgpr_read_b32 v116, a52\n"
    "v_accvgpr_read_b32 v117, a53\n"
    "v_accvgpr_read_b32 v118, a54\n"
    "v_accvgpr_read_b32 v119, a55\n"
    "v_accvgpr_read_b32 v120, a56\n"
    "v_accvgpr_read_b32 v121, a57\n"
    "v_accvgpr_read_b32 v122, a58\n"
    "v_accvgpr_read_b32 v123, a59\n"
    "v_accvgpr_read_b32 v124, a60\n"
    "v_accvgpr_read_b32 v125, a61\n"
    "v_accvgpr_read_b32 v126, a62\n"
    "v_accvgpr_read_b32 v127, a63\n"
    "s_waitcnt vmcnt(0) lgkmcnt(0)\n"
    "s_barrier\n"
    "s_add_u32 s15, s15, 1\n"
    "ds_read_b128 v[20:23], v9\n"
    "ds_read_b128 v[64:67], v3 offset:0\n"
    "ds_read_b128 v[68:71], v3 offset:1024\n"
    "ds_read_b128 v[72:75], v3 offset:2048\n"
    "ds_read_b128 v[76:79], v3 offset:3072\n"
    "ds_read_b128 v[80:83], v3 offset:4096\n"
    "ds_read_b128 v[84:87], v3 offset:5120\n"
    "ds_read_b128 v[88:91], v3 offset:6144\n"
    "ds_read_b128 v[92:95], v3 offset:7168\n"
    "s_mov_b32 s55, 0x20000\n"
    "s_waitcnt lgkmcnt(8)\n"
    "v_max_f32 v8, v4, v20\n"
    "v_sub_f32 v9, v4, v8\n"
    "v_sub_f32 v10, v20, v8\n"
    "v_exp_f32 v9, v9\n"
    "v_exp_f32 v10, v10\n"
    "v_mul_f32 v8, v9, v6\n"
    "v_fma_f32 v8, v10, v22, v8\n"
    "v_rcp_f32 v8, v8\n"
    "s_nop 0\n"
    "v_mul_f32 v11, v9, v8\n"
    "v_mul_f32 v24, v10, v8\n"
    "v_max_f32 v8, v5, v21\n"
    "v_sub_f32 v9, v5, v8\n"
    "v_sub_f32 v10, v21, v8\n"
    "v_exp_f32 v9, v9\n"
    "v_exp_f32 v10, v10\n"
    "v_mul_f32 v8, v9, v7\n"
    "v_fma_f32 v8, v10, v23, v8\n"
    "v_rcp_f32 v8, v8\n"
    "s_nop 0\n"
    "v_mul_f32 v12, v9, v8\n"
    "v_mul_f32 v25, v10, v8\n"
    "s_waitcnt lgkmcnt(0)\n"
    "v_mul_f32 v96, v96, v11\n"
    "v_mul_f32 v97, v97, v11\n"
    "v_mul_f32 v98, v98, v11\n"
    "v_mul_f32 v99, v99, v11\n"
    "v_mul_f32 v100, v100, v11\n"
    "v_mul_f32 v101, v101, v11\n"
    "v_mul_f32 v102, v102, v11\n"
    "v_mul_f32 v103, v103, v11\n"
    "v_fma_f32 v96, v64, v24, v96\n"
    "v_fma_f32 v97, v65, v24, v97\n"
    "v_fma_f32 v98, v66, v24, v98\n"
    "v_fma_f32 v99, v67, v24, v99\n"
    "v_fma_f32 v100, v68, v24, v100\n"
    "v_fma_f32 v101, v69, v24, v101\n"
    "v_fma_f32 v102, v70, v24, v102\n"
    "v_fma_f32 v103, v71, v24, v103\n"
    "v_cvt_pk_bf16_f32 v32, v96, v97\n"
    "v_cvt_pk_bf16_f32 v33, v98, v99\n"
    "v_cvt_pk_bf16_f32 v34, v100, v101\n"
    "v_cvt_pk_bf16_f32 v35, v102, v103\n"
    "buffer_store_dwordx4 v[32:35], v2, s[28:31], 0 offen offset:2048 sc1\n"
    "v_mul_f32 v104, v104, v11\n"
    "v_mul_f32 v105, v105, v11\n"
    "v_mul_f32 v106, v106, v11\n"
    "v_mul_f32 v107, v107, v11\n"
    "v_mul_f32 v108, v108, v11\n"
    "v_mul_f32 v109, v109, v11\n"
    "v_mul_f32 v110, v110, v11\n"
    "v_mul_f32 v111, v111, v11\n"
    "v_fma_f32 v104, v72, v24, v104\n"
    "v_fma_f32 v105, v73, v24, v105\n"
    "v_fma_f32 v106, v74, v24, v106\n"
    "v_fma_f32 v107, v75, v24, v107\n"
    "v_fma_f32 v108, v76, v24, v108\n"
    "v_fma_f32 v109, v77, v24, v109\n"
    "v_fma_f32 v110, v78, v24, v110\n"
    "v_fma_f32 v111, v79, v24, v111\n"
    "v_cvt_pk_bf16_f32 v36, v104, v105\n"
    "v_cvt_pk_bf16_f32 v37, v106, v107\n"
    "v_cvt_pk_bf16_f32 v38, v108, v109\n"
    "v_cvt_pk_bf16_f32 v39, v110, v111\n"
    "buffer_store_dwordx4 v[36:39], v2, s[28:31], 0 offen offset:3072 sc1\n"
    "v_mul_f32 v112, v112, v12\n"
    "v_mul_f32 v113, v113, v12\n"
    "v_mul_f32 v114, v114, v12\n"
    "v_mul_f32 v115, v115, v12\n"
    "v_mul_f32 v116, v116, v12\n"
    "v_mul_f32 v117, v117, v12\n"
    "v_mul_f32 v118, v118, v12\n"
    "v_mul_f32 v119, v119, v12\n"
    "v_fma_f32 v112, v80, v25, v112\n"
    "v_fma_f32 v113, v81, v25, v113\n"
    "v_fma_f32 v114, v82, v25, v114\n"
    "v_fma_f32 v115, v83, v25, v115\n"
    "v_fma_f32 v116, v84, v25, v116\n"
    "v_fma_f32 v117, v85, v25, v117\n"
    "v_fma_f32 v118, v86, v25, v118\n"
    "v_fma_f32 v119, v87, v25, v119\n"
    "v_cvt_pk_bf16_f32 v40, v112, v113\n"
    "v_cvt_pk_bf16_f32 v41, v114, v115\n"
    "v_cvt_pk_bf16_f32 v42, v116, v117\n"
    "v_cvt_pk_bf16_f32 v43, v118, v119\n"
    "buffer_store_dwordx4 v[40:43], v2, s[28:31], s55 offen offset:2048 sc1\n"
    "v_mul_f32 v120, v120, v12\n"
    "v_mul_f32 v121, v121, v12\n"
    "v_mul_f32 v122, v122, v12\n"
    "v_mul_f32 v123, v123, v12\n"
    "v_mul_f32 v124, v124, v12\n"
    "v_mul_f32 v125, v125, v12\n"
    "v_mul_f32 v126, v126, v12\n"
    "v_mul_f32 v127, v127, v12\n"
    "v_fma_f32 v120, v88, v25, v120\n"
    "v_fma_f32 v121, v89, v25, v121\n"
    "v_fma_f32 v122, v90, v25, v122\n"
    "v_fma_f32 v123, v91, v25, v123\n"
    "v_fma_f32 v124, v92, v25, v124\n"
    "v_fma_f32 v125, v93, v25, v125\n"
    "v_fma_f32 v126, v94, v25, v126\n"
    "v_fma_f32 v127, v95, v25, v127\n"
    "v_cvt_pk_bf16_f32 v44, v120, v121\n"
    "v_cvt_pk_bf16_f32 v45, v122, v123\n"
    "v_cvt_pk_bf16_f32 v46, v124, v125\n"
    "v_cvt_pk_bf16_f32 v47, v126, v127\n"
    "buffer_store_dwordx4 v[44:47], v2, s[28:31], s55 offen offset:3072 sc1\n"
    "L_done_%=:\n"
    "s_waitcnt vmcnt(0) lgkmcnt(0)\n"
    "L_sync_%=:\n"
    "s_cmp_ge_u32 s15, 35\n"
    "s_cbranch_scc1 L_synced_%=\n"
    "s_barrier\n"
    "s_add_u32 s15, s15, 1\n"
    "s_branch L_sync_%=\n"
    "L_synced_%=:\n"
    "s_barrier\n"
    "s_cmp_eq_u32 s34, 0\n"
    "s_cbranch_scc0 L_pbc_%=\n"
    "s_sub_u32 s52, 15, s35\n"
    "s_lshl_b32 s52, s52, 7\n"
    "s_lshl_b32 s53, s35, 7\n"
    "s_lshl_b32 s54, s36, 2\n"
    "s_add_u32 s54, s54, 655360\n"
    "s_add_u32 s52, s52, s54\n"
    "s_add_u32 s53, s53, s54\n"
    "v_mov_b32 v20, s52\n"
    "v_mov_b32 v22, s53\n"
    "v_mov_b32 v21, 1\n"
    "s_mov_b64 exec, 1\n"
    "global_store_dword v20, v21, s[96:97] sc1\n"
    "global_store_dword v22, v21, s[96:97] sc1\n"
    "s_mov_b64 exec, -1\n"
    "L_pbc_%=:\n"
    "s_load_dwordx2 s[94:95], %0, 72\n"
    "s_load_dwordx2 s[20:21], %0, 96\n"
    "s_mov_b64 s[82:83], s[96:97]\n"
    "s_add_u32 s82, s82, 33554432\n"
    "s_addc_u32 s83, s83, 0\n"
    "s_mov_b64 s[84:85], s[96:97]\n"
    "s_add_u32 s84, s84, 8388608\n"
    "s_addc_u32 s85, s85, 0\n"
    "s_mov_b64 s[86:87], s[96:97]\n"
    "s_add_u32 s86, s86, 4194304\n"
    "s_addc_u32 s87, s87, 0\n"
    "s_mov_b64 s[14:15], s[96:97]\n"
    "s_add_u32 s14, s14, 65536\n"
    "s_addc_u32 s15, s15, 0\n"
    "s_mov_b64 s[16:17], s[96:97]\n"
    "s_add_u32 s16, s16, 16384\n"
    "s_addc_u32 s17, s17, 0\n"
    "s_mov_b64 s[18:19], s[96:97]\n"
    "s_add_u32 s18, s18, 16777216\n"
    "s_addc_u32 s19, s19, 0\n"
    "s_waitcnt lgkmcnt(0)\n"
    "v_and_b32 v3, 63, %2\n"
    "v_lshrrev_b32 v8, 6, %2\n"
    "v_lshlrev_b32 v2, 4, v3\n"
    "v_readfirstlane_b32 s52, v8\n"
    "s_and_b32 s64, %1, 7\n"
    "s_lshr_b32 s65, %1, 3\n"
    "s_and_b32 s54, s65, 3\n"
    "s_lshr_b32 s65, s65, 2\n"
    "s_lshl_b32 s61, s54, 1\n"
    "s_add_u32 s61, s61, s65\n"
    "s_lshr_b32 s53, s64, 1\n"
    "s_lshl_b32 s53, s53, 3\n"
    "s_add_u32 s53, s53, s65\n"
    "s_and_b32 s64, s64, 1\n"
    "s_lshl_b32 s64, s64, 2\n"
    "s_add_u32 s54, s54, s64\n"
    "s_cmp_ge_u32 s61, 8\n"
    "s_cselect_b32 s65, 8, 0\n"
    "s_sub_u32 s61, s61, s65\n"
    "s_mul_i32 s64, s53, 524288\n"
    "s_mul_i32 s65, s52, 8192\n"
    "s_add_u32 s64, s64, s65\n"
    "s_mov_b64 s[24:25], s[82:83]\n"
    "s_add_u32 s24, s24, s64\n"
    "s_addc_u32 s25, s25, 0\n"
    "s_and_b32 s25, s25, 0xffff\n"
    "s_mov_b32 s26, 524288\n"
    "s_mov_b32 s27, 0x20000\n"
    "s_mul_i32 s64, s52, 262144\n"
    "s_lshl_b32 s65, s54, 7\n"
    "s_add_u32 s64, s64, s65\n"
    "s_sub_u32 s65, 4194304, s64\n"
    "s_mov_b64 s[28:29], s[88:89]\n"
    "s_add_u32 s28, s28, s64\n"
    "s_addc_u32 s29, s29, 0\n"
    "s_and_b32 s29, s29, 0xffff\n"
    "s_mov_b32 s30, s65\n"
    "s_mov_b32 s31, 0x20000\n"
    "v_bfe_u32 v8, v3, 2, 3\n"
    "v_lshlrev_b32 v4, 10, v8\n"
    "v_lshrrev_b32 v8, 5, v3\n"
    "v_lshl_add_u32 v4, v8, 6, v4\n"
    "v_and_b32 v9, 3, v3\n"
    "v_bfe_u32 v8, v3, 4, 1\n"
    "v_lshlrev_b32 v8, 1, v8\n"
    "v_xor_b32 v9, v9, v8\n"
    "v_lshl_add_u32 v4, v9, 4, v4\n"
    "s_mul_i32 s55, s52, 16384\n"
    "s_mov_b32 s60, 0\n"
    "s_mov_b32 s63, s61\n"
    "s_lshl_b32 s60, s61, 15\n"
    "s_add_u32 m0, s55, 0\n"
    "s_add_u32 s57, s60, 0\n"
    "buffer_load_dwordx4 v4, s[28:31], s57 offen lds\n"
    "s_add_u32 m0, s55, 1024\n"
    "s_add_u32 s57, s60, 8192\n"
    "buffer_load_dwordx4 v4, s[28:31], s57 offen lds\n"
    "s_add_u32 m0, s55, 2048\n"
    "s_add_u32 s57, s60, 16384\n"
    "buffer_load_dwordx4 v4, s[28:31], s57 offen lds\n"
    "s_add_u32 m0, s55, 3072\n"
    "s_add_u32 s57, s60, 24576\n"
    "buffer_load_dwordx4 v4, s[28:31], s57 offen lds\n"
    "s_add_u32 s61, s61, 1\n"
    "s_cmp_eq_u32 s61, 8\n"
    "s_cselect_b32 s61, 0, s61\n"
    "s_lshl_b32 s60, s61, 15\n"
    "s_add_u32 m0, s55, 4096\n"
    "s_add_u32 s57, s60, 0\n"
    "buffer_load_dwordx4 v4, s[28:31], s57 offen lds\n"
    "s_add_u32 m0, s55, 5120\n"
    "s_add_u32 s57, s60, 8192\n"
    "buffer_load_dwordx4 v4, s[28:31], s57 offen lds\n"
    "s_add_u32 m0, s55, 6144\n"
    "s_add_u32 s57, s60, 16384\n"
    "buffer_load_dwordx4 v4, s[28:31], s57 offen lds\n"
    "s_add_u32 m0, s55, 7168\n"
    "s_add_u32 s57, s60, 24576\n"
    "buffer_load_dwordx4 v4, s[28:31], s57 offen lds\n"
    "s_add_u32 s61, s61, 1\n"
    "s_cmp_eq_u32 s61, 8\n"
    "s_cselect_b32 s61, 0, s61\n"
    "s_lshl_b32 s60, s61, 15\n"
    "s_add_u32 m0, s55, 8192\n"
    "s_add_u32 s57, s60, 0\n"
    "buffer_load_dwordx4 v4, s[28:31], s57 offen lds\n"
    "s_add_u32 m0, s55, 9216\n"
    "s_add_u32 s57, s60, 8192\n"
    "buffer_load_dwordx4 v4, s[28:31], s57 offen lds\n"
    "s_add_u32 m0, s55, 10240\n"
    "s_add_u32 s57, s60, 16384\n"
    "buffer_load_dwordx4 v4, s[28:31], s57 offen lds\n"
    "s_add_u32 m0, s55, 11264\n"
    "s_add_u32 s57, s60, 24576\n"
    "buffer_load_dwordx4 v4, s[28:31], s57 offen lds\n"
    "s_add_u32 s61, s61, 1\n"
    "s_cmp_eq_u32 s61, 8\n"
    "s_cselect_b32 s61, 0, s61\n"
    "s_lshl_b32 s60, s61, 15\n"
    "s_add_u32 m0, s55, 12288\n"
    "s_add_u32 s57, s60, 0\n"
    "buffer_load_dwordx4 v4, s[28:31], s57 offen lds\n"
    "s_add_u32 m0, s55, 13312\n"
    "s_add_u32 s57, s60, 8192\n"
    "buffer_load_dwordx4 v4, s[28:31], s57 offen lds\n"
    "s_add_u32 m0, s55, 14336\n"
    "s_add_u32 s57, s60, 16384\n"
    "buffer_load_dwordx4 v4, s[28:31], s57 offen lds\n"
    "s_add_u32 m0, s55, 15360\n"
    "s_add_u32 s57, s60, 24576\n"
    "buffer_load_dwordx4 v4, s[28:31], s57 offen lds\n"
    "s_add_u32 s61, s61, 1\n"
    "s_cmp_eq_u32 s61, 8\n"
    "s_cselect_b32 s61, 0, s61\n"
    "v_lshrrev_b32 v8, 5, v3\n"
    "v_lshlrev_b32 v5, 10, v8\n"
    "v_bfe_u32 v9, v3, 2, 3\n"
    "v_lshl_add_u32 v5, v9, 6, v5\n"
    "v_and_b32 v9, 3, v3\n"
    "v_lshl_add_u32 v5, v9, 3, v5\n"
    "v_bfe_u32 v9, v3, 4, 1\n"
    "v_lshl_add_u32 v5, v9, 5, v5\n"
    "v_add_u32 v5, s55, v5\n"
    "v_xor_b32 v6, 32, v5\n"
    "s_mov_b32 s62, 0xffff0000\n"
    "s_lshr_b32 s64, s52, 1\n"
    "s_lshl_b32 s66, s53, 2\n"
    "s_add_u32 s66, s66, s64\n"
    "s_and_b32 s64, s52, 1\n"
    "s_lshl_b32 s67, s54, 1\n"
    "s_add_u32 s67, s67, s64\n"
    "v_and_b32 v7, 15, v3\n"
    "v_lshrrev_b32 v8, 4, v3\n"
    "v_lshlrev_b32 v11, 10, v7\n"
    "v_lshl_add_u32 v11, v8, 3, v11\n"
    "v_lshlrev_b32 v12, 4, v8\n"
    "v_lshlrev_b32 v13, 5, v3\n"
    "s_lshl_b32 s64, s67, 7\n"
    "s_mov_b64 s[36:37], s[96:97]\n"
    "s_add_u32 s36, s36, s64\n"
    "s_addc_u32 s37, s37, 0\n"
    "s_and_b32 s37, s37, 0xffff\n"
    "s_mov_b32 s38, 12288\n"
    "s_mov_b32 s39, 0x20000\n"
    "s_lshl_b32 s64, s66, 14\n"
    "s_lshl_b32 s65, s67, 6\n"
    "s_add_u32 s64, s64, s65\n"
    "s_mov_b64 s[32:33], s[94:95]\n"
    "s_add_u32 s32, s32, s64\n"
    "s_addc_u32 s33, s33, 0\n"
    "s_and_b32 s33, s33, 0xffff\n"
    "s_mov_b32 s34, 16384\n"
    "s_mov_b32 s35, 0x20000\n"
    "s_lshl_b32 s64, s66, 4\n"
    "s_add_u32 s64, s64, s67\n"
    "s_lshl_b32 s64, s64, 10\n"
    "s_mov_b64 s[44:45], s[86:87]\n"
    "s_add_u32 s44, s44, s64\n"
    "s_addc_u32 s45, s45, 0\n"
    "s_and_b32 s45, s45, 0xffff\n"
    "s_mov_b32 s46, 1024\n"
    "s_mov_b32 s47, 0x20000\n"
    "s_lshl_b32 s64, s64, 1\n"
    "s_mov_b64 s[40:41], s[84:85]\n"
    "s_add_u32 s40, s40, s64\n"
    "s_addc_u32 s41, s41, 0\n"
    "s_and_b32 s41, s41, 0xffff\n"
    "s_mov_b32 s42, 2048\n"
    "s_mov_b32 s43, 0x20000\n"
    "s_lshl_b32 s64, s66, 11\n"
    "s_lshl_b32 s65, s67, 7\n"
    "s_add_u32 s64, s64, s65\n"
    "s_mov_b64 s[48:49], s[14:15]\n"
    "s_add_u32 s48, s48, s64\n"
    "s_addc_u32 s49, s49, 0\n"
    "s_and_b32 s49, s49, 0xffff\n"
    "s_mov_b32 s50, 128\n"
    "s_mov_b32 s51, 0x20000\n"
    "s_lshr_b32 s64, s53, 1\n"
    "s_lshl_b32 s64, s64, 7\n"
    "s_lshl_b32 s65, s52, 3\n"
    "s_add_u32 s64, s64, s65\n"
    "s_add_u32 s64, s64, 655360\n"
    "v_and_b32 v14, 1, v3\n"
    "v_lshlrev_b32 v14, 2, v14\n"
    "v_add_u32 v14, s64, v14\n"
    "s_mov_b32 s66, 200000\n"
    "L_wpa3_%=:\n"
    "global_load_dword v15, v14, s[96:97] offset:0 sc1\n"
    "s_waitcnt vmcnt(0)\n"
    "v_cmp_ne_u32 vcc, 1, v15\n"
    "s_sub_u32 s66, s66, 1\n"
    "s_cbranch_vccz L_wda3_%=\n"
    "s_sleep 2\n"
    "s_cmp_lg_u32 s66, 0\n"
    "s_cbranch_scc1 L_wpa3_%=\n"
    "L_wda3_%=:\n"
    "s_lshl_b32 s56, s63, 10\n"
    "s_add_u32 s58, s56, 0\n"
    "buffer_load_dwordx4 a[64:67], v2, s[24:27], s58 offen sc1\n"
    "s_add_u32 s58, s56, 131072\n"
    "buffer_load_dwordx4 a[68:71], v2, s[24:27], s58 offen sc1\n"
    "s_add_u32 s58, s56, 262144\n"
    "buffer_load_dwordx4 a[72:75], v2, s[24:27], s58 offen sc1\n"
    "s_add_u32 s58, s56, 393216\n"
    "buffer_load_dwordx4 a[76:79], v2, s[24:27], s58 offen sc1\n"
    "s_add_u32 s63, s63, 1\n"
    "s_cmp_eq_u32 s63, 8\n"
    "s_cselect_b32 s63, 0, s63\n"
    "s_lshl_b32 s56, s63, 10\n"
    "s_add_u32 s58, s56, 0\n"
    "buffer_load_dwordx4 a[80:83], v2, s[24:27], s58 offen sc1\n"
    "s_add_u32 s58, s56, 131072\n"
    "buffer_load_dwordx4 a[84:87], v2, s[24:27], s58 offen sc1\n"
    "s_add_u32 s58, s56, 262144\n"
    "buffer_load_dwordx4 a[88:91], v2, s[24:27], s58 offen sc1\n"
    "s_add_u32 s58, s56, 393216\n"
    "buffer_load_dwordx4 a[92:95], v2, s[24:27], s58 offen sc1\n"
    "s_add_u32 s63, s63, 1\n"
    "s_cmp_eq_u32 s63, 8\n"
    "s_cselect_b32 s63, 0, s63\n"
    "s_lshl_b32 s56, s63, 10\n"
    "s_add_u32 s58, s56, 0\n"
    "buffer_load_dwordx4 a[96:99], v2, s[24:27], s58 offen sc1\n"
    "s_add_u32 s58, s56, 131072\n"
    "buffer_load_dwordx4 a[100:103], v2, s[24:27], s58 offen sc1\n"
    "s_add_u32 s58, s56, 262144\n"
    "buffer_load_dwordx4 a[104:107], v2, s[24:27], s58 offen sc1\n"
    "s_add_u32 s58, s56, 393216\n"
    "buffer_load_dwordx4 a[108:111], v2, s[24:27], s58 offen sc1\n"
    "s_add_u32 s63, s63, 1\n"
    "s_cmp_eq_u32 s63, 8\n"
    "s_cselect_b32 s63, 0, s63\n"
    "s_lshl_b32 s56, s63, 10\n"
    "s_add_u32 s58, s56, 0\n"
    "buffer_load_dwordx4 a[112:115], v2, s[24:27], s58 offen sc1\n"
    "s_add_u32 s58, s56, 131072\n"
    "buffer_load_dwordx4 a[116:119], v2, s[24:27], s58 offen sc1\n"
    "s_add_u32 s58, s56, 262144\n"
    "buffer_load_dwordx4 a[120:123], v2, s[24:27], s58 offen sc1\n"
    "s_add_u32 s58, s56, 393216\n"
    "buffer_load_dwordx4 a[124:127], v2, s[24:27], s58 offen sc1\n"
    "s_add_u32 s63, s63, 1\n"
    "s_cmp_eq_u32 s63, 8\n"
    "s_cselect_b32 s63, 0, s63\n"
    "s_and_b32 s67, %1, 7\n"
    "s_mul_i32 s67, s67, 14\n"
    "s_lshr_b32 s64, %1, 3\n"
    "s_add_u32 s67, s67, s64\n"
    "s_cmp_lt_u32 s64, 14\n"
    "s_cselect_b64 s[68:69], s[90:91], s[92:93]\n"
    "s_cselect_b32 s64, 0, 14\n"
    "s_cselect_b32 s65, 0, 98\n"
    "s_sub_u32 s64, s67, s64\n"
    "s_add_u32 s67, s67, s65\n"
    "s_lshl_b32 s64, s64, 5\n"
    "s_mul_i32 s65, s52, 229376\n"
    "s_add_u32 s64, s64, s65\n"
    "s_cmp_lt_u32 %1, 224\n"
    "s_cselect_b32 s65, 229376, 0\n"
    "s_cselect_b32 s64, s64, 0\n"
    "s_cselect_b32 s66, 14400, 0\n"
    "s_mov_b64 s[68:69], s[68:69]\n"
    "s_add_u32 s68, s68, s64\n"
    "s_addc_u32 s69, s69, 0\n"
    "s_and_b32 s69, s69, 0xffff\n"
    "s_mov_b32 s70, s65\n"
    "s_mov_b32 s71, 0x20000\n"
    "s_lshl_b32 s64, s52, 8\n"
    "s_mov_b64 s[72:73], s[96:97]\n"
    "s_add_u32 s72, s72, s64\n"
    "s_addc_u32 s73, s73, 0\n"
    "s_and_b32 s73, s73, 0xffff\n"
    "s_mov_b32 s74, 10240\n"
    "s_mov_b32 s75, 0x20000\n"
    "s_lshl_b32 s64, s67, 6\n"
    "s_cmp_lt_u32 %1, 224\n"
    "s_cselect_b32 s64, s64, 0\n"
    "s_mov_b64 s[76:77], s[16:17]\n"
    "s_add_u32 s76, s76, s64\n"
    "s_addc_u32 s77, s77, 0\n"
    "s_and_b32 s77, s77, 0xffff\n"
    "s_mov_b32 s78, s66\n"
    "s_mov_b32 s79, 0x20000\n"
    "v_lshrrev_b32 v7, 1, v3\n"
    "v_mul_u32_u24 v124, 3584, v7\n"
    "v_and_b32 v7, 1, v3\n"
    "v_lshl_add_u32 v124, v7, 4, v124\n"
    "v_and_b32 v7, 15, v3\n"
    "v_lshlrev_b32 v8, 11, v7\n"
    "v_sub_u32 v8, 8192, v8\n"
    "v_add_u32 v125, v8, v12\n"
    "v_mov_b32 v9, 0x100000\n"
    "v_cmp_lt_u32 vcc, 1, v7\n"
    "v_cmp_eq_u32 s[80:81], 0, v7\n"
    "v_mul_u32_u24 v127, 14336, v7\n"
    "v_add_u32 v127, v127, v12\n"
    "v_cndmask_b32 v125, v125, v9, vcc\n"
    "v_cndmask_b32 v126, 0, 1.0, s[80:81]\n"
    "s_lshl_b32 s64, s52, 11\n"
    "s_add_u32 s64, s64, 131072\n"
    "s_mov_b32 m0, s64\n"
    "s_mov_b32 s65, 114688\n"
    "buffer_load_dwordx4 v124, s[68:71], 0 offen lds\n"
    "s_add_u32 m0, s64, 1024\n"
    "s_nop 0\n"
    "buffer_load_dwordx4 v124, s[68:71], s65 offen lds\n"
    "buffer_load_dwordx4 v[100:103], v125, s[72:75], 0 offen offset:0 sc1\n"
    "buffer_load_dwordx4 v[104:107], v125, s[72:75], 0 offen offset:64 sc1\n"
    "buffer_load_dwordx4 v[108:111], v125, s[72:75], 0 offen offset:128 sc1\n"
    "buffer_load_dwordx4 v[112:115], v125, s[72:75], 0 offen offset:192 sc1\n"
    "v_lshrrev_b32 v7, 2, v3\n"
    "v_lshlrev_b32 v124, 5, v7\n"
    "v_and_b32 v7, 3, v3\n"
    "v_lshl_add_u32 v124, v7, 3, v124\n"
    "v_add_u32 v124, s64, v124\n"
    "s_waitcnt vmcnt(18)\n"
    "ds_read_b64_tr_b16 v[16:17], v5 offset:0\n"
    "ds_read_b64_tr_b16 v[18:19], v5 offset:2048\n"
    "ds_read_b64_tr_b16 v[20:21], v6 offset:0\n"
    "ds_read_b64_tr_b16 v[22:23], v6 offset:2048\n"
    "ds_read_b64_tr_b16 v[24:25], v5 offset:512\n"
    "ds_read_b64_tr_b16 v[26:27], v5 offset:2560\n"
    "ds_read_b64_tr_b16 v[28:29], v6 offset:512\n"
    "ds_read_b64_tr_b16 v[30:31], v6 offset:2560\n"
    "s_waitcnt lgkmcnt(0)\n"
    "v_mfma_f32_16x16x32_bf16 a[0:3], v[16:19], a[64:67], 0\n"
    "s_lshl_b32 s60, s61, 15\n"
    "s_add_u32 m0, s55, 0\n"
    "s_add_u32 s57, s60, 0\n"
    "buffer_load_dwordx4 v4, s[28:31], s57 offen lds\n"
    "v_mfma_f32_16x16x32_bf16 a[4:7], v[20:23], a[64:67], 0\n"
    "s_waitcnt vmcnt(15)\n"
    "ds_read_b64_tr_b16 v[32:33], v5 offset:4096\n"
    "ds_read_b64_tr_b16 v[34:35], v5 offset:6144\n"
    "v_mfma_f32_16x16x32_bf16 a[8:11], v[24:27], a[64:67], 0\n"
    "ds_read_b64_tr_b16 v[36:37], v6 offset:4096\n"
    "ds_read_b64_tr_b16 v[38:39], v6 offset:6144\n"
    "v_mfma_f32_16x16x32_bf16 a[12:15], v[28:31], a[64:67], 0\n"
    "s_lshl_b32 s56, s63, 10\n"
    "s_add_u32 s58, s56, 0\n"
    "buffer_load_dwordx4 a[64:67], v2, s[24:27], s58 offen sc1\n"
    "v_mfma_f32_16x16x32_bf16 a[16:19], v[16:19], a[68:71], 0\n"
    "s_add_u32 m0, s55, 1024\n"
    "s_add_u32 s57, s60, 8192\n"
    "buffer_load_dwordx4 v4, s[28:31], s57 offen lds\n"
    "v_mfma_f32_16x16x32_bf16 a[20:23], v[20:23], a[68:71], 0\n"
    "ds_read_b64_tr_b16 v[40:41], v5 offset:4608\n"
    "ds_read_b64_tr_b16 v[42:43], v5 offset:6656\n"
    "v_mfma_f32_16x16x32_bf16 a[24:27], v[24:27], a[68:71], 0\n"
    "ds_read_b64_tr_b16 v[44:45], v6 offset:4608\n"
    "ds_read_b64_tr_b16 v[46:47], v6 offset:6656\n"
    "v_mfma_f32_16x16x32_bf16 a[28:31], v[28:31], a[68:71], 0\n"
    "s_add_u32 s58, s56, 131072\n"
    "buffer_load_dwordx4 a[68:71], v2, s[24:27], s58 offen sc1\n"
    "v_mfma_f32_16x16x32_bf16 a[32:35], v[16:19], a[72:75], 0\n"
    "s_add_u32 m0, s55, 2048\n"
    "s_add_u32 s57, s60, 16384\n"
    "buffer_load_dwordx4 v4, s[28:31], s57 offen lds\n"
    "v_mfma_f32_16x16x32_bf16 a[36:39], v[20:23], a[72:75], 0\n"
    "v_mfma_f32_16x16x32_bf16 a[40:43], v[24:27], a[72:75], 0\n"
    "v_mfma_f32_16x16x32_bf16 a[44:47], v[28:31], a[72:75], 0\n"
    "s_add_u32 s58, s56, 262144\n"
    "buffer_load_dwordx4 a[72:75], v2, s[24:27], s58 offen sc1\n"
    "v_mfma_f32_16x16x32_bf16 a[48:51], v[16:19], a[76:79], 0\n"
    "s_add_u32 m0, s55, 3072\n"
    "s_add_u32 s57, s60, 24576\n"
    "buffer_load_dwordx4 v4, s[28:31], s57 offen lds\n"
    "s_add_u32 s61, s61, 1\n"
    "s_cmp_eq_u32 s61, 8\n"
    "s_cselect_b32 s61, 0, s61\n"
    "v_mfma_f32_16x16x32_bf16 a[52:55], v[20:23], a[76:79], 0\n"
    "v_mfma_f32_16x16x32_bf16 a[56:59], v[24:27], a[76:79], 0\n"
    "v_mfma_f32_16x16x32_bf16 a[60:63], v[28:31], a[76:79], 0\n"
    "s_add_u32 s58, s56, 393216\n"
    "buffer_load_dwordx4 a[76:79], v2, s[24:27], s58 offen sc1\n"
    "s_add_u32 s63, s63, 1\n"
    "s_cmp_eq_u32 s63, 8\n"
    "s_cselect_b32 s63, 0, s63\n"
    "global_load_dword v15, v14, s[96:97] offset:64 sc1\n"
    "s_waitcnt lgkmcnt(0)\n"
    "v_mfma_f32_16x16x32_bf16 a[0:3], v[32:35], a[80:83], a[0:3]\n"
    "s_lshl_b32 s60, s61, 15\n"
    "s_add_u32 m0, s55, 4096\n"
    "s_add_u32 s57, s60, 0\n"
    "buffer_load_dwordx4 v4, s[28:31], s57 offen lds\n"
    "v_mfma_f32_16x16x32_bf16 a[4:7], v[36:39], a[80:83], a[4:7]\n"
    "s_waitcnt vmcnt(20)\n"
    "ds_read_b64_tr_b16 v[16:17], v5 offset:8192\n"
    "ds_read_b64_tr_b16 v[18:19], v5 offset:10240\n"
    "v_mfma_f32_16x16x32_bf16 a[8:11], v[40:43], a[80:83], a[8:11]\n"
    "ds_read_b64_tr_b16 v[20:21], v6 offset:8192\n"
    "ds_read_b64_tr_b16 v[22:23], v6 offset:10240\n"
    "v_mfma_f32_16x16x32_bf16 a[12:15], v[44:47], a[80:83], a[12:15]\n"
    "s_lshl_b32 s56, s63, 10\n"
    "s_add_u32 s58, s56, 0\n"
    "buffer_load_dwordx4 a[80:83], v2, s[24:27], s58 offen sc1\n"
    "v_mfma_f32_16x16x32_bf16 a[16:19], v[32:35], a[84:87], a[16:19]\n"
    "s_add_u32 m0, s55, 5120\n"
    "s_add_u32 s57, s60, 8192\n"
    "buffer_load_dwordx4 v4, s[28:31], s57 offen lds\n"
    "v_mfma_f32_16x16x32_bf16 a[20:23], v[36:39], a[84:87], a[20:23]\n"
    "ds_read_b64_tr_b16 v[24:25], v5 offset:8704\n"
    "ds_read_b64_tr_b16 v[26:27], v5 offset:10752\n"
    "v_mfma_f32_16x16x32_bf16 a[24:27], v[40:43], a[84:87], a[24:27]\n"
    "ds_read_b64_tr_b16 v[28:29], v6 offset:8704\n"
    "ds_read_b64_tr_b16 v[30:31], v6 offset:10752\n"
    "v_mfma_f32_16x16x32_bf16 a[28:31], v[44:47], a[84:87], a[28:31]\n"
    "s_add_u32 s58, s56, 131072\n"
    "buffer_load_dwordx4 a[84:87], v2, s[24:27], s58 offen sc1\n"
    "v_mfma_f32_16x16x32_bf16 a[32:35], v[32:35], a[88:91], a[32:35]\n"
    "s_add_u32 m0, s55, 6144\n"
    "s_add_u32 s57, s60, 16384\n"
    "buffer_load_dwordx4 v4, s[28:31], s57 offen lds\n"
    "v_mfma_f32_16x16x32_bf16 a[36:39], v[36:39], a[88:91], a[36:39]\n"
    "v_mfma_f32_16x16x32_bf16 a[40:43], v[40:43], a[88:91], a[40:43]\n"
    "v_mfma_f32_16x16x32_bf16 a[44:47], v[44:47], a[88:91], a[44:47]\n"
    "s_add_u32 s58, s56, 262144\n"
    "buffer_load_dwordx4 a[88:91], v2, s[24:27], s58 offen sc1\n"
    "v_mfma_f32_16x16x32_bf16 a[48:51], v[32:35], a[92:95], a[48:51]\n"
    "s_add_u32 m0, s55, 7168\n"
    "s_add_u32 s57, s60, 24576\n"
    "buffer_load_dwordx4 v4, s[28:31], s57 offen lds\n"
    "s_add_u32 s61, s61, 1\n"
    "s_cmp_eq_u32 s61, 8\n"
    "s_cselect_b32 s61, 0, s61\n"
    "v_mfma_f32_16x16x32_bf16 a[52:55], v[36:39], a[92:95], a[52:55]\n"
    "v_mfma_f32_16x16x32_bf16 a[56:59], v[40:43], a[92:95], a[56:59]\n"
    "v_mfma_f32_16x16x32_bf16 a[60:63], v[44:47], a[92:95], a[60:63]\n"
    "s_add_u32 s58, s56, 393216\n"
    "buffer_load_dwordx4 a[92:95], v2, s[24:27], s58 offen sc1\n"
    "s_add_u32 s63, s63, 1\n"
    "s_cmp_eq_u32 s63, 8\n"
    "s_cselect_b32 s63, 0, s63\n"
    "s_waitcnt lgkmcnt(0)\n"
    "v_mfma_f32_16x16x32_bf16 a[0:3], v[16:19], a[96:99], a[0:3]\n"
    "s_lshl_b32 s60, s61, 15\n"
    "s_add_u32 m0, s55, 8192\n"
    "s_add_u32 s57, s60, 0\n"
    "buffer_load_dwordx4 v4, s[28:31], s57 offen lds\n"
    "v_mfma_f32_16x16x32_bf16 a[4:7], v[20:23], a[96:99], a[4:7]\n"
    "s_waitcnt vmcnt(24)\n"
    "ds_read_b64_tr_b16 v[32:33], v5 offset:12288\n"
    "ds_read_b64_tr_b16 v[34:35], v5 offset:14336\n"
    "v_mfma_f32_16x16x32_bf16 a[8:11], v[24:27], a[96:99], a[8:11]\n"
    "ds_read_b64_tr_b16 v[36:37], v6 offset:12288\n"
    "ds_read_b64_tr_b16 v[38:39], v6 offset:14336\n"
    "v_mfma_f32_16x16x32_bf16 a[12:15], v[28:31], a[96:99], a[12:15]\n"
    "s_lshl_b32 s56, s63, 10\n"
    "s_add_u32 s58, s56, 0\n"
    "buffer_load_dwordx4 a[96:99], v2, s[24:27], s58 offen sc1\n"
    "v_mfma_f32_16x16x32_bf16 a[16:19], v[16:19], a[100:103], a[16:19]\n"
    "s_add_u32 m0, s55, 9216\n"
    "s_add_u32 s57, s60, 8192\n"
    "buffer_load_dwordx4 v4, s[28:31], s57 offen lds\n"
    "v_mfma_f32_16x16x32_bf16 a[20:23], v[20:23], a[100:103], a[20:23]\n"
    "ds_read_b64_tr_b16 v[40:41], v5 offset:12800\n"
    "ds_read_b64_tr_b16 v[42:43], v5 offset:14848\n"
    "v_mfma_f32_16x16x32_bf16 a[24:27], v[24:27], a[100:103], a[24:27]\n"
    "ds_read_b64_tr_b16 v[44:45], v6 offset:12800\n"
    "ds_read_b64_tr_b16 v[46:47], v6 offset:14848\n"
    "v_mfma_f32_16x16x32_bf16 a[28:31], v[28:31], a[100:103], a[28:31]\n"
    "s_add_u32 s58, s56, 131072\n"
    "buffer_load_dwordx4 a[100:103], v2, s[24:27], s58 offen sc1\n"
    "v_mfma_f32_16x16x32_bf16 a[32:35], v[16:19], a[104:107], a[32:35]\n"
    "s_add_u32 m0, s55, 10240\n"
    "s_add_u32 s57, s60, 16384\n"
    "buffer_load_dwordx4 v4, s[28:31], s57 offen lds\n"
    "v_mfma_f32_16x16x32_bf16 a[36:39], v[20:23], a[104:107], a[36:39]\n"
    "v_mfma_f32_16x16x32_bf16 a[40:43], v[24:27], a[104:107], a[40:43]\n"
    "v_mfma_f32_16x16x32_bf16 a[44:47], v[28:31], a[104:107], a[44:47]\n"
    "s_add_u32 s58, s56, 262144\n"
    "buffer_load_dwordx4 a[104:107], v2, s[24:27], s58 offen sc1\n"
    "v_mfma_f32_16x16x32_bf16 a[48:51], v[16:19], a[108:111], a[48:51]\n"
    "s_add_u32 m0, s55, 11264\n"
    "s_add_u32 s57, s60, 24576\n"
    "buffer_load_dwordx4 v4, s[28:31], s57 offen lds\n"
    "s_add_u32 s61, s61, 1\n"
    "s_cmp_eq_u32 s61, 8\n"
    "s_cselect_b32 s61, 0, s61\n"
    "v_mfma_f32_16x16x32_bf16 a[52:55], v[20:23], a[108:111], a[52:55]\n"
    "v_mfma_f32_16x16x32_bf16 a[56:59], v[24:27], a[108:111], a[56:59]\n"
    "v_mfma_f32_16x16x32_bf16 a[60:63], v[28:31], a[108:111], a[60:63]\n"
    "s_add_u32 s58, s56, 393216\n"
    "buffer_load_dwordx4 a[108:111], v2, s[24:27], s58 offen sc1\n"
    "s_add_u32 s63, s63, 1\n"
    "s_cmp_eq_u32 s63, 8\n"
    "s_cselect_b32 s63, 0, s63\n"
    "s_waitcnt lgkmcnt(0)\n"
    "v_mfma_f32_16x16x32_bf16 a[0:3], v[32:35], a[112:115], a[0:3]\n"
    "s_lshl_b32 s60, s61, 15\n"
    "s_add_u32 m0, s55, 12288\n"
    "s_add_u32 s57, s60, 0\n"
    "buffer_load_dwordx4 v4, s[28:31], s57 offen lds\n"
    "v_mfma_f32_16x16x32_bf16 a[4:7], v[36:39], a[112:115], a[4:7]\n"
    "s_waitcnt vmcnt(18)\n"
    "ds_read_b64_tr_b16 v[16:17], v5 offset:0\n"
    "ds_read_b64_tr_b16 v[18:19], v5 offset:2048\n"
    "v_mfma_f32_16x16x32_bf16 a[8:11], v[40:43], a[112:115], a[8:11]\n"
    "ds_read_b64_tr_b16 v[20:21], v6 offset:0\n"
    "ds_read_b64_tr_b16 v[22:23], v6 offset:2048\n"
    "v_mfma_f32_16x16x32_bf16 a[12:15], v[44:47], a[112:115], a[12:15]\n"
    "s_lshl_b32 s56, s63, 10\n"
    "s_add_u32 s58, s56, 0\n"
    "buffer_load_dwordx4 a[112:115], v2, s[24:27], s58 offen sc1\n"
    "v_mfma_f32_16x16x32_bf16 a[16:19], v[32:35], a[116:119], a[16:19]\n"
    "s_add_u32 m0, s55, 13312\n"
    "s_add_u32 s57, s60, 8192\n"
    "buffer_load_dwordx4 v4, s[28:31], s57 offen lds\n"
    "v_mfma_f32_16x16x32_bf16 a[20:23], v[36:39], a[116:119], a[20:23]\n"
    "ds_read_b64_tr_b16 v[24:25], v5 offset:512\n"
    "ds_read_b64_tr_b16 v[26:27], v5 offset:2560\n"
    "v_mfma_f32_16x16x32_bf16 a[24:27], v[40:43], a[116:119], a[24:27]\n"
    "ds_read_b64_tr_b16 v[28:29], v6 offset:512\n"
    "ds_read_b64_tr_b16 v[30:31], v6 offset:2560\n"
    "v_mfma_f32_16x16x32_bf16 a[28:31], v[44:47], a[116:119], a[28:31]\n"
    "s_add_u32 s58, s56, 131072\n"
    "buffer_load_dwordx4 a[116:119], v2, s[24:27], s58 offen sc1\n"
    "v_mfma_f32_16x16x32_bf16 a[32:35], v[32:35], a[120:123], a[32:35]\n"
    "s_add_u32 m0, s55, 14336\n"
    "s_add_u32 s57, s60, 16384\n"
    "buffer_load_dwordx4 v4, s[28:31], s57 offen lds\n"
    "v_mfma_f32_16x16x32_bf16 a[36:39], v[36:39], a[120:123], a[36:39]\n"
    "v_mfma_f32_16x16x32_bf16 a[40:43], v[40:43], a[120:123], a[40:43]\n"
    "v_mfma_f32_16x16x32_bf16 a[44:47], v[44:47], a[120:123], a[44:47]\n"
    "s_add_u32 s58, s56, 262144\n"
    "buffer_load_dwordx4 a[120:123], v2, s[24:27], s58 offen sc1\n"
    "v_mfma_f32_16x16x32_bf16 a[48:51], v[32:35], a[124:127], a[48:51]\n"
    "s_add_u32 m0, s55, 15360\n"
    "s_add_u32 s57, s60, 24576\n"
    "buffer_load_dwordx4 v4, s[28:31], s57 offen lds\n"
    "s_add_u32 s61, s61, 1\n"
    "s_cmp_eq_u32 s61, 8\n"
    "s_cselect_b32 s61, 0, s61\n"
    "v_mfma_f32_16x16x32_bf16 a[52:55], v[36:39], a[124:127], a[52:55]\n"
    "v_mfma_f32_16x16x32_bf16 a[56:59], v[40:43], a[124:127], a[56:59]\n"
    "v_mfma_f32_16x16x32_bf16 a[60:63], v[44:47], a[124:127], a[60:63]\n"
    "s_add_u32 s58, s56, 393216\n"
    "buffer_load_dwordx4 a[124:127], v2, s[24:27], s58 offen sc1\n"
    "s_add_u32 s63, s63, 1\n"
    "s_cmp_eq_u32 s63, 8\n"
    "s_cselect_b32 s63, 0, s63\n"
    "ds_read_b64_tr_b16 v[116:117], v124 offset:0\n"
    "ds_read_b64_tr_b16 v[118:119], v124 offset:512\n"
    "ds_read_b64_tr_b16 v[120:121], v124 offset:1024\n"
    "ds_read_b64_tr_b16 v[122:123], v124 offset:1536\n"
    "v_add_f32 v100, v100, v126\n"
    "v_add_f32 v101, v101, v126\n"
    "v_add_f32 v102, v102, v126\n"
    "v_add_f32 v103, v103, v126\n"
    "v_add_f32 v104, v104, v126\n"
    "v_add_f32 v105, v105, v126\n"
    "v_add_f32 v106, v106, v126\n"
    "v_add_f32 v107, v107, v126\n"
    "v_add_f32 v108, v108, v126\n"
    "v_add_f32 v109, v109, v126\n"
    "v_add_f32 v110, v110, v126\n"
    "v_add_f32 v111, v111, v126\n"
    "v_add_f32 v112, v112, v126\n"
    "v_add_f32 v113, v113, v126\n"
    "v_add_f32 v114, v114, v126\n"
    "v_add_f32 v115, v115, v126\n"
    "v_cvt_pk_bf16_f32 v100, v100, v101\n"
    "v_cvt_pk_bf16_f32 v101, v102, v103\n"
    "v_cvt_pk_bf16_f32 v102, v104, v105\n"
    "v_cvt_pk_bf16_f32 v103, v106, v107\n"
    "v_cvt_pk_bf16_f32 v108, v108, v109\n"
    "v_cvt_pk_bf16_f32 v109, v110, v111\n"
    "v_cvt_pk_bf16_f32 v110, v112, v113\n"
    "v_cvt_pk_bf16_f32 v111, v114, v115\n"
    "s_waitcnt lgkmcnt(0)\n"
    "v_mfma_f32_16x16x32_bf16 v[48:51], v[116:119], v[100:103], 0\n"
    "v_mfma_f32_16x16x32_bf16 v[48:51], v[120:123], v[108:111], v[48:51]\n"
    "s_waitcnt vmcnt(24)\n"
    "v_cmp_ne_u32 vcc, 1, v15\n"
    "s_cbranch_vccz L_wdb3_%=\n"
    "s_mov_b32 s66, 200000\n"
    "L_wpb3_%=:\n"
    "global_load_dword v15, v14, s[96:97] offset:64 sc1\n"
    "s_waitcnt vmcnt(0)\n"
    "v_cmp_ne_u32 vcc, 1, v15\n"
    "s_sub_u32 s66, s66, 1\n"
    "s_cbranch_vccz L_wdb3_%=\n"
    "s_sleep 2\n"
    "s_cmp_lg_u32 s66, 0\n"
    "s_cbranch_scc1 L_wpb3_%=\n"
    "L_wdb3_%=:\n"
    "v_mfma_f32_16x16x32_bf16 a[0:3], v[16:19], a[64:67], a[0:3]\n"
    "s_lshl_b32 s60, s61, 15\n"
    "s_add_u32 m0, s55, 0\n"
    "s_add_u32 s57, s60, 2097152\n"
    "buffer_load_dwordx4 v4, s[28:31], s57 offen lds\n"
    "v_mfma_f32_16x16x32_bf16 a[4:7], v[20:23], a[64:67], a[4:7]\n"
    "s_waitcnt vmcnt(17)\n"
    "ds_read_b64_tr_b16 v[32:33], v5 offset:4096\n"
    "ds_read_b64_tr_b16 v[34:35], v5 offset:6144\n"
    "v_mfma_f32_16x16x32_bf16 a[8:11], v[24:27], a[64:67], a[8:11]\n"
    "ds_read_b64_tr_b16 v[36:37], v6 offset:4096\n"
    "ds_read_b64_tr_b16 v[38:39], v6 offset:6144\n"
    "v_mfma_f32_16x16x32_bf16 a[12:15], v[28:31], a[64:67], a[12:15]\n"
    "s_lshl_b32 s56, s63, 10\n"
    "s_add_u32 s58, s56, 65536\n"
    "buffer_load_dwordx4 a[64:67], v2, s[24:27], s58 offen sc1\n"
    "v_mfma_f32_16x16x32_bf16 a[16:19], v[16:19], a[68:71], a[16:19]\n"
    "s_add_u32 m0, s55, 1024\n"
    "s_add_u32 s57, s60, 2105344\n"
    "buffer_load_dwordx4 v4, s[28:31], s57 offen lds\n"
    "v_mfma_f32_16x16x32_bf16 a[20:23], v[20:23], a[68:71], a[20:23]\n"
    "ds_read_b64_tr_b16 v[40:41], v5 offset:4608\n"
    "ds_read_b64_tr_b16 v[42:43], v5 offset:6656\n"
    "v_mfma_f32_16x16x32_bf16 a[24:27], v[24:27], a[68:71], a[24:27]\n"
    "ds_read_b64_tr_b16 v[44:45], v6 offset:4608\n"
    "ds_read_b64_tr_b16 v[46:47], v6 offset:6656\n"
    "v_mfma_f32_16x16x32_bf16 a[28:31], v[28:31], a[68:71], a[28:31]\n"
    "s_add_u32 s58, s56, 196608\n"
    "buffer_load_dwordx4 a[68:71], v2, s[24:27], s58 offen sc1\n"
    "v_mfma_f32_16x16x32_bf16 a[32:35], v[16:19], a[72:75], a[32:35]\n"
    "s_add_u32 m0, s55, 2048\n"
    "s_add_u32 s57, s60, 2113536\n"
    "buffer_load_dwordx4 v4, s[28:31], s57 offen lds\n"
    "v_mfma_f32_16x16x32_bf16 a[36:39], v[20:23], a[72:75], a[36:39]\n"
    "v_mfma_f32_16x16x32_bf16 a[40:43], v[24:27], a[72:75], a[40:43]\n"
    "v_mfma_f32_16x16x32_bf16 a[44:47], v[28:31], a[72:75], a[44:47]\n"
    "s_add_u32 s58, s56, 327680\n"
    "buffer_load_dwordx4 a[72:75], v2, s[24:27], s58 offen sc1\n"
    "v_mfma_f32_16x16x32_bf16 a[48:51], v[16:19], a[76:79], a[48:51]\n"
    "s_add_u32 m0, s55, 3072\n"
    "s_add_u32 s57, s60, 2121728\n"
    "buffer_load_dwordx4 v4, s[28:31], s57 offen lds\n"
    "s_add_u32 s61, s61, 1\n"
    "s_cmp_eq_u32 s61, 8\n"
    "s_cselect_b32 s61, 0, s61\n"
    "v_mfma_f32_16x16x32_bf16 a[52:55], v[20:23], a[76:79], a[52:55]\n"
    "v_mfma_f32_16x16x32_bf16 a[56:59], v[24:27], a[76:79], a[56:59]\n"
    "v_mfma_f32_16x16x32_bf16 a[60:63], v[28:31], a[76:79], a[60:63]\n"
    "s_add_u32 s58, s56, 458752\n"
    "buffer_load_dwordx4 a[76:79], v2, s[24:27], s58 offen sc1\n"
    "s_add_u32 s63, s63, 1\n"
    "s_cmp_eq_u32 s63, 8\n"
    "s_cselect_b32 s63, 0, s63\n"
    "s_waitcnt lgkmcnt(0)\n"
    "v_mfma_f32_16x16x32_bf16 a[0:3], v[32:35], a[80:83], a[0:3]\n"
    "s_lshl_b32 s60, s61, 15\n"
    "s_add_u32 m0, s55, 4096\n"
    "s_add_u32 s57, s60, 2097152\n"
    "buffer_load_dwordx4 v4, s[28:31], s57 offen lds\n"
    "v_mfma_f32_16x16x32_bf16 a[4:7], v[36:39], a[80:83], a[4:7]\n"
    "s_waitcnt vmcnt(17)\n"
    "ds_read_b64_tr_b16 v[16:17], v5 offset:8192\n"
    "ds_read_b64_tr_b16 v[18:19], v5 offset:10240\n"
    "v_mfma_f32_16x16x32_bf16 a[8:11], v[40:43], a[80:83], a[8:11]\n"
    "ds_read_b64_tr_b16 v[20:21], v6 offset:8192\n"
    "ds_read_b64_tr_b16 v[22:23], v6 offset:10240\n"
    "v_mfma_f32_16x16x32_bf16 a[12:15], v[44:47], a[80:83], a[12:15]\n"
    "s_lshl_b32 s56, s63, 10\n"
    "s_add_u32 s58, s56, 65536\n"
    "buffer_load_dwordx4 a[80:83], v2, s[24:27], s58 offen sc1\n"
    "v_mfma_f32_16x16x32_bf16 a[16:19], v[32:35], a[84:87], a[16:19]\n"
    "s_add_u32 m0, s55, 5120\n"
    "s_add_u32 s57, s60, 2105344\n"
    "buffer_load_dwordx4 v4, s[28:31], s57 offen lds\n"
    "v_mfma_f32_16x16x32_bf16 a[20:23], v[36:39], a[84:87], a[20:23]\n"
    "ds_read_b64_tr_b16 v[24:25], v5 offset:8704\n"
    "ds_read_b64_tr_b16 v[26:27], v5 offset:10752\n"
    "v_mfma_f32_16x16x32_bf16 a[24:27], v[40:43], a[84:87], a[24:27]\n"
    "ds_read_b64_tr_b16 v[28:29], v6 offset:8704\n"
    "ds_read_b64_tr_b16 v[30:31], v6 offset:10752\n"
    "v_mfma_f32_16x16x32_bf16 a[28:31], v[44:47], a[84:87], a[28:31]\n"
    "s_add_u32 s58, s56, 196608\n"
    "buffer_load_dwordx4 a[84:87], v2, s[24:27], s58 offen sc1\n"
    "v_mfma_f32_16x16x32_bf16 a[32:35], v[32:35], a[88:91], a[32:35]\n"
    "s_add_u32 m0, s55, 6144\n"
    "s_add_u32 s57, s60, 2113536\n"
    "buffer_load_dwordx4 v4, s[28:31], s57 offen lds\n"
    "v_mfma_f32_16x16x32_bf16 a[36:39], v[36:39], a[88:91], a[36:39]\n"
    "v_mfma_f32_16x16x32_bf16 a[40:43], v[40:43], a[88:91], a[40:43]\n"
    "v_mfma_f32_16x16x32_bf16 a[44:47], v[44:47], a[88:91], a[44:47]\n"
    "s_add_u32 s58, s56, 327680\n"
    "buffer_load_dwordx4 a[88:91], v2, s[24:27], s58 offen sc1\n"
    "v_mfma_f32_16x16x32_bf16 a[48:51], v[32:35], a[92:95], a[48:51]\n"
    "s_add_u32 m0, s55, 7168\n"
    "s_add_u32 s57, s60, 2121728\n"
    "buffer_load_dwordx4 v4, s[28:31], s57 offen lds\n"
    "s_add_u32 s61, s61, 1\n"
    "s_cmp_eq_u32 s61, 8\n"
    "s_cselect_b32 s61, 0, s61\n"
    "v_mfma_f32_16x16x32_bf16 a[52:55], v[36:39], a[92:95], a[52:55]\n"
    "v_mfma_f32_16x16x32_bf16 a[56:59], v[40:43], a[92:95], a[56:59]\n"
    "v_mfma_f32_16x16x32_bf16 a[60:63], v[44:47], a[92:95], a[60:63]\n"
    "s_add_u32 s58, s56, 458752\n"
    "buffer_load_dwordx4 a[92:95], v2, s[24:27], s58 offen sc1\n"
    "s_add_u32 s63, s63, 1\n"
    "s_cmp_eq_u32 s63, 8\n"
    "s_cselect_b32 s63, 0, s63\n"
    "s_waitcnt lgkmcnt(0)\n"
    "v_mfma_f32_16x16x32_bf16 a[0:3], v[16:19], a[96:99], a[0:3]\n"
    "s_lshl_b32 s60, s61, 15\n"
    "s_add_u32 m0, s55, 8192\n"
    "s_add_u32 s57, s60, 2097152\n"
    "buffer_load_dwordx4 v4, s[28:31], s57 offen lds\n"
    "v_mfma_f32_16x16x32_bf16 a[4:7], v[20:23], a[96:99], a[4:7]\n"
    "s_waitcnt vmcnt(17)\n"
    "ds_read_b64_tr_b16 v[32:33], v5 offset:12288\n"
    "ds_read_b64_tr_b16 v[34:35], v5 offset:14336\n"
    "v_mfma_f32_16x16x32_bf16 a[8:11], v[24:27], a[96:99], a[8:11]\n"
    "ds_read_b64_tr_b16 v[36:37], v6 offset:12288\n"
    "ds_read_b64_tr_b16 v[38:39], v6 offset:14336\n"
    "v_mfma_f32_16x16x32_bf16 a[12:15], v[28:31], a[96:99], a[12:15]\n"
    "s_lshl_b32 s56, s63, 10\n"
    "s_add_u32 s58, s56, 65536\n"
    "buffer_load_dwordx4 a[96:99], v2, s[24:27], s58 offen sc1\n"
    "v_mfma_f32_16x16x32_bf16 a[16:19], v[16:19], a[100:103], a[16:19]\n"
    "s_add_u32 m0, s55, 9216\n"
    "s_add_u32 s57, s60, 2105344\n"
    "buffer_load_dwordx4 v4, s[28:31], s57 offen lds\n"
    "v_mfma_f32_16x16x32_bf16 a[20:23], v[20:23], a[100:103], a[20:23]\n"
    "ds_read_b64_tr_b16 v[40:41], v5 offset:12800\n"
    "ds_read_b64_tr_b16 v[42:43], v5 offset:14848\n"
    "v_mfma_f32_16x16x32_bf16 a[24:27], v[24:27], a[100:103], a[24:27]\n"
    "ds_read_b64_tr_b16 v[44:45], v6 offset:12800\n"
    "ds_read_b64_tr_b16 v[46:47], v6 offset:14848\n"
    "v_mfma_f32_16x16x32_bf16 a[28:31], v[28:31], a[100:103], a[28:31]\n"
    "s_add_u32 s58, s56, 196608\n"
    "buffer_load_dwordx4 a[100:103], v2, s[24:27], s58 offen sc1\n"
    "v_mfma_f32_16x16x32_bf16 a[32:35], v[16:19], a[104:107], a[32:35]\n"
    "s_add_u32 m0, s55, 10240\n"
    "s_add_u32 s57, s60, 2113536\n"
    "buffer_load_dwordx4 v4, s[28:31], s57 offen lds\n"
    "v_mfma_f32_16x16x32_bf16 a[36:39], v[20:23], a[104:107], a[36:39]\n"
    "v_mfma_f32_16x16x32_bf16 a[40:43], v[24:27], a[104:107], a[40:43]\n"
    "v_mfma_f32_16x16x32_bf16 a[44:47], v[28:31], a[104:107], a[44:47]\n"
    "s_add_u32 s58, s56, 327680\n"
    "buffer_load_dwordx4 a[104:107], v2, s[24:27], s58 offen sc1\n"
    "v_mfma_f32_16x16x32_bf16 a[48:51], v[16:19], a[108:111], a[48:51]\n"
    "s_add_u32 m0, s55, 11264\n"
    "s_add_u32 s57, s60, 2121728\n"
    "buffer_load_dwordx4 v4, s[28:31], s57 offen lds\n"
    "s_add_u32 s61, s61, 1\n"
    "s_cmp_eq_u32 s61, 8\n"
    "s_cselect_b32 s61, 0, s61\n"
    "v_mfma_f32_16x16x32_bf16 a[52:55], v[20:23], a[108:111], a[52:55]\n"
    "v_mfma_f32_16x16x32_bf16 a[56:59], v[24:27], a[108:111], a[56:59]\n"
    "v_mfma_f32_16x16x32_bf16 a[60:63], v[28:31], a[108:111], a[60:63]\n"
    "s_add_u32 s58, s56, 458752\n"
    "buffer_load_dwordx4 a[108:111], v2, s[24:27], s58 offen sc1\n"
    "s_add_u32 s63, s63, 1\n"
    "s_cmp_eq_u32 s63, 8\n"
    "s_cselect_b32 s63, 0, s63\n"
    "s_waitcnt lgkmcnt(0)\n"
    "v_mfma_f32_16x16x32_bf16 a[0:3], v[32:35], a[112:115], a[0:3]\n"
    "s_lshl_b32 s60, s61, 15\n"
    "s_add_u32 m0, s55, 12288\n"
    "s_add_u32 s57, s60, 2097152\n"
    "buffer_load_dwordx4 v4, s[28:31], s57 offen lds\n"
    "v_mfma_f32_16x16x32_bf16 a[4:7], v[36:39], a[112:115], a[4:7]\n"
    "s_waitcnt vmcnt(17)\n"
    "ds_read_b64_tr_b16 v[16:17], v5 offset:0\n"
    "ds_read_b64_tr_b16 v[18:19], v5 offset:2048\n"
    "v_mfma_f32_16x16x32_bf16 a[8:11], v[40:43], a[112:115], a[8:11]\n"
    "ds_read_b64_tr_b16 v[20:21], v6 offset:0\n"
    "ds_read_b64_tr_b16 v[22:23], v6 offset:2048\n"
    "v_mfma_f32_16x16x32_bf16 a[12:15], v[44:47], a[112:115], a[12:15]\n"
    "s_lshl_b32 s56, s63, 10\n"
    "s_add_u32 s58, s56, 65536\n"
    "buffer_load_dwordx4 a[112:115], v2, s[24:27], s58 offen sc1\n"
    "v_mfma_f32_16x16x32_bf16 a[16:19], v[32:35], a[116:119], a[16:19]\n"
    "s_add_u32 m0, s55, 13312\n"
    "s_add_u32 s57, s60, 2105344\n"
    "buffer_load_dwordx4 v4, s[28:31], s57 offen lds\n"
    "v_mfma_f32_16x16x32_bf16 a[20:23], v[36:39], a[116:119], a[20:23]\n"
    "ds_read_b64_tr_b16 v[24:25], v5 offset:512\n"
    "ds_read_b64_tr_b16 v[26:27], v5 offset:2560\n"
    "v_mfma_f32_16x16x32_bf16 a[24:27], v[40:43], a[116:119], a[24:27]\n"
    "ds_read_b64_tr_b16 v[28:29], v6 offset:512\n"
    "ds_read_b64_tr_b16 v[30:31], v6 offset:2560\n"
    "v_mfma_f32_16x16x32_bf16 a[28:31], v[44:47], a[116:119], a[28:31]\n"
    "s_add_u32 s58, s56, 196608\n"
    "buffer_load_dwordx4 a[116:119], v2, s[24:27], s58 offen sc1\n"
    "v_mfma_f32_16x16x32_bf16 a[32:35], v[32:35], a[120:123], a[32:35]\n"
    "s_add_u32 m0, s55, 14336\n"
    "s_add_u32 s57, s60, 2113536\n"
    "buffer_load_dwordx4 v4, s[28:31], s57 offen lds\n"
    "v_mfma_f32_16x16x32_bf16 a[36:39], v[36:39], a[120:123], a[36:39]\n"
    "v_mfma_f32_16x16x32_bf16 a[40:43], v[40:43], a[120:123], a[40:43]\n"
    "v_mfma_f32_16x16x32_bf16 a[44:47], v[44:47], a[120:123], a[44:47]\n"
    "s_add_u32 s58, s56, 327680\n"
    "buffer_load_dwordx4 a[120:123], v2, s[24:27], s58 offen sc1\n"
    "v_mfma_f32_16x16x32_bf16 a[48:51], v[32:35], a[124:127], a[48:51]\n"
    "s_add_u32 m0, s55, 15360\n"
    "s_add_u32 s57, s60, 2121728\n"
    "buffer_load_dwordx4 v4, s[28:31], s57 offen lds\n"
    "s_add_u32 s61, s61, 1\n"
    "s_cmp_eq_u32 s61, 8\n"
    "s_cselect_b32 s61, 0, s61\n"
    "v_mfma_f32_16x16x32_bf16 a[52:55], v[36:39], a[124:127], a[52:55]\n"
    "v_mfma_f32_16x16x32_bf16 a[56:59], v[40:43], a[124:127], a[56:59]\n"
    "v_mfma_f32_16x16x32_bf16 a[60:63], v[44:47], a[124:127], a[60:63]\n"
    "s_add_u32 s58, s56, 458752\n"
    "buffer_load_dwordx4 a[124:127], v2, s[24:27], s58 offen sc1\n"
    "s_add_u32 s63, s63, 1\n"
    "s_cmp_eq_u32 s63, 8\n"
    "s_cselect_b32 s63, 0, s63\n"
    "s_waitcnt lgkmcnt(0)\n"
    "v_mfma_f32_16x16x32_bf16 a[0:3], v[16:19], a[64:67], a[0:3]\n"
    "s_lshl_b32 s60, s61, 15\n"
    "s_add_u32 m0, s55, 0\n"
    "s_add_u32 s57, s60, 2097152\n"
    "buffer_load_dwordx4 v4, s[28:31], s57 offen lds\n"
    "v_mfma_f32_16x16x32_bf16 a[4:7], v[20:23], a[64:67], a[4:7]\n"
    "s_waitcnt vmcnt(17)\n"
    "ds_read_b64_tr_b16 v[32:33], v5 offset:4096\n"
    "ds_read_b64_tr_b16 v[34:35], v5 offset:6144\n"
    "v_mfma_f32_16x16x32_bf16 a[8:11], v[24:27], a[64:67], a[8:11]\n"
    "ds_read_b64_tr_b16 v[36:37], v6 offset:4096\n"
    "ds_read_b64_tr_b16 v[38:39], v6 offset:6144\n"
    "v_mfma_f32_16x16x32_bf16 a[12:15], v[28:31], a[64:67], a[12:15]\n"
    "s_lshl_b32 s56, s63, 10\n"
    "s_add_u32 s58, s56, 65536\n"
    "buffer_load_dwordx4 a[64:67], v2, s[24:27], s58 offen sc1\n"
    "v_mfma_f32_16x16x32_bf16 a[16:19], v[16:19], a[68:71], a[16:19]\n"
    "s_add_u32 m0, s55, 1024\n"
    "s_add_u32 s57, s60, 2105344\n"
    "buffer_load_dwordx4 v4, s[28:31], s57 offen lds\n"
    "v_mfma_f32_16x16x32_bf16 a[20:23], v[20:23], a[68:71], a[20:23]\n"
    "ds_read_b64_tr_b16 v[40:41], v5 offset:4608\n"
    "ds_read_b64_tr_b16 v[42:43], v5 offset:6656\n"
    "v_mfma_f32_16x16x32_bf16 a[24:27], v[24:27], a[68:71], a[24:27]\n"
    "ds_read_b64_tr_b16 v[44:45], v6 offset:4608\n"
    "ds_read_b64_tr_b16 v[46:47], v6 offset:6656\n"
    "v_mfma_f32_16x16x32_bf16 a[28:31], v[28:31], a[68:71], a[28:31]\n"
    "s_add_u32 s58, s56, 196608\n"
    "buffer_load_dwordx4 a[68:71], v2, s[24:27], s58 offen sc1\n"
    "v_mfma_f32_16x16x32_bf16 a[32:35], v[16:19], a[72:75], a[32:35]\n"
    "s_add_u32 m0, s55, 2048\n"
    "s_add_u32 s57, s60, 2113536\n"
    "buffer_load_dwordx4 v4, s[28:31], s57 offen lds\n"
    "v_mfma_f32_16x16x32_bf16 a[36:39], v[20:23], a[72:75], a[36:39]\n"
    "v_mfma_f32_16x16x32_bf16 a[40:43], v[24:27], a[72:75], a[40:43]\n"
    "v_mfma_f32_16x16x32_bf16 a[44:47], v[28:31], a[72:75], a[44:47]\n"
    "s_add_u32 s58, s56, 327680\n"
    "buffer_load_dwordx4 a[72:75], v2, s[24:27], s58 offen sc1\n"
    "v_mfma_f32_16x16x32_bf16 a[48:51], v[16:19], a[76:79], a[48:51]\n"
    "s_add_u32 m0, s55, 3072\n"
    "s_add_u32 s57, s60, 2121728\n"
    "buffer_load_dwordx4 v4, s[28:31], s57 offen lds\n"
    "s_add_u32 s61, s61, 1\n"
    "s_cmp_eq_u32 s61, 8\n"
    "s_cselect_b32 s61, 0, s61\n"
    "v_mfma_f32_16x16x32_bf16 a[52:55], v[20:23], a[76:79], a[52:55]\n"
    "v_mfma_f32_16x16x32_bf16 a[56:59], v[24:27], a[76:79], a[56:59]\n"
    "v_mfma_f32_16x16x32_bf16 a[60:63], v[28:31], a[76:79], a[60:63]\n"
    "s_add_u32 s58, s56, 458752\n"
    "buffer_load_dwordx4 a[76:79], v2, s[24:27], s58 offen sc1\n"
    "s_add_u32 s63, s63, 1\n"
    "s_cmp_eq_u32 s63, 8\n"
    "s_cselect_b32 s63, 0, s63\n"
    "s_waitcnt lgkmcnt(0)\n"
    "v_mfma_f32_16x16x32_bf16 a[0:3], v[32:35], a[80:83], a[0:3]\n"
    "s_lshl_b32 s60, s61, 15\n"
    "s_add_u32 m0, s55, 4096\n"
    "s_add_u32 s57, s60, 2097152\n"
    "buffer_load_dwordx4 v4, s[28:31], s57 offen lds\n"
    "v_mfma_f32_16x16x32_bf16 a[4:7], v[36:39], a[80:83], a[4:7]\n"
    "s_waitcnt vmcnt(17)\n"
    "ds_read_b64_tr_b16 v[16:17], v5 offset:8192\n"
    "ds_read_b64_tr_b16 v[18:19], v5 offset:10240\n"
    "v_mfma_f32_16x16x32_bf16 a[8:11], v[40:43], a[80:83], a[8:11]\n"
    "ds_read_b64_tr_b16 v[20:21], v6 offset:8192\n"
    "ds_read_b64_tr_b16 v[22:23], v6 offset:10240\n"
    "v_mfma_f32_16x16x32_bf16 a[12:15], v[44:47], a[80:83], a[12:15]\n"
    "s_lshl_b32 s56, s63, 10\n"
    "s_add_u32 s58, s56, 65536\n"
    "buffer_load_dwordx4 a[80:83], v2, s[24:27], s58 offen sc1\n"
    "v_mfma_f32_16x16x32_bf16 a[16:19], v[32:35], a[84:87], a[16:19]\n"
    "s_add_u32 m0, s55, 5120\n"
    "s_add_u32 s57, s60, 2105344\n"
    "buffer_load_dwordx4 v4, s[28:31], s57 offen lds\n"
    "v_mfma_f32_16x16x32_bf16 a[20:23], v[36:39], a[84:87], a[20:23]\n"
    "ds_read_b64_tr_b16 v[24:25], v5 offset:8704\n"
    "ds_read_b64_tr_b16 v[26:27], v5 offset:10752\n"
    "v_mfma_f32_16x16x32_bf16 a[24:27], v[40:43], a[84:87], a[24:27]\n"
    "ds_read_b64_tr_b16 v[28:29], v6 offset:8704\n"
    "ds_read_b64_tr_b16 v[30:31], v6 offset:10752\n"
    "v_mfma_f32_16x16x32_bf16 a[28:31], v[44:47], a[84:87], a[28:31]\n"
    "s_add_u32 s58, s56, 196608\n"
    "buffer_load_dwordx4 a[84:87], v2, s[24:27], s58 offen sc1\n"
    "v_mfma_f32_16x16x32_bf16 a[32:35], v[32:35], a[88:91], a[32:35]\n"
    "s_add_u32 m0, s55, 6144\n"
    "s_add_u32 s57, s60, 2113536\n"
    "buffer_load_dwordx4 v4, s[28:31], s57 offen lds\n"
    "v_mfma_f32_16x16x32_bf16 a[36:39], v[36:39], a[88:91], a[36:39]\n"
    "v_mfma_f32_16x16x32_bf16 a[40:43], v[40:43], a[88:91], a[40:43]\n"
    "v_mfma_f32_16x16x32_bf16 a[44:47], v[44:47], a[88:91], a[44:47]\n"
    "s_add_u32 s58, s56, 327680\n"
    "buffer_load_dwordx4 a[88:91], v2, s[24:27], s58 offen sc1\n"
    "v_mfma_f32_16x16x32_bf16 a[48:51], v[32:35], a[92:95], a[48:51]\n"
    "s_add_u32 m0, s55, 7168\n"
    "s_add_u32 s57, s60, 2121728\n"
    "buffer_load_dwordx4 v4, s[28:31], s57 offen lds\n"
    "s_add_u32 s61, s61, 1\n"
    "s_cmp_eq_u32 s61, 8\n"
    "s_cselect_b32 s61, 0, s61\n"
    "v_mfma_f32_16x16x32_bf16 a[52:55], v[36:39], a[92:95], a[52:55]\n"
    "v_mfma_f32_16x16x32_bf16 a[56:59], v[40:43], a[92:95], a[56:59]\n"
    "v_mfma_f32_16x16x32_bf16 a[60:63], v[44:47], a[92:95], a[60:63]\n"
    "s_add_u32 s58, s56, 458752\n"
    "buffer_load_dwordx4 a[92:95], v2, s[24:27], s58 offen sc1\n"
    "s_add_u32 s63, s63, 1\n"
    "s_cmp_eq_u32 s63, 8\n"
    "s_cselect_b32 s63, 0, s63\n"
    "s_waitcnt lgkmcnt(0)\n"
    "v_mfma_f32_16x16x32_bf16 a[0:3], v[16:19], a[96:99], a[0:3]\n"
    "s_lshl_b32 s60, s61, 15\n"
    "s_add_u32 m0, s55, 8192\n"
    "s_add_u32 s57, s60, 2097152\n"
    "buffer_load_dwordx4 v4, s[28:31], s57 offen lds\n"
    "v_mfma_f32_16x16x32_bf16 a[4:7], v[20:23], a[96:99], a[4:7]\n"
    "s_waitcnt vmcnt(17)\n"
    "ds_read_b64_tr_b16 v[32:33], v5 offset:12288\n"
    "ds_read_b64_tr_b16 v[34:35], v5 offset:14336\n"
    "v_mfma_f32_16x16x32_bf16 a[8:11], v[24:27], a[96:99], a[8:11]\n"
    "ds_read_b64_tr_b16 v[36:37], v6 offset:12288\n"
    "ds_read_b64_tr_b16 v[38:39], v6 offset:14336\n"
    "v_mfma_f32_16x16x32_bf16 a[12:15], v[28:31], a[96:99], a[12:15]\n"
    "s_lshl_b32 s56, s63, 10\n"
    "s_add_u32 s58, s56, 65536\n"
    "buffer_load_dwordx4 a[96:99], v2, s[24:27], s58 offen sc1\n"
    "v_mfma_f32_16x16x32_bf16 a[16:19], v[16:19], a[100:103], a[16:19]\n"
    "s_add_u32 m0, s55, 9216\n"
    "s_add_u32 s57, s60, 2105344\n"
    "buffer_load_dwordx4 v4, s[28:31], s57 offen lds\n"
    "v_mfma_f32_16x16x32_bf16 a[20:23], v[20:23], a[100:103], a[20:23]\n"
    "ds_read_b64_tr_b16 v[40:41], v5 offset:12800\n"
    "ds_read_b64_tr_b16 v[42:43], v5 offset:14848\n"
    "v_mfma_f32_16x16x32_bf16 a[24:27], v[24:27], a[100:103], a[24:27]\n"
    "ds_read_b64_tr_b16 v[44:45], v6 offset:12800\n"
    "ds_read_b64_tr_b16 v[46:47], v6 offset:14848\n"
    "v_mfma_f32_16x16x32_bf16 a[28:31], v[28:31], a[100:103], a[28:31]\n"
    "s_add_u32 s58, s56, 196608\n"
    "buffer_load_dwordx4 a[100:103], v2, s[24:27], s58 offen sc1\n"
    "v_mfma_f32_16x16x32_bf16 a[32:35], v[16:19], a[104:107], a[32:35]\n"
    "s_add_u32 m0, s55, 10240\n"
    "s_add_u32 s57, s60, 2113536\n"
    "buffer_load_dwordx4 v4, s[28:31], s57 offen lds\n"
    "v_mfma_f32_16x16x32_bf16 a[36:39], v[20:23], a[104:107], a[36:39]\n"
    "v_mfma_f32_16x16x32_bf16 a[40:43], v[24:27], a[104:107], a[40:43]\n"
    "v_mfma_f32_16x16x32_bf16 a[44:47], v[28:31], a[104:107], a[44:47]\n"
    "s_add_u32 s58, s56, 327680\n"
    "buffer_load_dwordx4 a[104:107], v2, s[24:27], s58 offen sc1\n"
    "v_mfma_f32_16x16x32_bf16 a[48:51], v[16:19], a[108:111], a[48:51]\n"
    "s_add_u32 m0, s55, 11264\n"
    "s_add_u32 s57, s60, 2121728\n"
    "buffer_load_dwordx4 v4, s[28:31], s57 offen lds\n"
    "s_add_u32 s61, s61, 1\n"
    "s_cmp_eq_u32 s61, 8\n"
    "s_cselect_b32 s61, 0, s61\n"
    "v_mfma_f32_16x16x32_bf16 a[52:55], v[20:23], a[108:111], a[52:55]\n"
    "v_mfma_f32_16x16x32_bf16 a[56:59], v[24:27], a[108:111], a[56:59]\n"
    "v_mfma_f32_16x16x32_bf16 a[60:63], v[28:31], a[108:111], a[60:63]\n"
    "s_add_u32 s58, s56, 458752\n"
    "buffer_load_dwordx4 a[108:111], v2, s[24:27], s58 offen sc1\n"
    "s_add_u32 s63, s63, 1\n"
    "s_cmp_eq_u32 s63, 8\n"
    "s_cselect_b32 s63, 0, s63\n"
    "s_waitcnt lgkmcnt(0)\n"
    "v_mfma_f32_16x16x32_bf16 a[0:3], v[32:35], a[112:115], a[0:3]\n"
    "s_lshl_b32 s60, s61, 15\n"
    "s_add_u32 m0, s55, 12288\n"
    "s_add_u32 s57, s60, 2097152\n"
    "buffer_load_dwordx4 v4, s[28:31], s57 offen lds\n"
    "v_mfma_f32_16x16x32_bf16 a[4:7], v[36:39], a[112:115], a[4:7]\n"
    "s_waitcnt vmcnt(17)\n"
    "ds_read_b64_tr_b16 v[16:17], v5 offset:0\n"
    "ds_read_b64_tr_b16 v[18:19], v5 offset:2048\n"
    "v_mfma_f32_16x16x32_bf16 a[8:11], v[40:43], a[112:115], a[8:11]\n"
    "ds_read_b64_tr_b16 v[20:21], v6 offset:0\n"
    "ds_read_b64_tr_b16 v[22:23], v6 offset:2048\n"
    "v_mfma_f32_16x16x32_bf16 a[12:15], v[44:47], a[112:115], a[12:15]\n"
    "s_lshl_b32 s56, s63, 10\n"
    "s_add_u32 s58, s56, 65536\n"
    "buffer_load_dwordx4 a[112:115], v2, s[24:27], s58 offen sc1\n"
    "v_mfma_f32_16x16x32_bf16 a[16:19], v[32:35], a[116:119], a[16:19]\n"
    "s_add_u32 m0, s55, 13312\n"
    "s_add_u32 s57, s60, 2105344\n"
    "buffer_load_dwordx4 v4, s[28:31], s57 offen lds\n"
    "v_mfma_f32_16x16x32_bf16 a[20:23], v[36:39], a[116:119], a[20:23]\n"
    "ds_read_b64_tr_b16 v[24:25], v5 offset:512\n"
    "ds_read_b64_tr_b16 v[26:27], v5 offset:2560\n"
    "v_mfma_f32_16x16x32_bf16 a[24:27], v[40:43], a[116:119], a[24:27]\n"
    "ds_read_b64_tr_b16 v[28:29], v6 offset:512\n"
    "ds_read_b64_tr_b16 v[30:31], v6 offset:2560\n"
    "v_mfma_f32_16x16x32_bf16 a[28:31], v[44:47], a[116:119], a[28:31]\n"
    "s_add_u32 s58, s56, 196608\n"
    "buffer_load_dwordx4 a[116:119], v2, s[24:27], s58 offen sc1\n"
    "v_mfma_f32_16x16x32_bf16 a[32:35], v[32:35], a[120:123], a[32:35]\n"
    "s_add_u32 m0, s55, 14336\n"
    "s_add_u32 s57, s60, 2113536\n"
    "buffer_load_dwordx4 v4, s[28:31], s57 offen lds\n"
    "v_mfma_f32_16x16x32_bf16 a[36:39], v[36:39], a[120:123], a[36:39]\n"
    "v_mfma_f32_16x16x32_bf16 a[40:43], v[40:43], a[120:123], a[40:43]\n"
    "v_mfma_f32_16x16x32_bf16 a[44:47], v[44:47], a[120:123], a[44:47]\n"
    "s_add_u32 s58, s56, 327680\n"
    "buffer_load_dwordx4 a[120:123], v2, s[24:27], s58 offen sc1\n"
    "v_mfma_f32_16x16x32_bf16 a[48:51], v[32:35], a[124:127], a[48:51]\n"
    "s_add_u32 m0, s55, 15360\n"
    "s_add_u32 s57, s60, 2121728\n"
    "buffer_load_dwordx4 v4, s[28:31], s57 offen lds\n"
    "s_add_u32 s61, s61, 1\n"
    "s_cmp_eq_u32 s61, 8\n"
    "s_cselect_b32 s61, 0, s61\n"
    "v_mfma_f32_16x16x32_bf16 a[52:55], v[36:39], a[124:127], a[52:55]\n"
    "v_mfma_f32_16x16x32_bf16 a[56:59], v[40:43], a[124:127], a[56:59]\n"
    "v_mfma_f32_16x16x32_bf16 a[60:63], v[44:47], a[124:127], a[60:63]\n"
    "s_add_u32 s58, s56, 458752\n"
    "buffer_load_dwordx4 a[124:127], v2, s[24:27], s58 offen sc1\n"
    "s_add_u32 s63, s63, 1\n"
    "s_cmp_eq_u32 s63, 8\n"
    "s_cselect_b32 s63, 0, s63\n"
    "s_waitcnt lgkmcnt(0)\n"
    "v_mfma_f32_16x16x32_bf16 a[0:3], v[16:19], a[64:67], a[0:3]\n"
    "s_waitcnt vmcnt(16)\n"
    "ds_read_b64_tr_b16 v[32:33], v5 offset:4096\n"
    "ds_read_b64_tr_b16 v[34:35], v5 offset:6144\n"
    "v_mfma_f32_16x16x32_bf16 a[4:7], v[20:23], a[64:67], a[4:7]\n"
    "ds_read_b64_tr_b16 v[36:37], v6 offset:4096\n"
    "ds_read_b64_tr_b16 v[38:39], v6 offset:6144\n"
    "v_mfma_f32_16x16x32_bf16 a[8:11], v[24:27], a[64:67], a[8:11]\n"
    "ds_read_b64_tr_b16 v[40:41], v5 offset:4608\n"
    "ds_read_b64_tr_b16 v[42:43], v5 offset:6656\n"
    "v_mfma_f32_16x16x32_bf16 a[12:15], v[28:31], a[64:67], a[12:15]\n"
    "ds_read_b64_tr_b16 v[44:45], v6 offset:4608\n"
    "ds_read_b64_tr_b16 v[46:47], v6 offset:6656\n"
    "v_mfma_f32_16x16x32_bf16 a[16:19], v[16:19], a[68:71], a[16:19]\n"
    "v_mfma_f32_16x16x32_bf16 a[20:23], v[20:23], a[68:71], a[20:23]\n"
    "v_mfma_f32_16x16x32_bf16 a[24:27], v[24:27], a[68:71], a[24:27]\n"
    "v_mfma_f32_16x16x32_bf16 a[28:31], v[28:31], a[68:71], a[28:31]\n"
    "v_mfma_f32_16x16x32_bf16 a[32:35], v[16:19], a[72:75], a[32:35]\n"
    "v_mfma_f32_16x16x32_bf16 a[36:39], v[20:23], a[72:75], a[36:39]\n"
    "v_mfma_f32_16x16x32_bf16 a[40:43], v[24:27], a[72:75], a[40:43]\n"
    "v_mfma_f32_16x16x32_bf16 a[44:47], v[28:31], a[72:75], a[44:47]\n"
    "v_mfma_f32_16x16x32_bf16 a[48:51], v[16:19], a[76:79], a[48:51]\n"
    "v_mfma_f32_16x16x32_bf16 a[52:55], v[20:23], a[76:79], a[52:55]\n"
    "v_mfma_f32_16x16x32_bf16 a[56:59], v[24:27], a[76:79], a[56:59]\n"
    "v_mfma_f32_16x16x32_bf16 a[60:63], v[28:31], a[76:79], a[60:63]\n"
    "buffer_load_dwordx2 v[80:81], v11, s[32:35], 0 offen offset:0\n"
    "buffer_load_dwordx2 v[82:83], v11, s[32:35], 0 offen offset:32\n"
    "s_mov_b32 s64, 4096\n"
    "buffer_load_dwordx4 v[84:87], v12, s[36:39], s64 offen offset:0 sc1\n"
    "buffer_load_dwordx4 v[88:91], v12, s[36:39], s64 offen offset:64 sc1\n"
    "s_mov_b32 s65, 8192\n"
    "buffer_load_dwordx4 v[92:95], v12, s[36:39], s65 offen offset:0 sc1\n"
    "buffer_load_dwordx4 v[96:99], v12, s[36:39], s65 offen offset:64 sc1\n"
    "s_waitcnt lgkmcnt(0)\n"
    "v_mfma_f32_16x16x32_bf16 a[0:3], v[32:35], a[80:83], a[0:3]\n"
    "s_waitcnt vmcnt(14)\n"
    "ds_read_b64_tr_b16 v[16:17], v5 offset:8192\n"
    "ds_read_b64_tr_b16 v[18:19], v5 offset:10240\n"
    "v_mfma_f32_16x16x32_bf16 a[4:7], v[36:39], a[80:83], a[4:7]\n"
    "ds_read_b64_tr_b16 v[20:21], v6 offset:8192\n"
    "ds_read_b64_tr_b16 v[22:23], v6 offset:10240\n"
    "v_mfma_f32_16x16x32_bf16 a[8:11], v[40:43], a[80:83], a[8:11]\n"
    "ds_read_b64_tr_b16 v[24:25], v5 offset:8704\n"
    "ds_read_b64_tr_b16 v[26:27], v5 offset:10752\n"
    "v_mfma_f32_16x16x32_bf16 a[12:15], v[44:47], a[80:83], a[12:15]\n"
    "ds_read_b64_tr_b16 v[28:29], v6 offset:8704\n"
    "ds_read_b64_tr_b16 v[30:31], v6 offset:10752\n"
    "v_mfma_f32_16x16x32_bf16 a[16:19], v[32:35], a[84:87], a[16:19]\n"
    "v_mfma_f32_16x16x32_bf16 a[20:23], v[36:39], a[84:87], a[20:23]\n"
    "v_mfma_f32_16x16x32_bf16 a[24:27], v[40:43], a[84:87], a[24:27]\n"
    "v_mfma_f32_16x16x32_bf16 a[28:31], v[44:47], a[84:87], a[28:31]\n"
    "v_mfma_f32_16x16x32_bf16 a[32:35], v[32:35], a[88:91], a[32:35]\n"
    "v_mfma_f32_16x16x32_bf16 a[36:39], v[36:39], a[88:91], a[36:39]\n"
    "v_mfma_f32_16x16x32_bf16 a[40:43], v[40:43], a[88:91], a[40:43]\n"
    "v_mfma_f32_16x16x32_bf16 a[44:47], v[44:47], a[88:91], a[44:47]\n"
    "v_mfma_f32_16x16x32_bf16 a[48:51], v[32:35], a[92:95], a[48:51]\n"
    "v_mfma_f32_16x16x32_bf16 a[52:55], v[36:39], a[92:95], a[52:55]\n"
    "v_mfma_f32_16x16x32_bf16 a[56:59], v[40:43], a[92:95], a[56:59]\n"
    "v_mfma_f32_16x16x32_bf16 a[60:63], v[44:47], a[92:95], a[60:63]\n"
    "s_waitcnt lgkmcnt(0)\n"
    "v_mfma_f32_16x16x32_bf16 a[0:3], v[16:19], a[96:99], a[0:3]\n"
    "s_waitcnt vmcnt(6)\n"
    "ds_read_b64_tr_b16 v[32:33], v5 offset:12288\n"
    "ds_read_b64_tr_b16 v[34:35], v5 offset:14336\n"
    "v_mfma_f32_16x16x32_bf16 a[4:7], v[20:23], a[96:99], a[4:7]\n"
    "ds_read_b64_tr_b16 v[36:37], v6 offset:12288\n"
    "ds_read_b64_tr_b16 v[38:39], v6 offset:14336\n"
    "v_mfma_f32_16x16x32_bf16 a[8:11], v[24:27], a[96:99], a[8:11]\n"
    "ds_read_b64_tr_b16 v[40:41], v5 offset:12800\n"
    "ds_read_b64_tr_b16 v[42:43], v5 offset:14848\n"
    "v_mfma_f32_16x16x32_bf16 a[12:15], v[28:31], a[96:99], a[12:15]\n"
    "ds_read_b64_tr_b16 v[44:45], v6 offset:12800\n"
    "ds_read_b64_tr_b16 v[46:47], v6 offset:14848\n"
    "v_mfma_f32_16x16x32_bf16 a[16:19], v[16:19], a[100:103], a[16:19]\n"
    "v_mfma_f32_16x16x32_bf16 a[20:23], v[20:23], a[100:103], a[20:23]\n"
    "v_mfma_f32_16x16x32_bf16 a[24:27], v[24:27], a[100:103], a[24:27]\n"
    "v_mfma_f32_16x16x32_bf16 a[28:31], v[28:31], a[100:103], a[28:31]\n"
    "v_mfma_f32_16x16x32_bf16 a[32:35], v[16:19], a[104:107], a[32:35]\n"
    "v_mfma_f32_16x16x32_bf16 a[36:39], v[20:23], a[104:107], a[36:39]\n"
    "v_mfma_f32_16x16x32_bf16 a[40:43], v[24:27], a[104:107], a[40:43]\n"
    "v_mfma_f32_16x16x32_bf16 a[44:47], v[28:31], a[104:107], a[44:47]\n"
    "v_mfma_f32_16x16x32_bf16 a[48:51], v[16:19], a[108:111], a[48:51]\n"
    "v_mfma_f32_16x16x32_bf16 a[52:55], v[20:23], a[108:111], a[52:55]\n"
    "v_mfma_f32_16x16x32_bf16 a[56:59], v[24:27], a[108:111], a[56:59]\n"
    "v_mfma_f32_16x16x32_bf16 a[60:63], v[28:31], a[108:111], a[60:63]\n"
    "s_waitcnt lgkmcnt(0)\n"
    "v_mfma_f32_16x16x32_bf16 a[0:3], v[32:35], a[112:115], a[0:3]\n"
    "v_mfma_f32_16x16x32_bf16 a[4:7], v[36:39], a[112:115], a[4:7]\n"
    "v_mfma_f32_16x16x32_bf16 a[8:11], v[40:43], a[112:115], a[8:11]\n"
    "v_mfma_f32_16x16x32_bf16 a[12:15], v[44:47], a[112:115], a[12:15]\n"
    "v_mfma_f32_16x16x32_bf16 a[16:19], v[32:35], a[116:119], a[16:19]\n"
    "v_mfma_f32_16x16x32_bf16 a[20:23], v[36:39], a[116:119], a[20:23]\n"
    "v_mfma_f32_16x16x32_bf16 a[24:27], v[40:43], a[116:119], a[24:27]\n"
    "v_mfma_f32_16x16x32_bf16 a[28:31], v[44:47], a[116:119], a[28:31]\n"
    "v_mfma_f32_16x16x32_bf16 a[32:35], v[32:35], a[120:123], a[32:35]\n"
    "v_mfma_f32_16x16x32_bf16 a[36:39], v[36:39], a[120:123], a[36:39]\n"
    "v_mfma_f32_16x16x32_bf16 a[40:43], v[40:43], a[120:123], a[40:43]\n"
    "v_mfma_f32_16x16x32_bf16 a[44:47], v[44:47], a[120:123], a[44:47]\n"
    "v_mfma_f32_16x16x32_bf16 a[48:51], v[32:35], a[124:127], a[48:51]\n"
    "v_mfma_f32_16x16x32_bf16 a[52:55], v[36:39], a[124:127], a[52:55]\n"
    "v_mfma_f32_16x16x32_bf16 a[56:59], v[40:43], a[124:127], a[56:59]\n"
    "v_mfma_f32_16x16x32_bf16 a[60:63], v[44:47], a[124:127], a[60:63]\n"
    "s_lshl_b32 s65, s52, 14\n"
    "v_add_u32 v10, s65, v2\n"
    "s_lshl_b32 s64, s52, 11\n"
    "v_add_u32 v7, s64, v2\n"
    "v_add_u32 v8, 0x10000, v7\n"
    "s_nop 4\n"
    "ds_write_b128 v10, a[0:3] offset:0\n"
    "ds_write_b128 v10, a[4:7] offset:1024\n"
    "ds_write_b128 v10, a[8:11] offset:2048\n"
    "ds_write_b128 v10, a[12:15] offset:3072\n"
    "ds_write_b128 v10, a[16:19] offset:4096\n"
    "ds_write_b128 v10, a[20:23] offset:5120\n"
    "ds_write_b128 v10, a[24:27] offset:6144\n"
    "ds_write_b128 v10, a[28:31] offset:7168\n"
    "ds_write_b128 v10, a[32:35] offset:8192\n"
    "ds_write_b128 v10, a[36:39] offset:9216\n"
    "ds_write_b128 v10, a[40:43] offset:10240\n"
    "ds_write_b128 v10, a[44:47] offset:11264\n"
    "ds_write_b128 v10, a[48:51] offset:12288\n"
    "ds_write_b128 v10, a[52:55] offset:13312\n"
    "ds_write_b128 v10, a[56:59] offset:14336\n"
    "ds_write_b128 v10, a[60:63] offset:15360\n"
    "s_lshl_b32 s65, s52, 11\n"
    "s_add_u32 s65, s65, 131072\n"
    "v_add_u32 v9, s65, v2\n"
    "v_add_u32 v124, 131072, v2\n"
    "ds_write_b128 v9, v[48:51]\n"
    "s_waitcnt lgkmcnt(0)\n"
    "s_barrier\n"
    "ds_read_b128 v[16:19], v7 offset:0\n"
    "ds_read_b128 v[20:23], v7 offset:1024\n"
    "ds_read_b128 v[24:27], v7 offset:16384\n"
    "ds_read_b128 v[28:31], v7 offset:17408\n"
    "ds_read_b128 v[32:35], v7 offset:32768\n"
    "ds_read_b128 v[36:39], v7 offset:33792\n"
    "ds_read_b128 v[40:43], v7 offset:49152\n"
    "ds_read_b128 v[44:47], v7 offset:50176\n"
    "ds_read_b128 v[48:51], v8 offset:0\n"
    "ds_read_b128 v[52:55], v8 offset:1024\n"
    "ds_read_b128 v[56:59], v8 offset:16384\n"
    "ds_read_b128 v[60:63], v8 offset:17408\n"
    "ds_read_b128 v[64:67], v8 offset:32768\n"
    "ds_read_b128 v[68:71], v8 offset:33792\n"
    "ds_read_b128 v[72:75], v8 offset:49152\n"
    "ds_read_b128 v[76:79], v8 offset:50176\n"
    "s_waitcnt lgkmcnt(12)\n"
    "v_add_f32 v16, v16, v24\n"
    "v_add_f32 v17, v17, v25\n"
    "v_add_f32 v18, v18, v26\n"
    "v_add_f32 v19, v19, v27\n"
    "v_add_f32 v20, v20, v28\n"
    "v_add_f32 v21, v21, v29\n"
    "v_add_f32 v22, v22, v30\n"
    "v_add_f32 v23, v23, v31\n"
    "s_waitcnt lgkmcnt(10)\n"
    "v_add_f32 v16, v16, v32\n"
    "v_add_f32 v17, v17, v33\n"
    "v_add_f32 v18, v18, v34\n"
    "v_add_f32 v19, v19, v35\n"
    "v_add_f32 v20, v20, v36\n"
    "v_add_f32 v21, v21, v37\n"
    "v_add_f32 v22, v22, v38\n"
    "v_add_f32 v23, v23, v39\n"
    "s_waitcnt lgkmcnt(8)\n"
    "v_add_f32 v16, v16, v40\n"
    "v_add_f32 v17, v17, v41\n"
    "v_add_f32 v18, v18, v42\n"
    "v_add_f32 v19, v19, v43\n"
    "v_add_f32 v20, v20, v44\n"
    "v_add_f32 v21, v21, v45\n"
    "v_add_f32 v22, v22, v46\n"
    "v_add_f32 v23, v23, v47\n"
    "s_waitcnt lgkmcnt(6)\n"
    "v_add_f32 v16, v16, v48\n"
    "v_add_f32 v17, v17, v49\n"
    "v_add_f32 v18, v18, v50\n"
    "v_add_f32 v19, v19, v51\n"
    "v_add_f32 v20, v20, v52\n"
    "v_add_f32 v21, v21, v53\n"
    "v_add_f32 v22, v22, v54\n"
    "v_add_f32 v23, v23, v55\n"
    "s_waitcnt lgkmcnt(4)\n"
    "v_add_f32 v16, v16, v56\n"
    "v_add_f32 v17, v17, v57\n"
    "v_add_f32 v18, v18, v58\n"
    "v_add_f32 v19, v19, v59\n"
    "v_add_f32 v20, v20, v60\n"
    "v_add_f32 v21, v21, v61\n"
    "v_add_f32 v22, v22, v62\n"
    "v_add_f32 v23, v23, v63\n"
    "s_waitcnt lgkmcnt(2)\n"
    "v_add_f32 v16, v16, v64\n"
    "v_add_f32 v17, v17, v65\n"
    "v_add_f32 v18, v18, v66\n"
    "v_add_f32 v19, v19, v67\n"
    "v_add_f32 v20, v20, v68\n"
    "v_add_f32 v21, v21, v69\n"
    "v_add_f32 v22, v22, v70\n"
    "v_add_f32 v23, v23, v71\n"
    "s_waitcnt lgkmcnt(0)\n"
    "v_add_f32 v16, v16, v72\n"
    "v_add_f32 v17, v17, v73\n"
    "v_add_f32 v18, v18, v74\n"
    "v_add_f32 v19, v19, v75\n"
    "v_add_f32 v20, v20, v76\n"
    "v_add_f32 v21, v21, v77\n"
    "v_add_f32 v22, v22, v78\n"
    "v_add_f32 v23, v23, v79\n"
    "s_waitcnt vmcnt(0)\n"
    "v_lshlrev_b32 v40, 16, v80\n"
    "v_and_b32 v41, s62, v80\n"
    "v_lshlrev_b32 v44, 16, v82\n"
    "v_and_b32 v45, s62, v82\n"
    "v_lshlrev_b32 v42, 16, v81\n"
    "v_and_b32 v43, s62, v81\n"
    "v_lshlrev_b32 v46, 16, v83\n"
    "v_and_b32 v47, s62, v83\n"
    "v_fma_f32 v16, v16, v84, v40\n"
    "v_fma_f32 v17, v17, v85, v41\n"
    "v_fma_f32 v18, v18, v86, v42\n"
    "v_fma_f32 v19, v19, v87, v43\n"
    "v_fma_f32 v20, v20, v88, v44\n"
    "v_fma_f32 v21, v21, v89, v45\n"
    "v_fma_f32 v22, v22, v90, v46\n"
    "v_fma_f32 v23, v23, v91, v47\n"
    "buffer_store_dwordx4 v[16:19], v2, s[40:43], 0 offen sc1\n"
    "buffer_store_dwordx4 v[20:23], v2, s[40:43], 0 offen offset:1024 sc1\n"
    "v_add_f32 v92, 1.0, v92\n"
    "v_add_f32 v93, 1.0, v93\n"
    "v_add_f32 v94, 1.0, v94\n"
    "v_add_f32 v95, 1.0, v95\n"
    "v_add_f32 v96, 1.0, v96\n"
    "v_add_f32 v97, 1.0, v97\n"
    "v_add_f32 v98, 1.0, v98\n"
    "v_add_f32 v99, 1.0, v99\n"
    "v_cvt_pk_bf16_f32 v40, v92, v93\n"
    "v_cvt_pk_bf16_f32 v41, v94, v95\n"
    "v_cvt_pk_bf16_f32 v42, v96, v97\n"
    "v_cvt_pk_bf16_f32 v43, v98, v99\n"
    "v_lshlrev_b32 v92, 16, v40\n"
    "v_and_b32 v93, s62, v40\n"
    "v_lshlrev_b32 v94, 16, v41\n"
    "v_and_b32 v95, s62, v41\n"
    "v_lshlrev_b32 v96, 16, v42\n"
    "v_and_b32 v97, s62, v42\n"
    "v_lshlrev_b32 v98, 16, v43\n"
    "v_and_b32 v99, s62, v43\n"
    "v_mul_f32 v92, v92, v16\n"
    "v_mul_f32 v93, v93, v17\n"
    "v_mul_f32 v94, v94, v18\n"
    "v_mul_f32 v95, v95, v19\n"
    "v_mul_f32 v96, v96, v20\n"
    "v_mul_f32 v97, v97, v21\n"
    "v_mul_f32 v98, v98, v22\n"
    "v_mul_f32 v99, v99, v23\n"
    "v_cvt_pk_bf16_f32 v40, v92, v93\n"
    "v_cvt_pk_bf16_f32 v41, v94, v95\n"
    "v_cvt_pk_bf16_f32 v42, v96, v97\n"
    "v_cvt_pk_bf16_f32 v43, v98, v99\n"
    "buffer_store_dwordx4 v[40:43], v2, s[44:47], 0 offen sc1\n"
    "v_add_f32 v48, v16, v17\n"
    "v_add_f32 v48, v48, v18\n"
    "v_add_f32 v48, v48, v19\n"
    "v_add_f32 v48, v48, v20\n"
    "v_add_f32 v48, v48, v21\n"
    "v_add_f32 v48, v48, v22\n"
    "v_add_f32 v48, v48, v23\n"
    "v_mov_b32 v9, v48\n"
    "s_nop 1\n"
    "v_permlane16_swap_b32 v48, v9\n"
    "v_add_f32 v48, v48, v9\n"
    "v_mov_b32 v9, v48\n"
    "s_nop 1\n"
    "v_permlane32_swap_b32 v48, v9\n"
    "v_add_f32 v48, v48, v9\n"
    "v_mul_f32 v48, 0x3d000000, v48\n"
    "v_sub_f32 v80, v16, v48\n"
    "v_sub_f32 v81, v17, v48\n"
    "v_sub_f32 v82, v18, v48\n"
    "v_sub_f32 v83, v19, v48\n"
    "v_sub_f32 v84, v20, v48\n"
    "v_sub_f32 v85, v21, v48\n"
    "v_sub_f32 v86, v22, v48\n"
    "v_sub_f32 v87, v23, v48\n"
    "v_mul_f32 v49, v80, v80\n"
    "v_fmac_f32 v49, v81, v81\n"
    "v_fmac_f32 v49, v82, v82\n"
    "v_fmac_f32 v49, v83, v83\n"
    "v_fmac_f32 v49, v84, v84\n"
    "v_fmac_f32 v49, v85, v85\n"
    "v_fmac_f32 v49, v86, v86\n"
    "v_fmac_f32 v49, v87, v87\n"
    "v_mov_b32 v9, v49\n"
    "s_nop 1\n"
    "v_permlane16_swap_b32 v49, v9\n"
    "v_add_f32 v49, v49, v9\n"
    "v_mov_b32 v9, v49\n"
    "s_nop 1\n"
    "v_permlane32_swap_b32 v49, v9\n"
    "v_add_f32 v49, v49, v9\n"
    "v_lshlrev_b32 v7, 3, v3\n"
    "buffer_store_dwordx2 v[48:49], v7, s[48:51], 0 offen sc1\n"
    "s_cmp_eq_u32 s52, 7\n"
    "s_cbranch_scc0 L_nouv_%=\n"
    "ds_read_b128 v[56:59], v124 offset:0\n"
    "ds_read_b128 v[60:63], v124 offset:2048\n"
    "ds_read_b128 v[64:67], v124 offset:4096\n"
    "ds_read_b128 v[68:71], v124 offset:6144\n"
    "ds_read_b128 v[72:75], v124 offset:8192\n"
    "ds_read_b128 v[76:79], v124 offset:10240\n"
    "ds_read_b128 v[80:83], v124 offset:12288\n"
    "ds_read_b128 v[84:87], v124 offset:14336\n"
    "s_waitcnt lgkmcnt(0)\n"
    "v_add_f32 v56, v56, v60\n"
    "v_add_f32 v57, v57, v61\n"
    "v_add_f32 v58, v58, v62\n"
    "v_add_f32 v59, v59, v63\n"
    "v_add_f32 v56, v56, v64\n"
    "v_add_f32 v57, v57, v65\n"
    "v_add_f32 v58, v58, v66\n"
    "v_add_f32 v59, v59, v67\n"
    "v_add_f32 v56, v56, v68\n"
    "v_add_f32 v57, v57, v69\n"
    "v_add_f32 v58, v58, v70\n"
    "v_add_f32 v59, v59, v71\n"
    "v_add_f32 v56, v56, v72\n"
    "v_add_f32 v57, v57, v73\n"
    "v_add_f32 v58, v58, v74\n"
    "v_add_f32 v59, v59, v75\n"
    "v_add_f32 v56, v56, v76\n"
    "v_add_f32 v57, v57, v77\n"
    "v_add_f32 v58, v58, v78\n"
    "v_add_f32 v59, v59, v79\n"
    "v_add_f32 v56, v56, v80\n"
    "v_add_f32 v57, v57, v81\n"
    "v_add_f32 v58, v58, v82\n"
    "v_add_f32 v59, v59, v83\n"
    "v_add_f32 v56, v56, v84\n"
    "v_add_f32 v57, v57, v85\n"
    "v_add_f32 v58, v58, v86\n"
    "v_add_f32 v59, v59, v87\n"
    "buffer_store_dwordx4 v[56:59], v127, s[76:79], 0 offen sc1\n"
    "s_cmp_ge_u32 s67, 112\n"
    "s_cselect_b32 s64, 112, 0\n"
    "s_sub_u32 s64, s67, s64\n"
    "s_lshr_b32 s64, s64, 2\n"
    "s_lshl_b32 s64, s64, 6\n"
    "s_add_u32 s64, s64, 657408\n"
    "v_mov_b32 v8, s64\n"
    "v_mov_b32 v9, 1\n"
    "s_cmp_ge_u32 %1, 224\n"
    "s_cbranch_scc1 L_nouv_%=\n"
    "s_mov_b64 exec, 1\n"
    "s_waitcnt vmcnt(0)\n"
    "global_atomic_add v8, v9, s[96:97] sc1\n"
    "s_mov_b64 exec, -1\n"
    "L_nouv_%=:\n"
    "s_lshr_b32 s64, s53, 2\n"
    "s_lshl_b32 s64, s64, 6\n"
    "s_add_u32 s64, s64, 659456\n"
    "s_waitcnt vmcnt(0) lgkmcnt(0)\n"
    "s_barrier\n"
    "s_cmp_eq_u32 s52, 0\n"
    "s_cbranch_scc0 L_pb3_%=\n"
    "v_mov_b32 v7, s64\n"
    "v_mov_b32 v8, 1\n"
    "s_mov_b64 exec, 1\n"
    "global_atomic_add v7, v8, s[96:97] sc1\n"
    "s_mov_b64 exec, -1\n"
    "L_pb3_%=:\n"
    "s_cmp_ge_u32 %1, 224\n"
    "s_cbranch_scc1 L_p5_%=\n"
    "v_and_b32 v3, 63, %2\n"
    "v_lshrrev_b32 v10, 6, %2\n"
    "v_lshlrev_b32 v2, 4, v3\n"
    "v_readfirstlane_b32 s48, v10\n"
    "s_and_b32 s54, %1, 7\n"
    "s_lshr_b32 s55, %1, 3\n"
    "s_mul_i32 s54, s54, 28\n"
    "s_add_u32 s54, s54, s55\n"
    "s_lshr_b32 s50, s54, 3\n"
    "s_and_b32 s49, s54, 7\n"
    "s_lshl_b32 s54, s50, 7\n"
    "s_sub_u32 s56, 1835008, s54\n"
    "s_mov_b64 s[28:29], s[90:91]\n"
    "s_add_u32 s28, s28, s54\n"
    "s_addc_u32 s29, s29, 0\n"
    "s_and_b32 s29, s29, 0xffff\n"
    "s_mov_b32 s30, s56\n"
    "s_mov_b32 s31, 0x20000\n"
    "s_mov_b64 s[32:33], s[92:93]\n"
    "s_add_u32 s32, s32, s54\n"
    "s_addc_u32 s33, s33, 0\n"
    "s_and_b32 s33, s33, 0xffff\n"
    "s_mov_b32 s34, s56\n"
    "s_mov_b32 s35, 0x20000\n"
    "v_bfe_u32 v10, v3, 2, 3\n"
    "v_lshl_add_u32 v10, s48, 3, v10\n"
    "v_mul_u32_u24 v4, 3584, v10\n"
    "v_lshrrev_b32 v10, 5, v3\n"
    "v_lshl_add_u32 v4, v10, 6, v4\n"
    "v_and_b32 v11, 3, v3\n"
    "v_bfe_u32 v10, v3, 4, 1\n"
    "v_lshlrev_b32 v10, 1, v10\n"
    "v_xor_b32 v11, v11, v10\n"
    "v_lshl_add_u32 v4, v11, 4, v4\n"
    "s_lshl_b32 s51, s48, 11\n"
    "s_add_u32 m0, s51, 0\n"
    "s_mov_b32 s55, 0\n"
    "buffer_load_dwordx4 v4, s[28:31], s55 offen lds\n"
    "s_add_u32 m0, s51, 1024\n"
    "s_mov_b32 s55, 0\n"
    "buffer_load_dwordx4 v4, s[32:35], s55 offen lds\n"
    "s_lshl_b32 s54, s49, 18\n"
    "s_lshl_b32 s55, s48, 15\n"
    "s_add_u32 s54, s54, s55\n"
    "s_mov_b64 s[24:25], s[86:87]\n"
    "s_add_u32 s24, s24, s54\n"
    "s_addc_u32 s25, s25, 0\n"
    "s_and_b32 s25, s25, 0xffff\n"
    "s_mov_b32 s26, 0x8000\n"
    "s_mov_b32 s27, 0x20000\n"
    "s_add_u32 m0, s51, 16384\n"
    "s_mov_b32 s55, 229376\n"
    "buffer_load_dwordx4 v4, s[28:31], s55 offen lds\n"
    "s_add_u32 m0, s51, 17408\n"
    "s_mov_b32 s55, 229376\n"
    "buffer_load_dwordx4 v4, s[32:35], s55 offen lds\n"
    "s_add_u32 m0, s51, 32768\n"
    "s_mov_b32 s55, 458752\n"
    "buffer_load_dwordx4 v4, s[28:31], s55 offen lds\n"
    "s_add_u32 m0, s51, 33792\n"
    "s_mov_b32 s55, 458752\n"
    "buffer_load_dwordx4 v4, s[32:35], s55 offen lds\n"
    "s_add_u32 m0, s51, 49152\n"
    "s_mov_b32 s55, 688128\n"
    "buffer_load_dwordx4 v4, s[28:31], s55 offen lds\n"
    "s_add_u32 m0, s51, 50176\n"
    "s_mov_b32 s55, 688128\n"
    "buffer_load_dwordx4 v4, s[32:35], s55 offen lds\n"
    "s_add_u32 m0, s51, 65536\n"
    "s_mov_b32 s55, 917504\n"
    "buffer_load_dwordx4 v4, s[28:31], s55 offen lds\n"
    "s_add_u32 m0, s51, 66560\n"
    "s_mov_b32 s55, 917504\n"
    "buffer_load_dwordx4 v4, s[32:35], s55 offen lds\n"
    "s_add_u32 m0, s51, 81920\n"
    "s_mov_b32 s55, 1146880\n"
    "buffer_load_dwordx4 v4, s[28:31], s55 offen lds\n"
    "s_add_u32 m0, s51, 82944\n"
    "s_mov_b32 s55, 1146880\n"
    "buffer_load_dwordx4 v4, s[32:35], s55 offen lds\n"
    "s_add_u32 m0, s51, 98304\n"
    "s_mov_b32 s55, 1376256\n"
    "buffer_load_dwordx4 v4, s[28:31], s55 offen lds\n"
    "s_add_u32 m0, s51, 99328\n"
    "s_mov_b32 s55, 1376256\n"
    "buffer_load_dwordx4 v4, s[32:35], s55 offen lds\n"
    "s_add_u32 m0, s51, 114688\n"
    "s_mov_b32 s55, 1605632\n"
    "buffer_load_dwordx4 v4, s[28:31], s55 offen lds\n"
    "s_add_u32 m0, s51, 115712\n"
    "s_mov_b32 s55, 1605632\n"
    "buffer_load_dwordx4 v4, s[32:35], s55 offen lds\n"
    "v_lshrrev_b32 v10, 5, v3\n"
    "v_lshlrev_b32 v5, 11, v10\n"
    "v_bfe_u32 v11, v3, 2, 3\n"
    "v_lshl_add_u32 v5, v11, 6, v5\n"
    "v_and_b32 v11, 3, v3\n"
    "v_lshl_add_u32 v5, v11, 3, v5\n"
    "v_bfe_u32 v11, v3, 4, 1\n"
    "v_lshl_add_u32 v5, v11, 5, v5\n"
    "v_xor_b32 v6, 32, v5\n"
    "v_add_u32 v7, 0x10000, v5\n"
    "v_add_u32 v8, 0x10000, v6\n"
    "v_lshrrev_b32 v11, 4, v3\n"
    "v_lshlrev_b32 v12, 4, v11\n"
    "s_lshl_b32 s54, s50, 8\n"
    "s_mov_b64 s[36:37], s[16:17]\n"
    "s_add_u32 s36, s36, s54\n"
    "s_addc_u32 s37, s37, 0\n"
    "s_and_b32 s37, s37, 0xffff\n"
    "s_mov_b32 s38, 28672\n"
    "s_mov_b32 s39, 0x20000\n"
    "s_lshl_b32 s54, s49, 15\n"
    "s_lshl_b32 s55, s48, 12\n"
    "s_add_u32 s54, s54, s55\n"
    "s_mov_b64 s[40:41], s[14:15]\n"
    "s_add_u32 s40, s40, s54\n"
    "s_addc_u32 s41, s41, 0\n"
    "s_and_b32 s41, s41, 0xffff\n"
    "s_mov_b32 s42, 4096\n"
    "s_mov_b32 s43, 0x20000\n"
    "s_lshl_b32 s54, s49, 4\n"
    "s_lshl_b32 s55, s48, 1\n"
    "s_add_u32 s54, s54, s55\n"
    "s_mul_i32 s54, s54, 57344\n"
    "s_lshl_b32 s55, s50, 11\n"
    "s_add_u32 s54, s54, s55\n"
    "s_mov_b64 s[44:45], s[18:19]\n"
    "s_add_u32 s44, s44, s54\n"
    "s_addc_u32 s45, s45, 0\n"
    "s_and_b32 s45, s45, 0xffff\n"
    "s_mov_b32 s46, 114688\n"
    "s_mov_b32 s47, 0x20000\n"
    "s_lshl_b32 s54, s49, 6\n"
    "s_add_u32 s54, s54, 659456\n"
    "s_lshl_b32 s57, s50, 6\n"
    "s_add_u32 s57, s57, 657408\n"
    "s_cmp_eq_u32 s48, 0\n"
    "s_cbranch_scc0 L_pdu4_%=\n"
    "v_and_b32 v9, 0, v3\n"
    "v_lshlrev_b32 v9, 6, v9\n"
    "v_add_u32 v9, s57, v9\n"
    "s_mov_b32 s58, 200000\n"
    "L_plu4_%=:\n"
    "global_load_dword v10, v9, s[96:97] sc1\n"
    "s_waitcnt vmcnt(0)\n"
    "v_cmp_ne_u32 vcc, 8, v10\n"
    "s_sub_u32 s58, s58, 1\n"
    "s_cbranch_vccz L_pdu4_%=\n"
    "s_sleep 1\n"
    "s_cmp_lg_u32 s58, 0\n"
    "s_cbranch_scc1 L_plu4_%=\n"
    "L_pdu4_%=:\n"
    "s_cmp_eq_u32 s48, 0\n"
    "s_cbranch_scc0 L_pdw4_%=\n"
    "v_and_b32 v9, 0, v3\n"
    "v_lshlrev_b32 v9, 6, v9\n"
    "v_add_u32 v9, s54, v9\n"
    "s_mov_b32 s58, 200000\n"
    "L_plw4_%=:\n"
    "global_load_dword v10, v9, s[96:97] sc1\n"
    "s_waitcnt vmcnt(0)\n"
    "v_cmp_ne_u32 vcc, 32, v10\n"
    "s_sub_u32 s58, s58, 1\n"
    "s_cbranch_vccz L_pdw4_%=\n"
    "s_sleep 1\n"
    "s_cmp_lg_u32 s58, 0\n"
    "s_cbranch_scc1 L_plw4_%=\n"
    "L_pdw4_%=:\n"
    "s_waitcnt vmcnt(0)\n"
    "s_barrier\n"
    "s_mov_b32 s56, 0\n"
    "buffer_load_dwordx4 a[0:3], v2, s[24:27], s56 offen offset:0 sc1\n"
    "buffer_load_dwordx4 a[4:7], v2, s[24:27], s56 offen offset:1024 sc1\n"
    "s_mov_b32 s56, 16384\n"
    "buffer_load_dwordx4 a[64:67], v2, s[24:27], s56 offen offset:0 sc1\n"
    "buffer_load_dwordx4 a[68:71], v2, s[24:27], s56 offen offset:1024 sc1\n"
    "ds_read_b64_tr_b16 v[80:81], v5 offset:0\n"
    "ds_read_b64_tr_b16 v[82:83], v5 offset:4096\n"
    "ds_read_b64_tr_b16 v[84:85], v6 offset:0\n"
    "ds_read_b64_tr_b16 v[86:87], v6 offset:4096\n"
    "ds_read_b64_tr_b16 v[88:89], v5 offset:512\n"
    "ds_read_b64_tr_b16 v[90:91], v5 offset:4608\n"
    "ds_read_b64_tr_b16 v[92:93], v6 offset:512\n"
    "ds_read_b64_tr_b16 v[94:95], v6 offset:4608\n"
    "ds_read_b64_tr_b16 v[96:97], v5 offset:1024\n"
    "ds_read_b64_tr_b16 v[98:99], v5 offset:5120\n"
    "ds_read_b64_tr_b16 v[100:101], v6 offset:1024\n"
    "ds_read_b64_tr_b16 v[102:103], v6 offset:5120\n"
    "ds_read_b64_tr_b16 v[104:105], v5 offset:1536\n"
    "ds_read_b64_tr_b16 v[106:107], v5 offset:5632\n"
    "ds_read_b64_tr_b16 v[108:109], v6 offset:1536\n"
    "ds_read_b64_tr_b16 v[110:111], v6 offset:5632\n"
    "s_mov_b32 s56, 0\n"
    "buffer_load_dwordx4 a[8:11], v2, s[24:27], s56 offen offset:2048 sc1\n"
    "buffer_load_dwordx4 a[12:15], v2, s[24:27], s56 offen offset:3072 sc1\n"
    "s_mov_b32 s56, 16384\n"
    "buffer_load_dwordx4 a[72:75], v2, s[24:27], s56 offen offset:2048 sc1\n"
    "buffer_load_dwordx4 a[76:79], v2, s[24:27], s56 offen offset:3072 sc1\n"
    "s_mov_b32 s56, 4096\n"
    "buffer_load_dwordx4 a[16:19], v2, s[24:27], s56 offen offset:0 sc1\n"
    "buffer_load_dwordx4 a[20:23], v2, s[24:27], s56 offen offset:1024 sc1\n"
    "s_mov_b32 s56, 20480\n"
    "buffer_load_dwordx4 a[80:83], v2, s[24:27], s56 offen offset:0 sc1\n"
    "buffer_load_dwordx4 a[84:87], v2, s[24:27], s56 offen offset:1024 sc1\n"
    "s_mov_b32 s56, 4096\n"
    "buffer_load_dwordx4 a[24:27], v2, s[24:27], s56 offen offset:2048 sc1\n"
    "buffer_load_dwordx4 a[28:31], v2, s[24:27], s56 offen offset:3072 sc1\n"
    "s_mov_b32 s56, 20480\n"
    "buffer_load_dwordx4 a[88:91], v2, s[24:27], s56 offen offset:2048 sc1\n"
    "buffer_load_dwordx4 a[92:95], v2, s[24:27], s56 offen offset:3072 sc1\n"
    "s_waitcnt vmcnt(12)\n"
    "s_waitcnt lgkmcnt(12)\n"
    "v_mfma_f32_16x16x32_bf16 v[16:19], v[80:83], a[0:3], 0\n"
    "v_mfma_f32_16x16x32_bf16 v[48:51], v[80:83], a[64:67], 0\n"
    "ds_read_b64_tr_b16 v[80:81], v5 offset:8192\n"
    "ds_read_b64_tr_b16 v[82:83], v5 offset:12288\n"
    "v_mfma_f32_16x16x32_bf16 v[20:23], v[84:87], a[0:3], 0\n"
    "s_mov_b32 s56, 8192\n"
    "buffer_load_dwordx4 a[32:35], v2, s[24:27], s56 offen offset:0 sc1\n"
    "buffer_load_dwordx4 a[36:39], v2, s[24:27], s56 offen offset:1024 sc1\n"
    "v_mfma_f32_16x16x32_bf16 v[52:55], v[84:87], a[64:67], 0\n"
    "ds_read_b64_tr_b16 v[84:85], v6 offset:8192\n"
    "ds_read_b64_tr_b16 v[86:87], v6 offset:12288\n"
    "s_waitcnt lgkmcnt(12)\n"
    "v_mfma_f32_16x16x32_bf16 v[24:27], v[88:91], a[0:3], 0\n"
    "v_mfma_f32_16x16x32_bf16 v[56:59], v[88:91], a[64:67], 0\n"
    "ds_read_b64_tr_b16 v[88:89], v5 offset:8704\n"
    "ds_read_b64_tr_b16 v[90:91], v5 offset:12800\n"
    "v_mfma_f32_16x16x32_bf16 v[28:31], v[92:95], a[0:3], 0\n"
    "s_mov_b32 s56, 24576\n"
    "buffer_load_dwordx4 a[96:99], v2, s[24:27], s56 offen offset:0 sc1\n"
    "buffer_load_dwordx4 a[100:103], v2, s[24:27], s56 offen offset:1024 sc1\n"
    "v_mfma_f32_16x16x32_bf16 v[60:63], v[92:95], a[64:67], 0\n"
    "ds_read_b64_tr_b16 v[92:93], v6 offset:8704\n"
    "ds_read_b64_tr_b16 v[94:95], v6 offset:12800\n"
    "s_waitcnt lgkmcnt(12)\n"
    "v_mfma_f32_16x16x32_bf16 v[32:35], v[96:99], a[0:3], 0\n"
    "v_mfma_f32_16x16x32_bf16 v[64:67], v[96:99], a[64:67], 0\n"
    "ds_read_b64_tr_b16 v[96:97], v5 offset:9216\n"
    "ds_read_b64_tr_b16 v[98:99], v5 offset:13312\n"
    "v_mfma_f32_16x16x32_bf16 v[36:39], v[100:103], a[0:3], 0\n"
    "s_mov_b32 s56, 8192\n"
    "buffer_load_dwordx4 a[40:43], v2, s[24:27], s56 offen offset:2048 sc1\n"
    "buffer_load_dwordx4 a[44:47], v2, s[24:27], s56 offen offset:3072 sc1\n"
    "v_mfma_f32_16x16x32_bf16 v[68:71], v[100:103], a[64:67], 0\n"
    "ds_read_b64_tr_b16 v[100:101], v6 offset:9216\n"
    "ds_read_b64_tr_b16 v[102:103], v6 offset:13312\n"
    "s_waitcnt lgkmcnt(12)\n"
    "v_mfma_f32_16x16x32_bf16 v[40:43], v[104:107], a[0:3], 0\n"
    "v_mfma_f32_16x16x32_bf16 v[72:75], v[104:107], a[64:67], 0\n"
    "ds_read_b64_tr_b16 v[104:105], v5 offset:9728\n"
    "ds_read_b64_tr_b16 v[106:107], v5 offset:13824\n"
    "v_mfma_f32_16x16x32_bf16 v[44:47], v[108:111], a[0:3], 0\n"
    "s_mov_b32 s56, 24576\n"
    "buffer_load_dwordx4 a[104:107], v2, s[24:27], s56 offen offset:2048 sc1\n"
    "buffer_load_dwordx4 a[108:111], v2, s[24:27], s56 offen offset:3072 sc1\n"
    "v_mfma_f32_16x16x32_bf16 v[76:79], v[108:111], a[64:67], 0\n"
    "ds_read_b64_tr_b16 v[108:109], v6 offset:9728\n"
    "ds_read_b64_tr_b16 v[110:111], v6 offset:13824\n"
    "s_waitcnt lgkmcnt(12)\n"
    "v_mfma_f32_16x16x32_bf16 v[16:19], v[80:83], a[4:7], v[16:19]\n"
    "v_mfma_f32_16x16x32_bf16 v[48:51], v[80:83], a[68:71], v[48:51]\n"
    "ds_read_b64_tr_b16 v[80:81], v5 offset:16384\n"
    "ds_read_b64_tr_b16 v[82:83], v5 offset:20480\n"
    "v_mfma_f32_16x16x32_bf16 v[20:23], v[84:87], a[4:7], v[20:23]\n"
    "v_mfma_f32_16x16x32_bf16 v[52:55], v[84:87], a[68:71], v[52:55]\n"
    "ds_read_b64_tr_b16 v[84:85], v6 offset:16384\n"
    "ds_read_b64_tr_b16 v[86:87], v6 offset:20480\n"
    "s_waitcnt lgkmcnt(12)\n"
    "v_mfma_f32_16x16x32_bf16 v[24:27], v[88:91], a[4:7], v[24:27]\n"
    "v_mfma_f32_16x16x32_bf16 v[56:59], v[88:91], a[68:71], v[56:59]\n"
    "ds_read_b64_tr_b16 v[88:89], v5 offset:16896\n"
    "ds_read_b64_tr_b16 v[90:91], v5 offset:20992\n"
    "v_mfma_f32_16x16x32_bf16 v[28:31], v[92:95], a[4:7], v[28:31]\n"
    "v_mfma_f32_16x16x32_bf16 v[60:63], v[92:95], a[68:71], v[60:63]\n"
    "ds_read_b64_tr_b16 v[92:93], v6 offset:16896\n"
    "ds_read_b64_tr_b16 v[94:95], v6 offset:20992\n"
    "s_waitcnt lgkmcnt(12)\n"
    "v_mfma_f32_16x16x32_bf16 v[32:35], v[96:99], a[4:7], v[32:35]\n"
    "v_mfma_f32_16x16x32_bf16 v[64:67], v[96:99], a[68:71], v[64:67]\n"
    "ds_read_b64_tr_b16 v[96:97], v5 offset:17408\n"
    "ds_read_b64_tr_b16 v[98:99], v5 offset:21504\n"
    "v_mfma_f32_16x16x32_bf16 v[36:39], v[100:103], a[4:7], v[36:39]\n"
    "v_mfma_f32_16x16x32_bf16 v[68:71], v[100:103], a[68:71], v[68:71]\n"
    "ds_read_b64_tr_b16 v[100:101], v6 offset:17408\n"
    "ds_read_b64_tr_b16 v[102:103], v6 offset:21504\n"
    "s_waitcnt lgkmcnt(12)\n"
    "v_mfma_f32_16x16x32_bf16 v[40:43], v[104:107], a[4:7], v[40:43]\n"
    "v_mfma_f32_16x16x32_bf16 v[72:75], v[104:107], a[68:71], v[72:75]\n"
    "ds_read_b64_tr_b16 v[104:105], v5 offset:17920\n"
    "ds_read_b64_tr_b16 v[106:107], v5 offset:22016\n"
    "v_mfma_f32_16x16x32_bf16 v[44:47], v[108:111], a[4:7], v[44:47]\n"
    "v_mfma_f32_16x16x32_bf16 v[76:79], v[108:111], a[68:71], v[76:79]\n"
    "ds_read_b64_tr_b16 v[108:109], v6 offset:17920\n"
    "ds_read_b64_tr_b16 v[110:111], v6 offset:22016\n"
    "s_waitcnt vmcnt(16)\n"
    "s_waitcnt lgkmcnt(12)\n"
    "v_mfma_f32_16x16x32_bf16 v[16:19], v[80:83], a[8:11], v[16:19]\n"
    "v_mfma_f32_16x16x32_bf16 v[48:51], v[80:83], a[72:75], v[48:51]\n"
    "ds_read_b64_tr_b16 v[80:81], v5 offset:24576\n"
    "ds_read_b64_tr_b16 v[82:83], v5 offset:28672\n"
    "v_mfma_f32_16x16x32_bf16 v[20:23], v[84:87], a[8:11], v[20:23]\n"
    "s_mov_b32 s56, 12288\n"
    "buffer_load_dwordx4 a[48:51], v2, s[24:27], s56 offen offset:0 sc1\n"
    "buffer_load_dwordx4 a[52:55], v2, s[24:27], s56 offen offset:1024 sc1\n"
    "v_mfma_f32_16x16x32_bf16 v[52:55], v[84:87], a[72:75], v[52:55]\n"
    "ds_read_b64_tr_b16 v[84:85], v6 offset:24576\n"
    "ds_read_b64_tr_b16 v[86:87], v6 offset:28672\n"
    "s_waitcnt lgkmcnt(12)\n"
    "v_mfma_f32_16x16x32_bf16 v[24:27], v[88:91], a[8:11], v[24:27]\n"
    "v_mfma_f32_16x16x32_bf16 v[56:59], v[88:91], a[72:75], v[56:59]\n"
    "ds_read_b64_tr_b16 v[88:89], v5 offset:25088\n"
    "ds_read_b64_tr_b16 v[90:91], v5 offset:29184\n"
    "v_mfma_f32_16x16x32_bf16 v[28:31], v[92:95], a[8:11], v[28:31]\n"
    "s_mov_b32 s56, 28672\n"
    "buffer_load_dwordx4 a[112:115], v2, s[24:27], s56 offen offset:0 sc1\n"
    "buffer_load_dwordx4 a[116:119], v2, s[24:27], s56 offen offset:1024 sc1\n"
    "v_mfma_f32_16x16x32_bf16 v[60:63], v[92:95], a[72:75], v[60:63]\n"
    "ds_read_b64_tr_b16 v[92:93], v6 offset:25088\n"
    "ds_read_b64_tr_b16 v[94:95], v6 offset:29184\n"
    "s_waitcnt lgkmcnt(12)\n"
    "v_mfma_f32_16x16x32_bf16 v[32:35], v[96:99], a[8:11], v[32:35]\n"
    "v_mfma_f32_16x16x32_bf16 v[64:67], v[96:99], a[72:75], v[64:67]\n"
    "ds_read_b64_tr_b16 v[96:97], v5 offset:25600\n"
    "ds_read_b64_tr_b16 v[98:99], v5 offset:29696\n"
    "v_mfma_f32_16x16x32_bf16 v[36:39], v[100:103], a[8:11], v[36:39]\n"
    "s_mov_b32 s56, 12288\n"
    "buffer_load_dwordx4 a[56:59], v2, s[24:27], s56 offen offset:2048 sc1\n"
    "buffer_load_dwordx4 a[60:63], v2, s[24:27], s56 offen offset:3072 sc1\n"
    "v_mfma_f32_16x16x32_bf16 v[68:71], v[100:103], a[72:75], v[68:71]\n"
    "ds_read_b64_tr_b16 v[100:101], v6 offset:25600\n"
    "ds_read_b64_tr_b16 v[102:103], v6 offset:29696\n"
    "s_waitcnt lgkmcnt(12)\n"
    "v_mfma_f32_16x16x32_bf16 v[40:43], v[104:107], a[8:11], v[40:43]\n"
    "v_mfma_f32_16x16x32_bf16 v[72:75], v[104:107], a[72:75], v[72:75]\n"
    "ds_read_b64_tr_b16 v[104:105], v5 offset:26112\n"
    "ds_read_b64_tr_b16 v[106:107], v5 offset:30208\n"
    "v_mfma_f32_16x16x32_bf16 v[44:47], v[108:111], a[8:11], v[44:47]\n"
    "s_mov_b32 s56, 28672\n"
    "buffer_load_dwordx4 a[120:123], v2, s[24:27], s56 offen offset:2048 sc1\n"
    "buffer_load_dwordx4 a[124:127], v2, s[24:27], s56 offen offset:3072 sc1\n"
    "v_mfma_f32_16x16x32_bf16 v[76:79], v[108:111], a[72:75], v[76:79]\n"
    "ds_read_b64_tr_b16 v[108:109], v6 offset:26112\n"
    "ds_read_b64_tr_b16 v[110:111], v6 offset:30208\n"
    "s_waitcnt lgkmcnt(12)\n"
    "v_mfma_f32_16x16x32_bf16 v[16:19], v[80:83], a[12:15], v[16:19]\n"
    "v_mfma_f32_16x16x32_bf16 v[48:51], v[80:83], a[76:79], v[48:51]\n"
    "ds_read_b64_tr_b16 v[80:81], v5 offset:32768\n"
    "ds_read_b64_tr_b16 v[82:83], v5 offset:36864\n"
    "v_mfma_f32_16x16x32_bf16 v[20:23], v[84:87], a[12:15], v[20:23]\n"
    "v_mfma_f32_16x16x32_bf16 v[52:55], v[84:87], a[76:79], v[52:55]\n"
    "ds_read_b64_tr_b16 v[84:85], v6 offset:32768\n"
    "ds_read_b64_tr_b16 v[86:87], v6 offset:36864\n"
    "s_waitcnt lgkmcnt(12)\n"
    "v_mfma_f32_16x16x32_bf16 v[24:27], v[88:91], a[12:15], v[24:27]\n"
    "v_mfma_f32_16x16x32_bf16 v[56:59], v[88:91], a[76:79], v[56:59]\n"
    "ds_read_b64_tr_b16 v[88:89], v5 offset:33280\n"
    "ds_read_b64_tr_b16 v[90:91], v5 offset:37376\n"
    "v_mfma_f32_16x16x32_bf16 v[28:31], v[92:95], a[12:15], v[28:31]\n"
    "v_mfma_f32_16x16x32_bf16 v[60:63], v[92:95], a[76:79], v[60:63]\n"
    "ds_read_b64_tr_b16 v[92:93], v6 offset:33280\n"
    "ds_read_b64_tr_b16 v[94:95], v6 offset:37376\n"
    "s_waitcnt lgkmcnt(12)\n"
    "v_mfma_f32_16x16x32_bf16 v[32:35], v[96:99], a[12:15], v[32:35]\n"
    "v_mfma_f32_16x16x32_bf16 v[64:67], v[96:99], a[76:79], v[64:67]\n"
    "ds_read_b64_tr_b16 v[96:97], v5 offset:33792\n"
    "ds_read_b64_tr_b16 v[98:99], v5 offset:37888\n"
    "v_mfma_f32_16x16x32_bf16 v[36:39], v[100:103], a[12:15], v[36:39]\n"
    "v_mfma_f32_16x16x32_bf16 v[68:71], v[100:103], a[76:79], v[68:71]\n"
    "ds_read_b64_tr_b16 v[100:101], v6 offset:33792\n"
    "ds_read_b64_tr_b16 v[102:103], v6 offset:37888\n"
    "s_waitcnt lgkmcnt(12)\n"
    "v_mfma_f32_16x16x32_bf16 v[40:43], v[104:107], a[12:15], v[40:43]\n"
    "v_mfma_f32_16x16x32_bf16 v[72:75], v[104:107], a[76:79], v[72:75]\n"
    "ds_read_b64_tr_b16 v[104:105], v5 offset:34304\n"
    "ds_read_b64_tr_b16 v[106:107], v5 offset:38400\n"
    "v_mfma_f32_16x16x32_bf16 v[44:47], v[108:111], a[12:15], v[44:47]\n"
    "v_mfma_f32_16x16x32_bf16 v[76:79], v[108:111], a[76:79], v[76:79]\n"
    "ds_read_b64_tr_b16 v[108:109], v6 offset:34304\n"
    "ds_read_b64_tr_b16 v[110:111], v6 offset:38400\n"
    "s_waitcnt vmcnt(20)\n"
    "s_waitcnt lgkmcnt(12)\n"
    "v_mfma_f32_16x16x32_bf16 v[16:19], v[80:83], a[16:19], v[16:19]\n"
    "v_and_b32 v9, 15, v3\n"
    "v_lshrrev_b32 v10, 4, v3\n"
    "v_lshlrev_b32 v9, 3, v9\n"
    "v_mfma_f32_16x16x32_bf16 v[48:51], v[80:83], a[80:83], v[48:51]\n"
    "ds_read_b64_tr_b16 v[80:81], v5 offset:40960\n"
    "ds_read_b64_tr_b16 v[82:83], v5 offset:45056\n"
    "v_mfma_f32_16x16x32_bf16 v[20:23], v[84:87], a[16:19], v[20:23]\n"
    "v_lshl_add_u32 v9, v10, 9, v9\n"
    "buffer_load_dwordx2 v[112:113], v9, s[40:43], 0 offen offset:0 sc1\n"
    "buffer_load_dwordx2 v[114:115], v9, s[40:43], 0 offen offset:128 sc1\n"
    "v_mfma_f32_16x16x32_bf16 v[52:55], v[84:87], a[80:83], v[52:55]\n"
    "ds_read_b64_tr_b16 v[84:85], v6 offset:40960\n"
    "ds_read_b64_tr_b16 v[86:87], v6 offset:45056\n"
    "s_waitcnt lgkmcnt(12)\n"
    "v_mfma_f32_16x16x32_bf16 v[24:27], v[88:91], a[16:19], v[24:27]\n"
    "buffer_load_dwordx2 v[116:117], v9, s[40:43], 0 offen offset:256 sc1\n"
    "buffer_load_dwordx2 v[118:119], v9, s[40:43], 0 offen offset:384 sc1\n"
    "buffer_load_dwordx2 v[120:121], v9, s[40:43], 0 offen offset:2048 sc1\n"
    "v_mfma_f32_16x16x32_bf16 v[56:59], v[88:91], a[80:83], v[56:59]\n"
    "ds_read_b64_tr_b16 v[88:89], v5 offset:41472\n"
    "ds_read_b64_tr_b16 v[90:91], v5 offset:45568\n"
    "v_mfma_f32_16x16x32_bf16 v[28:31], v[92:95], a[16:19], v[28:31]\n"
    "buffer_load_dwordx2 v[122:123], v9, s[40:43], 0 offen offset:2176 sc1\n"
    "buffer_load_dwordx2 v[124:125], v9, s[40:43], 0 offen offset:2304 sc1\n"
    "buffer_load_dwordx2 v[126:127], v9, s[40:43], 0 offen offset:2432 sc1\n"
    "v_mfma_f32_16x16x32_bf16 v[60:63], v[92:95], a[80:83], v[60:63]\n"
    "ds_read_b64_tr_b16 v[92:93], v6 offset:41472\n"
    "ds_read_b64_tr_b16 v[94:95], v6 offset:45568\n"
    "s_waitcnt lgkmcnt(12)\n"
    "v_mfma_f32_16x16x32_bf16 v[32:35], v[96:99], a[16:19], v[32:35]\n"
    "v_mfma_f32_16x16x32_bf16 v[64:67], v[96:99], a[80:83], v[64:67]\n"
    "ds_read_b64_tr_b16 v[96:97], v5 offset:41984\n"
    "ds_read_b64_tr_b16 v[98:99], v5 offset:46080\n"
    "v_mfma_f32_16x16x32_bf16 v[36:39], v[100:103], a[16:19], v[36:39]\n"
    "v_mfma_f32_16x16x32_bf16 v[68:71], v[100:103], a[80:83], v[68:71]\n"
    "ds_read_b64_tr_b16 v[100:101], v6 offset:41984\n"
    "ds_read_b64_tr_b16 v[102:103], v6 offset:46080\n"
    "s_waitcnt lgkmcnt(12)\n"
    "v_mfma_f32_16x16x32_bf16 v[40:43], v[104:107], a[16:19], v[40:43]\n"
    "v_mfma_f32_16x16x32_bf16 v[72:75], v[104:107], a[80:83], v[72:75]\n"
    "ds_read_b64_tr_b16 v[104:105], v5 offset:42496\n"
    "ds_read_b64_tr_b16 v[106:107], v5 offset:46592\n"
    "v_mfma_f32_16x16x32_bf16 v[44:47], v[108:111], a[16:19], v[44:47]\n"
    "v_mfma_f32_16x16x32_bf16 v[76:79], v[108:111], a[80:83], v[76:79]\n"
    "ds_read_b64_tr_b16 v[108:109], v6 offset:42496\n"
    "ds_read_b64_tr_b16 v[110:111], v6 offset:46592\n"
    "s_waitcnt lgkmcnt(12)\n"
    "v_mfma_f32_16x16x32_bf16 v[16:19], v[80:83], a[20:23], v[16:19]\n"
    "v_mfma_f32_16x16x32_bf16 v[48:51], v[80:83], a[84:87], v[48:51]\n"
    "ds_read_b64_tr_b16 v[80:81], v5 offset:49152\n"
    "ds_read_b64_tr_b16 v[82:83], v5 offset:53248\n"
    "v_mfma_f32_16x16x32_bf16 v[20:23], v[84:87], a[20:23], v[20:23]\n"
    "v_mfma_f32_16x16x32_bf16 v[52:55], v[84:87], a[84:87], v[52:55]\n"
    "ds_read_b64_tr_b16 v[84:85], v6 offset:49152\n"
    "ds_read_b64_tr_b16 v[86:87], v6 offset:53248\n"
    "s_waitcnt lgkmcnt(12)\n"
    "v_mfma_f32_16x16x32_bf16 v[24:27], v[88:91], a[20:23], v[24:27]\n"
    "v_mfma_f32_16x16x32_bf16 v[56:59], v[88:91], a[84:87], v[56:59]\n"
    "ds_read_b64_tr_b16 v[88:89], v5 offset:49664\n"
    "ds_read_b64_tr_b16 v[90:91], v5 offset:53760\n"
    "v_mfma_f32_16x16x32_bf16 v[28:31], v[92:95], a[20:23], v[28:31]\n"
    "v_mfma_f32_16x16x32_bf16 v[60:63], v[92:95], a[84:87], v[60:63]\n"
    "ds_read_b64_tr_b16 v[92:93], v6 offset:49664\n"
    "ds_read_b64_tr_b16 v[94:95], v6 offset:53760\n"
    "s_waitcnt lgkmcnt(12)\n"
    "v_mfma_f32_16x16x32_bf16 v[32:35], v[96:99], a[20:23], v[32:35]\n"
    "v_mfma_f32_16x16x32_bf16 v[64:67], v[96:99], a[84:87], v[64:67]\n"
    "ds_read_b64_tr_b16 v[96:97], v5 offset:50176\n"
    "ds_read_b64_tr_b16 v[98:99], v5 offset:54272\n"
    "v_mfma_f32_16x16x32_bf16 v[36:39], v[100:103], a[20:23], v[36:39]\n"
    "v_mfma_f32_16x16x32_bf16 v[68:71], v[100:103], a[84:87], v[68:71]\n"
    "ds_read_b64_tr_b16 v[100:101], v6 offset:50176\n"
    "ds_read_b64_tr_b16 v[102:103], v6 offset:54272\n"
    "s_waitcnt lgkmcnt(12)\n"
    "v_mfma_f32_16x16x32_bf16 v[40:43], v[104:107], a[20:23], v[40:43]\n"
    "v_mfma_f32_16x16x32_bf16 v[72:75], v[104:107], a[84:87], v[72:75]\n"
    "ds_read_b64_tr_b16 v[104:105], v5 offset:50688\n"
    "ds_read_b64_tr_b16 v[106:107], v5 offset:54784\n"
    "v_mfma_f32_16x16x32_bf16 v[44:47], v[108:111], a[20:23], v[44:47]\n"
    "v_mfma_f32_16x16x32_bf16 v[76:79], v[108:111], a[84:87], v[76:79]\n"
    "ds_read_b64_tr_b16 v[108:109], v6 offset:50688\n"
    "ds_read_b64_tr_b16 v[110:111], v6 offset:54784\n"
    "s_waitcnt vmcnt(24)\n"
    "s_waitcnt lgkmcnt(12)\n"
    "v_mfma_f32_16x16x32_bf16 v[16:19], v[80:83], a[24:27], v[16:19]\n"
    "v_mfma_f32_16x16x32_bf16 v[48:51], v[80:83], a[88:91], v[48:51]\n"
    "ds_read_b64_tr_b16 v[80:81], v5 offset:57344\n"
    "ds_read_b64_tr_b16 v[82:83], v5 offset:61440\n"
    "v_mfma_f32_16x16x32_bf16 v[20:23], v[84:87], a[24:27], v[20:23]\n"
    "v_mfma_f32_16x16x32_bf16 v[52:55], v[84:87], a[88:91], v[52:55]\n"
    "ds_read_b64_tr_b16 v[84:85], v6 offset:57344\n"
    "ds_read_b64_tr_b16 v[86:87], v6 offset:61440\n"
    "s_waitcnt lgkmcnt(12)\n"
    "v_mfma_f32_16x16x32_bf16 v[24:27], v[88:91], a[24:27], v[24:27]\n"
    "v_mfma_f32_16x16x32_bf16 v[56:59], v[88:91], a[88:91], v[56:59]\n"
    "ds_read_b64_tr_b16 v[88:89], v5 offset:57856\n"
    "ds_read_b64_tr_b16 v[90:91], v5 offset:61952\n"
    "v_mfma_f32_16x16x32_bf16 v[28:31], v[92:95], a[24:27], v[28:31]\n"
    "v_mfma_f32_16x16x32_bf16 v[60:63], v[92:95], a[88:91], v[60:63]\n"
    "ds_read_b64_tr_b16 v[92:93], v6 offset:57856\n"
    "ds_read_b64_tr_b16 v[94:95], v6 offset:61952\n"
    "s_waitcnt lgkmcnt(12)\n"
    "v_mfma_f32_16x16x32_bf16 v[32:35], v[96:99], a[24:27], v[32:35]\n"
    "v_mfma_f32_16x16x32_bf16 v[64:67], v[96:99], a[88:91], v[64:67]\n"
    "ds_read_b64_tr_b16 v[96:97], v5 offset:58368\n"
    "ds_read_b64_tr_b16 v[98:99], v5 offset:62464\n"
    "v_mfma_f32_16x16x32_bf16 v[36:39], v[100:103], a[24:27], v[36:39]\n"
    "v_mfma_f32_16x16x32_bf16 v[68:71], v[100:103], a[88:91], v[68:71]\n"
    "ds_read_b64_tr_b16 v[100:101], v6 offset:58368\n"
    "ds_read_b64_tr_b16 v[102:103], v6 offset:62464\n"
    "s_waitcnt lgkmcnt(12)\n"
    "v_mfma_f32_16x16x32_bf16 v[40:43], v[104:107], a[24:27], v[40:43]\n"
    "v_mfma_f32_16x16x32_bf16 v[72:75], v[104:107], a[88:91], v[72:75]\n"
    "ds_read_b64_tr_b16 v[104:105], v5 offset:58880\n"
    "ds_read_b64_tr_b16 v[106:107], v5 offset:62976\n"
    "v_mfma_f32_16x16x32_bf16 v[44:47], v[108:111], a[24:27], v[44:47]\n"
    "v_mfma_f32_16x16x32_bf16 v[76:79], v[108:111], a[88:91], v[76:79]\n"
    "ds_read_b64_tr_b16 v[108:109], v6 offset:58880\n"
    "ds_read_b64_tr_b16 v[110:111], v6 offset:62976\n"
    "s_waitcnt lgkmcnt(12)\n"
    "v_mfma_f32_16x16x32_bf16 v[16:19], v[80:83], a[28:31], v[16:19]\n"
    "v_mfma_f32_16x16x32_bf16 v[48:51], v[80:83], a[92:95], v[48:51]\n"
    "ds_read_b64_tr_b16 v[80:81], v7 offset:0\n"
    "ds_read_b64_tr_b16 v[82:83], v7 offset:4096\n"
    "v_mfma_f32_16x16x32_bf16 v[20:23], v[84:87], a[28:31], v[20:23]\n"
    "v_mfma_f32_16x16x32_bf16 v[52:55], v[84:87], a[92:95], v[52:55]\n"
    "ds_read_b64_tr_b16 v[84:85], v8 offset:0\n"
    "ds_read_b64_tr_b16 v[86:87], v8 offset:4096\n"
    "s_waitcnt lgkmcnt(12)\n"
    "v_mfma_f32_16x16x32_bf16 v[24:27], v[88:91], a[28:31], v[24:27]\n"
    "v_mfma_f32_16x16x32_bf16 v[56:59], v[88:91], a[92:95], v[56:59]\n"
    "ds_read_b64_tr_b16 v[88:89], v7 offset:512\n"
    "ds_read_b64_tr_b16 v[90:91], v7 offset:4608\n"
    "v_mfma_f32_16x16x32_bf16 v[28:31], v[92:95], a[28:31], v[28:31]\n"
    "v_mfma_f32_16x16x32_bf16 v[60:63], v[92:95], a[92:95], v[60:63]\n"
    "ds_read_b64_tr_b16 v[92:93], v8 offset:512\n"
    "ds_read_b64_tr_b16 v[94:95], v8 offset:4608\n"
    "s_waitcnt lgkmcnt(12)\n"
    "v_mfma_f32_16x16x32_bf16 v[32:35], v[96:99], a[28:31], v[32:35]\n"
    "v_mfma_f32_16x16x32_bf16 v[64:67], v[96:99], a[92:95], v[64:67]\n"
    "ds_read_b64_tr_b16 v[96:97], v7 offset:1024\n"
    "ds_read_b64_tr_b16 v[98:99], v7 offset:5120\n"
    "v_mfma_f32_16x16x32_bf16 v[36:39], v[100:103], a[28:31], v[36:39]\n"
    "v_mfma_f32_16x16x32_bf16 v[68:71], v[100:103], a[92:95], v[68:71]\n"
    "ds_read_b64_tr_b16 v[100:101], v8 offset:1024\n"
    "ds_read_b64_tr_b16 v[102:103], v8 offset:5120\n"
    "s_waitcnt lgkmcnt(12)\n"
    "v_mfma_f32_16x16x32_bf16 v[40:43], v[104:107], a[28:31], v[40:43]\n"
    "v_mfma_f32_16x16x32_bf16 v[72:75], v[104:107], a[92:95], v[72:75]\n"
    "ds_read_b64_tr_b16 v[104:105], v7 offset:1536\n"
    "ds_read_b64_tr_b16 v[106:107], v7 offset:5632\n"
    "v_mfma_f32_16x16x32_bf16 v[44:47], v[108:111], a[28:31], v[44:47]\n"
    "v_mfma_f32_16x16x32_bf16 v[76:79], v[108:111], a[92:95], v[76:79]\n"
    "ds_read_b64_tr_b16 v[108:109], v8 offset:1536\n"
    "ds_read_b64_tr_b16 v[110:111], v8 offset:5632\n"
    "s_waitcnt vmcnt(20)\n"
    "s_waitcnt lgkmcnt(12)\n"
    "v_mfma_f32_16x16x32_bf16 v[16:19], v[80:83], a[32:35], v[16:19]\n"
    "s_waitcnt vmcnt(0)\n"
    "v_add_f32 v15, v112, v114\n"
    "v_add_f32 v1, v120, v122\n"
    "v_add_f32 v15, v15, v116\n"
    "v_mfma_f32_16x16x32_bf16 v[48:51], v[80:83], a[96:99], v[48:51]\n"
    "ds_read_b64_tr_b16 v[80:81], v7 offset:8192\n"
    "ds_read_b64_tr_b16 v[82:83], v7 offset:12288\n"
    "v_mfma_f32_16x16x32_bf16 v[20:23], v[84:87], a[32:35], v[20:23]\n"
    "v_add_f32 v1, v1, v124\n"
    "v_add_f32 v15, v15, v118\n"
    "v_add_f32 v1, v1, v126\n"
    "v_mfma_f32_16x16x32_bf16 v[52:55], v[84:87], a[96:99], v[52:55]\n"
    "ds_read_b64_tr_b16 v[84:85], v8 offset:8192\n"
    "ds_read_b64_tr_b16 v[86:87], v8 offset:12288\n"
    "s_waitcnt lgkmcnt(12)\n"
    "v_mfma_f32_16x16x32_bf16 v[24:27], v[88:91], a[32:35], v[24:27]\n"
    "v_mov_b32 v10, v15\n"
    "v_mov_b32 v11, v1\n"
    "s_nop 0\n"
    "v_permlane16_swap_b32 v15, v10\n"
    "v_mfma_f32_16x16x32_bf16 v[56:59], v[88:91], a[96:99], v[56:59]\n"
    "ds_read_b64_tr_b16 v[88:89], v7 offset:8704\n"
    "ds_read_b64_tr_b16 v[90:91], v7 offset:12800\n"
    "v_mfma_f32_16x16x32_bf16 v[28:31], v[92:95], a[32:35], v[28:31]\n"
    "v_permlane16_swap_b32 v1, v11\n"
    "v_add_f32 v15, v15, v10\n"
    "v_add_f32 v1, v1, v11\n"
    "v_mfma_f32_16x16x32_bf16 v[60:63], v[92:95], a[96:99], v[60:63]\n"
    "ds_read_b64_tr_b16 v[92:93], v8 offset:8704\n"
    "ds_read_b64_tr_b16 v[94:95], v8 offset:12800\n"
    "s_waitcnt lgkmcnt(12)\n"
    "v_mfma_f32_16x16x32_bf16 v[32:35], v[96:99], a[32:35], v[32:35]\n"
    "v_mov_b32 v10, v15\n"
    "v_mov_b32 v11, v1\n"
    "s_nop 0\n"
    "v_permlane32_swap_b32 v15, v10\n"
    "v_mfma_f32_16x16x32_bf16 v[64:67], v[96:99], a[96:99], v[64:67]\n"
    "ds_read_b64_tr_b16 v[96:97], v7 offset:9216\n"
    "ds_read_b64_tr_b16 v[98:99], v7 offset:13312\n"
    "v_mfma_f32_16x16x32_bf16 v[36:39], v[100:103], a[32:35], v[36:39]\n"
    "v_permlane32_swap_b32 v1, v11\n"
    "v_add_f32 v15, v15, v10\n"
    "v_add_f32 v1, v1, v11\n"
    "v_mfma_f32_16x16x32_bf16 v[68:71], v[100:103], a[96:99], v[68:71]\n"
    "ds_read_b64_tr_b16 v[100:101], v8 offset:9216\n"
    "ds_read_b64_tr_b16 v[102:103], v8 offset:13312\n"
    "s_waitcnt lgkmcnt(12)\n"
    "v_mfma_f32_16x16x32_bf16 v[40:43], v[104:107], a[32:35], v[40:43]\n"
    "v_mul_f32 v15, 0x3d800000, v15\n"
    "v_mul_f32 v1, 0x3d800000, v1\n"
    "v_add_f32 v13, v113, v115\n"
    "v_mfma_f32_16x16x32_bf16 v[72:75], v[104:107], a[96:99], v[72:75]\n"
    "ds_read_b64_tr_b16 v[104:105], v7 offset:9728\n"
    "ds_read_b64_tr_b16 v[106:107], v7 offset:13824\n"
    "v_mfma_f32_16x16x32_bf16 v[44:47], v[108:111], a[32:35], v[44:47]\n"
    "v_add_f32 v14, v121, v123\n"
    "v_add_f32 v13, v13, v117\n"
    "v_add_f32 v14, v14, v125\n"
    "v_mfma_f32_16x16x32_bf16 v[76:79], v[108:111], a[96:99], v[76:79]\n"
    "ds_read_b64_tr_b16 v[108:109], v8 offset:9728\n"
    "ds_read_b64_tr_b16 v[110:111], v8 offset:13824\n"
    "s_waitcnt lgkmcnt(12)\n"
    "v_mfma_f32_16x16x32_bf16 v[16:19], v[80:83], a[36:39], v[16:19]\n"
    "v_add_f32 v13, v13, v119\n"
    "v_add_f32 v14, v14, v127\n"
    "v_sub_f32 v112, v112, v15\n"
    "v_mfma_f32_16x16x32_bf16 v[48:51], v[80:83], a[100:103], v[48:51]\n"
    "ds_read_b64_tr_b16 v[80:81], v7 offset:16384\n"
    "ds_read_b64_tr_b16 v[82:83], v7 offset:20480\n"
    "v_mfma_f32_16x16x32_bf16 v[20:23], v[84:87], a[36:39], v[20:23]\n"
    "v_sub_f32 v120, v120, v1\n"
    "v_mul_f32 v113, 0x42000000, v112\n"
    "v_mul_f32 v121, 0x42000000, v120\n"
    "v_mfma_f32_16x16x32_bf16 v[52:55], v[84:87], a[100:103], v[52:55]\n"
    "ds_read_b64_tr_b16 v[84:85], v8 offset:16384\n"
    "ds_read_b64_tr_b16 v[86:87], v8 offset:20480\n"
    "s_waitcnt lgkmcnt(12)\n"
    "v_mfma_f32_16x16x32_bf16 v[24:27], v[88:91], a[36:39], v[24:27]\n"
    "v_fmac_f32 v13, v112, v113\n"
    "v_fmac_f32 v14, v120, v121\n"
    "v_sub_f32 v114, v114, v15\n"
    "v_mfma_f32_16x16x32_bf16 v[56:59], v[88:91], a[100:103], v[56:59]\n"
    "ds_read_b64_tr_b16 v[88:89], v7 offset:16896\n"
    "ds_read_b64_tr_b16 v[90:91], v7 offset:20992\n"
    "v_mfma_f32_16x16x32_bf16 v[28:31], v[92:95], a[36:39], v[28:31]\n"
    "v_sub_f32 v122, v122, v1\n"
    "v_mul_f32 v115, 0x42000000, v114\n"
    "v_mul_f32 v123, 0x42000000, v122\n"
    "v_mfma_f32_16x16x32_bf16 v[60:63], v[92:95], a[100:103], v[60:63]\n"
    "ds_read_b64_tr_b16 v[92:93], v8 offset:16896\n"
    "ds_read_b64_tr_b16 v[94:95], v8 offset:20992\n"
    "s_waitcnt lgkmcnt(12)\n"
    "v_mfma_f32_16x16x32_bf16 v[32:35], v[96:99], a[36:39], v[32:35]\n"
    "v_fmac_f32 v13, v114, v115\n"
    "v_fmac_f32 v14, v122, v123\n"
    "v_sub_f32 v116, v116, v15\n"
    "v_mfma_f32_16x16x32_bf16 v[64:67], v[96:99], a[100:103], v[64:67]\n"
    "ds_read_b64_tr_b16 v[96:97], v7 offset:17408\n"
    "ds_read_b64_tr_b16 v[98:99], v7 offset:21504\n"
    "v_mfma_f32_16x16x32_bf16 v[36:39], v[100:103], a[36:39], v[36:39]\n"
    "v_sub_f32 v124, v124, v1\n"
    "v_mul_f32 v117, 0x42000000, v116\n"
    "v_mul_f32 v125, 0x42000000, v124\n"
    "v_mfma_f32_16x16x32_bf16 v[68:71], v[100:103], a[100:103], v[68:71]\n"
    "ds_read_b64_tr_b16 v[100:101], v8 offset:17408\n"
    "ds_read_b64_tr_b16 v[102:103], v8 offset:21504\n"
    "s_waitcnt lgkmcnt(12)\n"
    "v_mfma_f32_16x16x32_bf16 v[40:43], v[104:107], a[36:39], v[40:43]\n"
    "v_fmac_f32 v13, v116, v117\n"
    "v_fmac_f32 v14, v124, v125\n"
    "v_sub_f32 v118, v118, v15\n"
    "v_mfma_f32_16x16x32_bf16 v[72:75], v[104:107], a[100:103], v[72:75]\n"
    "ds_read_b64_tr_b16 v[104:105], v7 offset:17920\n"
    "ds_read_b64_tr_b16 v[106:107], v7 offset:22016\n"
    "v_mfma_f32_16x16x32_bf16 v[44:47], v[108:111], a[36:39], v[44:47]\n"
    "v_sub_f32 v126, v126, v1\n"
    "v_mul_f32 v119, 0x42000000, v118\n"
    "v_mul_f32 v127, 0x42000000, v126\n"
    "v_mfma_f32_16x16x32_bf16 v[76:79], v[108:111], a[100:103], v[76:79]\n"
    "ds_read_b64_tr_b16 v[108:109], v8 offset:17920\n"
    "ds_read_b64_tr_b16 v[110:111], v8 offset:22016\n"
    "s_waitcnt lgkmcnt(12)\n"
    "v_mfma_f32_16x16x32_bf16 v[16:19], v[80:83], a[40:43], v[16:19]\n"
    "s_mov_b32 s57, 0\n"
    "v_fmac_f32 v13, v118, v119\n"
    "v_mfma_f32_16x16x32_bf16 v[48:51], v[80:83], a[104:107], v[48:51]\n"
    "ds_read_b64_tr_b16 v[80:81], v7 offset:24576\n"
    "ds_read_b64_tr_b16 v[82:83], v7 offset:28672\n"
    "v_mfma_f32_16x16x32_bf16 v[20:23], v[84:87], a[40:43], v[20:23]\n"
    "buffer_load_dwordx4 a[0:3], v12, s[36:39], s57 offen offset:0 sc1\n"
    "v_fmac_f32 v14, v126, v127\n"
    "v_mfma_f32_16x16x32_bf16 v[52:55], v[84:87], a[104:107], v[52:55]\n"
    "ds_read_b64_tr_b16 v[84:85], v8 offset:24576\n"
    "ds_read_b64_tr_b16 v[86:87], v8 offset:28672\n"
    "s_waitcnt lgkmcnt(12)\n"
    "v_mfma_f32_16x16x32_bf16 v[24:27], v[88:91], a[40:43], v[24:27]\n"
    "buffer_load_dwordx4 a[16:19], v12, s[36:39], s57 offen offset:64 sc1\n"
    "v_mov_b32 v10, v13\n"
    "v_mfma_f32_16x16x32_bf16 v[56:59], v[88:91], a[104:107], v[56:59]\n"
    "ds_read_b64_tr_b16 v[88:89], v7 offset:25088\n"
    "ds_read_b64_tr_b16 v[90:91], v7 offset:29184\n"
    "v_mfma_f32_16x16x32_bf16 v[28:31], v[92:95], a[40:43], v[28:31]\n"
    "buffer_load_dwordx4 a[64:67], v12, s[36:39], s57 offen offset:128 sc1\n"
    "v_mov_b32 v11, v14\n"
    "v_mfma_f32_16x16x32_bf16 v[60:63], v[92:95], a[104:107], v[60:63]\n"
    "ds_read_b64_tr_b16 v[92:93], v8 offset:25088\n"
    "ds_read_b64_tr_b16 v[94:95], v8 offset:29184\n"
    "s_waitcnt lgkmcnt(12)\n"
    "v_mfma_f32_16x16x32_bf16 v[32:35], v[96:99], a[40:43], v[32:35]\n"
    "buffer_load_dwordx4 a[80:83], v12, s[36:39], s57 offen offset:192 sc1\n"
    "v_permlane16_swap_b32 v13, v10\n"
    "v_mfma_f32_16x16x32_bf16 v[64:67], v[96:99], a[104:107], v[64:67]\n"
    "ds_read_b64_tr_b16 v[96:97], v7 offset:25600\n"
    "ds_read_b64_tr_b16 v[98:99], v7 offset:29696\n"
    "v_mfma_f32_16x16x32_bf16 v[36:39], v[100:103], a[40:43], v[36:39]\n"
    "s_mov_b32 s57, 14336\n"
    "v_permlane16_swap_b32 v14, v11\n"
    "v_mfma_f32_16x16x32_bf16 v[68:71], v[100:103], a[104:107], v[68:71]\n"
    "ds_read_b64_tr_b16 v[100:101], v8 offset:25600\n"
    "ds_read_b64_tr_b16 v[102:103], v8 offset:29696\n"
    "s_waitcnt lgkmcnt(12)\n"
    "v_mfma_f32_16x16x32_bf16 v[40:43], v[104:107], a[40:43], v[40:43]\n"
    "buffer_load_dwordx4 a[4:7], v12, s[36:39], s57 offen offset:0 sc1\n"
    "v_add_f32 v13, v13, v10\n"
    "v_mfma_f32_16x16x32_bf16 v[72:75], v[104:107], a[104:107], v[72:75]\n"
    "ds_read_b64_tr_b16 v[104:105], v7 offset:26112\n"
    "ds_read_b64_tr_b16 v[106:107], v7 offset:30208\n"
    "v_mfma_f32_16x16x32_bf16 v[44:47], v[108:111], a[40:43], v[44:47]\n"
    "buffer_load_dwordx4 a[20:23], v12, s[36:39], s57 offen offset:64 sc1\n"
    "v_add_f32 v14, v14, v11\n"
    "v_mfma_f32_16x16x32_bf16 v[76:79], v[108:111], a[104:107], v[76:79]\n"
    "ds_read_b64_tr_b16 v[108:109], v8 offset:26112\n"
    "ds_read_b64_tr_b16 v[110:111], v8 offset:30208\n"
    "s_waitcnt lgkmcnt(12)\n"
    "v_mfma_f32_16x16x32_bf16 v[16:19], v[80:83], a[44:47], v[16:19]\n"
    "buffer_load_dwordx4 a[68:71], v12, s[36:39], s57 offen offset:128 sc1\n"
    "v_mov_b32 v10, v13\n"
    "v_mfma_f32_16x16x32_bf16 v[48:51], v[80:83], a[108:111], v[48:51]\n"
    "ds_read_b64_tr_b16 v[80:81], v7 offset:32768\n"
    "ds_read_b64_tr_b16 v[82:83], v7 offset:36864\n"
    "v_mfma_f32_16x16x32_bf16 v[20:23], v[84:87], a[44:47], v[20:23]\n"
    "buffer_load_dwordx4 a[84:87], v12, s[36:39], s57 offen offset:192 sc1\n"
    "v_mov_b32 v11, v14\n"
    "v_mfma_f32_16x16x32_bf16 v[52:55], v[84:87], a[108:111], v[52:55]\n"
    "ds_read_b64_tr_b16 v[84:85], v8 offset:32768\n"
    "ds_read_b64_tr_b16 v[86:87], v8 offset:36864\n"
    "s_waitcnt lgkmcnt(12)\n"
    "v_mfma_f32_16x16x32_bf16 v[24:27], v[88:91], a[44:47], v[24:27]\n"
    "s_mov_b32 s57, 7168\n"
    "v_permlane32_swap_b32 v13, v10\n"
    "v_mfma_f32_16x16x32_bf16 v[56:59], v[88:91], a[108:111], v[56:59]\n"
    "ds_read_b64_tr_b16 v[88:89], v7 offset:33280\n"
    "ds_read_b64_tr_b16 v[90:91], v7 offset:37376\n"
    "v_mfma_f32_16x16x32_bf16 v[28:31], v[92:95], a[44:47], v[28:31]\n"
    "buffer_load_dwordx4 a[8:11], v12, s[36:39], s57 offen offset:0 sc1\n"
    "v_permlane32_swap_b32 v14, v11\n"
    "v_mfma_f32_16x16x32_bf16 v[60:63], v[92:95], a[108:111], v[60:63]\n"
    "ds_read_b64_tr_b16 v[92:93], v8 offset:33280\n"
    "ds_read_b64_tr_b16 v[94:95], v8 offset:37376\n"
    "s_waitcnt lgkmcnt(12)\n"
    "v_mfma_f32_16x16x32_bf16 v[32:35], v[96:99], a[44:47], v[32:35]\n"
    "buffer_load_dwordx4 a[24:27], v12, s[36:39], s57 offen offset:64 sc1\n"
    "v_add_f32 v13, v13, v10\n"
    "v_mfma_f32_16x16x32_bf16 v[64:67], v[96:99], a[108:111], v[64:67]\n"
    "ds_read_b64_tr_b16 v[96:97], v7 offset:33792\n"
    "ds_read_b64_tr_b16 v[98:99], v7 offset:37888\n"
    "v_mfma_f32_16x16x32_bf16 v[36:39], v[100:103], a[44:47], v[36:39]\n"
    "buffer_load_dwordx4 a[72:75], v12, s[36:39], s57 offen offset:128 sc1\n"
    "v_add_f32 v14, v14, v11\n"
    "v_mfma_f32_16x16x32_bf16 v[68:71], v[100:103], a[108:111], v[68:71]\n"
    "ds_read_b64_tr_b16 v[100:101], v8 offset:33792\n"
    "ds_read_b64_tr_b16 v[102:103], v8 offset:37888\n"
    "s_waitcnt lgkmcnt(12)\n"
    "v_mfma_f32_16x16x32_bf16 v[40:43], v[104:107], a[44:47], v[40:43]\n"
    "buffer_load_dwordx4 a[88:91], v12, s[36:39], s57 offen offset:192 sc1\n"
    "v_mov_b32 v10, 0x3727c5ac\n"
    "v_mfma_f32_16x16x32_bf16 v[72:75], v[104:107], a[108:111], v[72:75]\n"
    "ds_read_b64_tr_b16 v[104:105], v7 offset:34304\n"
    "ds_read_b64_tr_b16 v[106:107], v7 offset:38400\n"
    "v_mfma_f32_16x16x32_bf16 v[44:47], v[108:111], a[44:47], v[44:47]\n"
    "s_mov_b32 s57, 21504\n"
    "v_mov_b32 v11, 0x3727c5ac\n"
    "v_mfma_f32_16x16x32_bf16 v[76:79], v[108:111], a[108:111], v[76:79]\n"
    "ds_read_b64_tr_b16 v[108:109], v8 offset:34304\n"
    "ds_read_b64_tr_b16 v[110:111], v8 offset:38400\n"
    "s_waitcnt lgkmcnt(12)\n"
    "v_mfma_f32_16x16x32_bf16 v[16:19], v[80:83], a[48:51], v[16:19]\n"
    "buffer_load_dwordx4 a[12:15], v12, s[36:39], s57 offen offset:0 sc1\n"
    "v_fmac_f32 v10, 0x3b000000, v13\n"
    "v_mfma_f32_16x16x32_bf16 v[48:51], v[80:83], a[112:115], v[48:51]\n"
    "ds_read_b64_tr_b16 v[80:81], v7 offset:40960\n"
    "ds_read_b64_tr_b16 v[82:83], v7 offset:45056\n"
    "v_mfma_f32_16x16x32_bf16 v[20:23], v[84:87], a[48:51], v[20:23]\n"
    "buffer_load_dwordx4 a[28:31], v12, s[36:39], s57 offen offset:64 sc1\n"
    "v_fmac_f32 v11, 0x3b000000, v14\n"
    "v_mfma_f32_16x16x32_bf16 v[52:55], v[84:87], a[112:115], v[52:55]\n"
    "ds_read_b64_tr_b16 v[84:85], v8 offset:40960\n"
    "ds_read_b64_tr_b16 v[86:87], v8 offset:45056\n"
    "s_waitcnt lgkmcnt(12)\n"
    "v_mfma_f32_16x16x32_bf16 v[24:27], v[88:91], a[48:51], v[24:27]\n"
    "buffer_load_dwordx4 a[76:79], v12, s[36:39], s57 offen offset:128 sc1\n"
    "v_rsq_f32 v13, v10\n"
    "v_mfma_f32_16x16x32_bf16 v[56:59], v[88:91], a[112:115], v[56:59]\n"
    "ds_read_b64_tr_b16 v[88:89], v7 offset:41472\n"
    "ds_read_b64_tr_b16 v[90:91], v7 offset:45568\n"
    "v_mfma_f32_16x16x32_bf16 v[28:31], v[92:95], a[48:51], v[28:31]\n"
    "buffer_load_dwordx4 a[92:95], v12, s[36:39], s57 offen offset:192 sc1\n"
    "v_rsq_f32 v14, v11\n"
    "v_mfma_f32_16x16x32_bf16 v[60:63], v[92:95], a[112:115], v[60:63]\n"
    "ds_read_b64_tr_b16 v[92:93], v8 offset:41472\n"
    "ds_read_b64_tr_b16 v[94:95], v8 offset:45568\n"
    "s_waitcnt lgkmcnt(12)\n"
    "v_mfma_f32_16x16x32_bf16 v[32:35], v[96:99], a[48:51], v[32:35]\n"
    "v_mul_f32 v15, v15, v13\n"
    "v_mul_f32 v1, v1, v14\n"
    "v_sub_f32 v15, 0, v15\n"
    "v_mfma_f32_16x16x32_bf16 v[64:67], v[96:99], a[112:115], v[64:67]\n"
    "ds_read_b64_tr_b16 v[96:97], v7 offset:41984\n"
    "ds_read_b64_tr_b16 v[98:99], v7 offset:46080\n"
    "v_mfma_f32_16x16x32_bf16 v[36:39], v[100:103], a[48:51], v[36:39]\n"
    "v_sub_f32 v1, 0, v1\n"
    "v_mfma_f32_16x16x32_bf16 v[68:71], v[100:103], a[112:115], v[68:71]\n"
    "ds_read_b64_tr_b16 v[100:101], v8 offset:41984\n"
    "ds_read_b64_tr_b16 v[102:103], v8 offset:46080\n"
    "s_waitcnt lgkmcnt(12)\n"
    "v_mfma_f32_16x16x32_bf16 v[40:43], v[104:107], a[48:51], v[40:43]\n"
    "v_mfma_f32_16x16x32_bf16 v[72:75], v[104:107], a[112:115], v[72:75]\n"
    "ds_read_b64_tr_b16 v[104:105], v7 offset:42496\n"
    "ds_read_b64_tr_b16 v[106:107], v7 offset:46592\n"
    "v_mfma_f32_16x16x32_bf16 v[44:47], v[108:111], a[48:51], v[44:47]\n"
    "v_mfma_f32_16x16x32_bf16 v[76:79], v[108:111], a[112:115], v[76:79]\n"
    "ds_read_b64_tr_b16 v[108:109], v8 offset:42496\n"
    "ds_read_b64_tr_b16 v[110:111], v8 offset:46592\n"
    "s_waitcnt lgkmcnt(12)\n"
    "v_mfma_f32_16x16x32_bf16 v[16:19], v[80:83], a[52:55], v[16:19]\n"
    "v_mfma_f32_16x16x32_bf16 v[48:51], v[80:83], a[116:119], v[48:51]\n"
    "ds_read_b64_tr_b16 v[80:81], v7 offset:49152\n"
    "ds_read_b64_tr_b16 v[82:83], v7 offset:53248\n"
    "v_mfma_f32_16x16x32_bf16 v[20:23], v[84:87], a[52:55], v[20:23]\n"
    "v_mfma_f32_16x16x32_bf16 v[52:55], v[84:87], a[116:119], v[52:55]\n"
    "ds_read_b64_tr_b16 v[84:85], v8 offset:49152\n"
    "ds_read_b64_tr_b16 v[86:87], v8 offset:53248\n"
    "s_waitcnt lgkmcnt(12)\n"
    "v_mfma_f32_16x16x32_bf16 v[24:27], v[88:91], a[52:55], v[24:27]\n"
    "v_mfma_f32_16x16x32_bf16 v[56:59], v[88:91], a[116:119], v[56:59]\n"
    "ds_read_b64_tr_b16 v[88:89], v7 offset:49664\n"
    "ds_read_b64_tr_b16 v[90:91], v7 offset:53760\n"
    "v_mfma_f32_16x16x32_bf16 v[28:31], v[92:95], a[52:55], v[28:31]\n"
    "v_mfma_f32_16x16x32_bf16 v[60:63], v[92:95], a[116:119], v[60:63]\n"
    "ds_read_b64_tr_b16 v[92:93], v8 offset:49664\n"
    "ds_read_b64_tr_b16 v[94:95], v8 offset:53760\n"
    "s_waitcnt lgkmcnt(12)\n"
    "v_mfma_f32_16x16x32_bf16 v[32:35], v[96:99], a[52:55], v[32:35]\n"
    "v_mfma_f32_16x16x32_bf16 v[64:67], v[96:99], a[116:119], v[64:67]\n"
    "ds_read_b64_tr_b16 v[96:97], v7 offset:50176\n"
    "ds_read_b64_tr_b16 v[98:99], v7 offset:54272\n"
    "v_mfma_f32_16x16x32_bf16 v[36:39], v[100:103], a[52:55], v[36:39]\n"
    "v_mfma_f32_16x16x32_bf16 v[68:71], v[100:103], a[116:119], v[68:71]\n"
    "ds_read_b64_tr_b16 v[100:101], v8 offset:50176\n"
    "ds_read_b64_tr_b16 v[102:103], v8 offset:54272\n"
    "s_waitcnt lgkmcnt(12)\n"
    "v_mfma_f32_16x16x32_bf16 v[40:43], v[104:107], a[52:55], v[40:43]\n"
    "v_mfma_f32_16x16x32_bf16 v[72:75], v[104:107], a[116:119], v[72:75]\n"
    "ds_read_b64_tr_b16 v[104:105], v7 offset:50688\n"
    "ds_read_b64_tr_b16 v[106:107], v7 offset:54784\n"
    "v_mfma_f32_16x16x32_bf16 v[44:47], v[108:111], a[52:55], v[44:47]\n"
    "v_mfma_f32_16x16x32_bf16 v[76:79], v[108:111], a[116:119], v[76:79]\n"
    "ds_read_b64_tr_b16 v[108:109], v8 offset:50688\n"
    "ds_read_b64_tr_b16 v[110:111], v8 offset:54784\n"
    "s_waitcnt lgkmcnt(12)\n"
    "v_mfma_f32_16x16x32_bf16 v[16:19], v[80:83], a[56:59], v[16:19]\n"
    "v_mfma_f32_16x16x32_bf16 v[48:51], v[80:83], a[120:123], v[48:51]\n"
    "ds_read_b64_tr_b16 v[80:81], v7 offset:57344\n"
    "ds_read_b64_tr_b16 v[82:83], v7 offset:61440\n"
    "v_mfma_f32_16x16x32_bf16 v[20:23], v[84:87], a[56:59], v[20:23]\n"
    "v_mfma_f32_16x16x32_bf16 v[52:55], v[84:87], a[120:123], v[52:55]\n"
    "ds_read_b64_tr_b16 v[84:85], v8 offset:57344\n"
    "ds_read_b64_tr_b16 v[86:87], v8 offset:61440\n"
    "s_waitcnt lgkmcnt(12)\n"
    "v_mfma_f32_16x16x32_bf16 v[24:27], v[88:91], a[56:59], v[24:27]\n"
    "v_mfma_f32_16x16x32_bf16 v[56:59], v[88:91], a[120:123], v[56:59]\n"
    "ds_read_b64_tr_b16 v[88:89], v7 offset:57856\n"
    "ds_read_b64_tr_b16 v[90:91], v7 offset:61952\n"
    "v_mfma_f32_16x16x32_bf16 v[28:31], v[92:95], a[56:59], v[28:31]\n"
    "v_mfma_f32_16x16x32_bf16 v[60:63], v[92:95], a[120:123], v[60:63]\n"
    "ds_read_b64_tr_b16 v[92:93], v8 offset:57856\n"
    "ds_read_b64_tr_b16 v[94:95], v8 offset:61952\n"
    "s_waitcnt lgkmcnt(12)\n"
    "v_mfma_f32_16x16x32_bf16 v[32:35], v[96:99], a[56:59], v[32:35]\n"
    "v_mfma_f32_16x16x32_bf16 v[64:67], v[96:99], a[120:123], v[64:67]\n"
    "ds_read_b64_tr_b16 v[96:97], v7 offset:58368\n"
    "ds_read_b64_tr_b16 v[98:99], v7 offset:62464\n"
    "v_mfma_f32_16x16x32_bf16 v[36:39], v[100:103], a[56:59], v[36:39]\n"
    "v_mfma_f32_16x16x32_bf16 v[68:71], v[100:103], a[120:123], v[68:71]\n"
    "ds_read_b64_tr_b16 v[100:101], v8 offset:58368\n"
    "ds_read_b64_tr_b16 v[102:103], v8 offset:62464\n"
    "s_waitcnt lgkmcnt(12)\n"
    "v_mfma_f32_16x16x32_bf16 v[40:43], v[104:107], a[56:59], v[40:43]\n"
    "v_mfma_f32_16x16x32_bf16 v[72:75], v[104:107], a[120:123], v[72:75]\n"
    "ds_read_b64_tr_b16 v[104:105], v7 offset:58880\n"
    "ds_read_b64_tr_b16 v[106:107], v7 offset:62976\n"
    "v_mfma_f32_16x16x32_bf16 v[44:47], v[108:111], a[56:59], v[44:47]\n"
    "v_mfma_f32_16x16x32_bf16 v[76:79], v[108:111], a[120:123], v[76:79]\n"
    "ds_read_b64_tr_b16 v[108:109], v8 offset:58880\n"
    "ds_read_b64_tr_b16 v[110:111], v8 offset:62976\n"
    "s_waitcnt lgkmcnt(12)\n"
    "v_mfma_f32_16x16x32_bf16 v[16:19], v[80:83], a[60:63], v[16:19]\n"
    "v_mfma_f32_16x16x32_bf16 v[48:51], v[80:83], a[124:127], v[48:51]\n"
    "v_mfma_f32_16x16x32_bf16 v[20:23], v[84:87], a[60:63], v[20:23]\n"
    "v_mfma_f32_16x16x32_bf16 v[52:55], v[84:87], a[124:127], v[52:55]\n"
    "s_waitcnt lgkmcnt(8)\n"
    "v_mfma_f32_16x16x32_bf16 v[24:27], v[88:91], a[60:63], v[24:27]\n"
    "v_mfma_f32_16x16x32_bf16 v[56:59], v[88:91], a[124:127], v[56:59]\n"
    "v_mfma_f32_16x16x32_bf16 v[28:31], v[92:95], a[60:63], v[28:31]\n"
    "v_mfma_f32_16x16x32_bf16 v[60:63], v[92:95], a[124:127], v[60:63]\n"
    "s_waitcnt lgkmcnt(4)\n"
    "v_mfma_f32_16x16x32_bf16 v[32:35], v[96:99], a[60:63], v[32:35]\n"
    "v_mfma_f32_16x16x32_bf16 v[64:67], v[96:99], a[124:127], v[64:67]\n"
    "v_mfma_f32_16x16x32_bf16 v[36:39], v[100:103], a[60:63], v[36:39]\n"
    "v_mfma_f32_16x16x32_bf16 v[68:71], v[100:103], a[124:127], v[68:71]\n"
    "s_waitcnt lgkmcnt(0)\n"
    "v_mfma_f32_16x16x32_bf16 v[40:43], v[104:107], a[60:63], v[40:43]\n"
    "v_mfma_f32_16x16x32_bf16 v[72:75], v[104:107], a[124:127], v[72:75]\n"
    "v_mfma_f32_16x16x32_bf16 v[44:47], v[108:111], a[60:63], v[44:47]\n"
    "v_mfma_f32_16x16x32_bf16 v[76:79], v[108:111], a[124:127], v[76:79]\n"
    "s_waitcnt vmcnt(0)\n"
    "v_accvgpr_read_b32 v80, a0\n"
    "v_accvgpr_read_b32 v81, a1\n"
    "v_accvgpr_read_b32 v82, a2\n"
    "v_accvgpr_read_b32 v83, a3\n"
    "v_accvgpr_read_b32 v84, a4\n"
    "v_accvgpr_read_b32 v85, a5\n"
    "v_accvgpr_read_b32 v86, a6\n"
    "v_accvgpr_read_b32 v87, a7\n"
    "v_accvgpr_read_b32 v88, a8\n"
    "v_accvgpr_read_b32 v89, a9\n"
    "v_accvgpr_read_b32 v90, a10\n"
    "v_accvgpr_read_b32 v91, a11\n"
    "v_accvgpr_read_b32 v92, a12\n"
    "v_accvgpr_read_b32 v93, a13\n"
    "v_accvgpr_read_b32 v94, a14\n"
    "v_accvgpr_read_b32 v95, a15\n"
    "v_accvgpr_read_b32 v96, a16\n"
    "v_accvgpr_read_b32 v97, a17\n"
    "v_accvgpr_read_b32 v98, a18\n"
    "v_accvgpr_read_b32 v99, a19\n"
    "v_accvgpr_read_b32 v100, a20\n"
    "v_accvgpr_read_b32 v101, a21\n"
    "v_accvgpr_read_b32 v102, a22\n"
    "v_accvgpr_read_b32 v103, a23\n"
    "v_accvgpr_read_b32 v104, a24\n"
    "v_accvgpr_read_b32 v105, a25\n"
    "v_accvgpr_read_b32 v106, a26\n"
    "v_accvgpr_read_b32 v107, a27\n"
    "v_accvgpr_read_b32 v108, a28\n"
    "v_accvgpr_read_b32 v109, a29\n"
    "v_accvgpr_read_b32 v110, a30\n"
    "v_accvgpr_read_b32 v111, a31\n"
    "v_fma_f32 v112, v15, v80, v84\n"
    "v_fma_f32 v16, v16, v13, v112\n"
    "v_fma_f32 v113, v15, v88, v92\n"
    "v_fma_f32 v32, v32, v13, v113\n"
    "v_fma_f32 v114, v15, v81, v85\n"
    "v_fma_f32 v17, v17, v13, v114\n"
    "v_fma_f32 v115, v15, v89, v93\n"
    "v_fma_f32 v33, v33, v13, v115\n"
    "v_fma_f32 v116, v15, v82, v86\n"
    "v_fma_f32 v18, v18, v13, v116\n"
    "v_fma_f32 v117, v15, v90, v94\n"
    "v_fma_f32 v34, v34, v13, v117\n"
    "v_fma_f32 v118, v15, v83, v87\n"
    "v_fma_f32 v19, v19, v13, v118\n"
    "v_fma_f32 v119, v15, v91, v95\n"
    "v_fma_f32 v35, v35, v13, v119\n"
    "v_max_f32 v16, 0, v16\n"
    "v_mul_f32 v16, v16, v32\n"
    "v_max_f32 v17, 0, v17\n"
    "v_mul_f32 v17, v17, v33\n"
    "v_max_f32 v18, 0, v18\n"
    "v_mul_f32 v18, v18, v34\n"
    "v_max_f32 v19, 0, v19\n"
    "v_mul_f32 v19, v19, v35\n"
    "v_cvt_pk_bf16_f32 v120, v16, v17\n"
    "v_cvt_pk_bf16_f32 v121, v18, v19\n"
    "v_fma_f32 v112, v15, v96, v100\n"
    "v_fma_f32 v20, v20, v13, v112\n"
    "v_fma_f32 v113, v15, v104, v108\n"
    "v_fma_f32 v36, v36, v13, v113\n"
    "v_fma_f32 v114, v15, v97, v101\n"
    "v_fma_f32 v21, v21, v13, v114\n"
    "v_fma_f32 v115, v15, v105, v109\n"
    "v_fma_f32 v37, v37, v13, v115\n"
    "v_fma_f32 v116, v15, v98, v102\n"
    "v_fma_f32 v22, v22, v13, v116\n"
    "v_fma_f32 v117, v15, v106, v110\n"
    "v_fma_f32 v38, v38, v13, v117\n"
    "v_fma_f32 v118, v15, v99, v103\n"
    "v_fma_f32 v23, v23, v13, v118\n"
    "v_fma_f32 v119, v15, v107, v111\n"
    "v_fma_f32 v39, v39, v13, v119\n"
    "v_max_f32 v20, 0, v20\n"
    "v_mul_f32 v20, v20, v36\n"
    "v_max_f32 v21, 0, v21\n"
    "v_mul_f32 v21, v21, v37\n"
    "v_max_f32 v22, 0, v22\n"
    "v_mul_f32 v22, v22, v38\n"
    "v_max_f32 v23, 0, v23\n"
    "v_mul_f32 v23, v23, v39\n"
    "v_cvt_pk_bf16_f32 v122, v20, v21\n"
    "v_cvt_pk_bf16_f32 v123, v22, v23\n"
    "s_mov_b32 s54, 0\n"
    "buffer_store_dwordx4 v[120:123], v2, s[44:47], s54 offen offset:0 sc1\n"
    "v_fma_f32 v112, v1, v80, v84\n"
    "v_fma_f32 v48, v48, v14, v112\n"
    "v_fma_f32 v113, v1, v88, v92\n"
    "v_fma_f32 v64, v64, v14, v113\n"
    "v_fma_f32 v114, v1, v81, v85\n"
    "v_fma_f32 v49, v49, v14, v114\n"
    "v_fma_f32 v115, v1, v89, v93\n"
    "v_fma_f32 v65, v65, v14, v115\n"
    "v_fma_f32 v116, v1, v82, v86\n"
    "v_fma_f32 v50, v50, v14, v116\n"
    "v_fma_f32 v117, v1, v90, v94\n"
    "v_fma_f32 v66, v66, v14, v117\n"
    "v_fma_f32 v118, v1, v83, v87\n"
    "v_fma_f32 v51, v51, v14, v118\n"
    "v_fma_f32 v119, v1, v91, v95\n"
    "v_fma_f32 v67, v67, v14, v119\n"
    "v_max_f32 v48, 0, v48\n"
    "v_mul_f32 v48, v48, v64\n"
    "v_max_f32 v49, 0, v49\n"
    "v_mul_f32 v49, v49, v65\n"
    "v_max_f32 v50, 0, v50\n"
    "v_mul_f32 v50, v50, v66\n"
    "v_max_f32 v51, 0, v51\n"
    "v_mul_f32 v51, v51, v67\n"
    "v_cvt_pk_bf16_f32 v124, v48, v49\n"
    "v_cvt_pk_bf16_f32 v125, v50, v51\n"
    "v_fma_f32 v112, v1, v96, v100\n"
    "v_fma_f32 v52, v52, v14, v112\n"
    "v_fma_f32 v113, v1, v104, v108\n"
    "v_fma_f32 v68, v68, v14, v113\n"
    "v_fma_f32 v114, v1, v97, v101\n"
    "v_fma_f32 v53, v53, v14, v114\n"
    "v_fma_f32 v115, v1, v105, v109\n"
    "v_fma_f32 v69, v69, v14, v115\n"
    "v_fma_f32 v116, v1, v98, v102\n"
    "v_fma_f32 v54, v54, v14, v116\n"
    "v_fma_f32 v117, v1, v106, v110\n"
    "v_fma_f32 v70, v70, v14, v117\n"
    "v_fma_f32 v118, v1, v99, v103\n"
    "v_fma_f32 v55, v55, v14, v118\n"
    "v_fma_f32 v119, v1, v107, v111\n"
    "v_fma_f32 v71, v71, v14, v119\n"
    "v_max_f32 v52, 0, v52\n"
    "v_mul_f32 v52, v52, v68\n"
    "v_max_f32 v53, 0, v53\n"
    "v_mul_f32 v53, v53, v69\n"
    "v_max_f32 v54, 0, v54\n"
    "v_mul_f32 v54, v54, v70\n"
    "v_max_f32 v55, 0, v55\n"
    "v_mul_f32 v55, v55, v71\n"
    "v_cvt_pk_bf16_f32 v126, v52, v53\n"
    "v_cvt_pk_bf16_f32 v127, v54, v55\n"
    "s_mov_b32 s54, 57344\n"
    "buffer_store_dwordx4 v[124:127], v2, s[44:47], s54 offen offset:0 sc1\n"
    "v_accvgpr_read_b32 v80, a64\n"
    "v_accvgpr_read_b32 v81, a65\n"
    "v_accvgpr_read_b32 v82, a66\n"
    "v_accvgpr_read_b32 v83, a67\n"
    "v_accvgpr_read_b32 v84, a68\n"
    "v_accvgpr_read_b32 v85, a69\n"
    "v_accvgpr_read_b32 v86, a70\n"
    "v_accvgpr_read_b32 v87, a71\n"
    "v_accvgpr_read_b32 v88, a72\n"
    "v_accvgpr_read_b32 v89, a73\n"
    "v_accvgpr_read_b32 v90, a74\n"
    "v_accvgpr_read_b32 v91, a75\n"
    "v_accvgpr_read_b32 v92, a76\n"
    "v_accvgpr_read_b32 v93, a77\n"
    "v_accvgpr_read_b32 v94, a78\n"
    "v_accvgpr_read_b32 v95, a79\n"
    "v_accvgpr_read_b32 v96, a80\n"
    "v_accvgpr_read_b32 v97, a81\n"
    "v_accvgpr_read_b32 v98, a82\n"
    "v_accvgpr_read_b32 v99, a83\n"
    "v_accvgpr_read_b32 v100, a84\n"
    "v_accvgpr_read_b32 v101, a85\n"
    "v_accvgpr_read_b32 v102, a86\n"
    "v_accvgpr_read_b32 v103, a87\n"
    "v_accvgpr_read_b32 v104, a88\n"
    "v_accvgpr_read_b32 v105, a89\n"
    "v_accvgpr_read_b32 v106, a90\n"
    "v_accvgpr_read_b32 v107, a91\n"
    "v_accvgpr_read_b32 v108, a92\n"
    "v_accvgpr_read_b32 v109, a93\n"
    "v_accvgpr_read_b32 v110, a94\n"
    "v_accvgpr_read_b32 v111, a95\n"
    "v_fma_f32 v112, v15, v80, v84\n"
    "v_fma_f32 v24, v24, v13, v112\n"
    "v_fma_f32 v113, v15, v88, v92\n"
    "v_fma_f32 v40, v40, v13, v113\n"
    "v_fma_f32 v114, v15, v81, v85\n"
    "v_fma_f32 v25, v25, v13, v114\n"
    "v_fma_f32 v115, v15, v89, v93\n"
    "v_fma_f32 v41, v41, v13, v115\n"
    "v_fma_f32 v116, v15, v82, v86\n"
    "v_fma_f32 v26, v26, v13, v116\n"
    "v_fma_f32 v117, v15, v90, v94\n"
    "v_fma_f32 v42, v42, v13, v117\n"
    "v_fma_f32 v118, v15, v83, v87\n"
    "v_fma_f32 v27, v27, v13, v118\n"
    "v_fma_f32 v119, v15, v91, v95\n"
    "v_fma_f32 v43, v43, v13, v119\n"
    "v_max_f32 v24, 0, v24\n"
    "v_mul_f32 v24, v24, v40\n"
    "v_max_f32 v25, 0, v25\n"
    "v_mul_f32 v25, v25, v41\n"
    "v_max_f32 v26, 0, v26\n"
    "v_mul_f32 v26, v26, v42\n"
    "v_max_f32 v27, 0, v27\n"
    "v_mul_f32 v27, v27, v43\n"
    "v_cvt_pk_bf16_f32 v120, v24, v25\n"
    "v_cvt_pk_bf16_f32 v121, v26, v27\n"
    "v_fma_f32 v112, v15, v96, v100\n"
    "v_fma_f32 v28, v28, v13, v112\n"
    "v_fma_f32 v113, v15, v104, v108\n"
    "v_fma_f32 v44, v44, v13, v113\n"
    "v_fma_f32 v114, v15, v97, v101\n"
    "v_fma_f32 v29, v29, v13, v114\n"
    "v_fma_f32 v115, v15, v105, v109\n"
    "v_fma_f32 v45, v45, v13, v115\n"
    "v_fma_f32 v116, v15, v98, v102\n"
    "v_fma_f32 v30, v30, v13, v116\n"
    "v_fma_f32 v117, v15, v106, v110\n"
    "v_fma_f32 v46, v46, v13, v117\n"
    "v_fma_f32 v118, v15, v99, v103\n"
    "v_fma_f32 v31, v31, v13, v118\n"
    "v_fma_f32 v119, v15, v107, v111\n"
    "v_fma_f32 v47, v47, v13, v119\n"
    "v_max_f32 v28, 0, v28\n"
    "v_mul_f32 v28, v28, v44\n"
    "v_max_f32 v29, 0, v29\n"
    "v_mul_f32 v29, v29, v45\n"
    "v_max_f32 v30, 0, v30\n"
    "v_mul_f32 v30, v30, v46\n"
    "v_max_f32 v31, 0, v31\n"
    "v_mul_f32 v31, v31, v47\n"
    "v_cvt_pk_bf16_f32 v122, v28, v29\n"
    "v_cvt_pk_bf16_f32 v123, v30, v31\n"
    "s_mov_b32 s54, 0\n"
    "buffer_store_dwordx4 v[120:123], v2, s[44:47], s54 offen offset:1024 sc1\n"
    "v_fma_f32 v112, v1, v80, v84\n"
    "v_fma_f32 v56, v56, v14, v112\n"
    "v_fma_f32 v113, v1, v88, v92\n"
    "v_fma_f32 v72, v72, v14, v113\n"
    "v_fma_f32 v114, v1, v81, v85\n"
    "v_fma_f32 v57, v57, v14, v114\n"
    "v_fma_f32 v115, v1, v89, v93\n"
    "v_fma_f32 v73, v73, v14, v115\n"
    "v_fma_f32 v116, v1, v82, v86\n"
    "v_fma_f32 v58, v58, v14, v116\n"
    "v_fma_f32 v117, v1, v90, v94\n"
    "v_fma_f32 v74, v74, v14, v117\n"
    "v_fma_f32 v118, v1, v83, v87\n"
    "v_fma_f32 v59, v59, v14, v118\n"
    "v_fma_f32 v119, v1, v91, v95\n"
    "v_fma_f32 v75, v75, v14, v119\n"
    "v_max_f32 v56, 0, v56\n"
    "v_mul_f32 v56, v56, v72\n"
    "v_max_f32 v57, 0, v57\n"
    "v_mul_f32 v57, v57, v73\n"
    "v_max_f32 v58, 0, v58\n"
    "v_mul_f32 v58, v58, v74\n"
    "v_max_f32 v59, 0, v59\n"
    "v_mul_f32 v59, v59, v75\n"
    "v_cvt_pk_bf16_f32 v124, v56, v57\n"
    "v_cvt_pk_bf16_f32 v125, v58, v59\n"
    "v_fma_f32 v112, v1, v96, v100\n"
    "v_fma_f32 v60, v60, v14, v112\n"
    "v_fma_f32 v113, v1, v104, v108\n"
    "v_fma_f32 v76, v76, v14, v113\n"
    "v_fma_f32 v114, v1, v97, v101\n"
    "v_fma_f32 v61, v61, v14, v114\n"
    "v_fma_f32 v115, v1, v105, v109\n"
    "v_fma_f32 v77, v77, v14, v115\n"
    "v_fma_f32 v116, v1, v98, v102\n"
    "v_fma_f32 v62, v62, v14, v116\n"
    "v_fma_f32 v117, v1, v106, v110\n"
    "v_fma_f32 v78, v78, v14, v117\n"
    "v_fma_f32 v118, v1, v99, v103\n"
    "v_fma_f32 v63, v63, v14, v118\n"
    "v_fma_f32 v119, v1, v107, v111\n"
    "v_fma_f32 v79, v79, v14, v119\n"
    "v_max_f32 v60, 0, v60\n"
    "v_mul_f32 v60, v60, v76\n"
    "v_max_f32 v61, 0, v61\n"
    "v_mul_f32 v61, v61, v77\n"
    "v_max_f32 v62, 0, v62\n"
    "v_mul_f32 v62, v62, v78\n"
    "v_max_f32 v63, 0, v63\n"
    "v_mul_f32 v63, v63, v79\n"
    "v_cvt_pk_bf16_f32 v126, v60, v61\n"
    "v_cvt_pk_bf16_f32 v127, v62, v63\n"
    "s_mov_b32 s54, 57344\n"
    "buffer_store_dwordx4 v[124:127], v2, s[44:47], s54 offen offset:1024 sc1\n"
    "s_lshl_b32 s54, s49, 7\n"
    "s_lshl_b32 s55, s50, 2\n"
    "s_add_u32 s54, s54, s55\n"
    "s_add_u32 s54, s54, 667648\n"
    "s_waitcnt vmcnt(0) lgkmcnt(0)\n"
    "s_barrier\n"
    "s_cmp_eq_u32 s48, 0\n"
    "s_cbranch_scc0 L_pb4_%=\n"
    "v_mov_b32 v9, s54\n"
    "v_mov_b32 v10, 1\n"
    "s_mov_b64 exec, 1\n"
    "global_atomic_add v9, v10, s[96:97] sc1\n"
    "s_mov_b64 exec, -1\n"
    "L_pb4_%=:\n"
    "L_p5_%=:\n"
    "v_and_b32 v3, 63, %2\n"
    "v_lshrrev_b32 v8, 6, %2\n"
    "v_lshlrev_b32 v2, 4, v3\n"
    "v_readfirstlane_b32 s52, v8\n"
    "s_and_b32 s64, %1, 7\n"
    "s_lshr_b32 s65, %1, 3\n"
    "s_and_b32 s54, s65, 7\n"
    "s_lshr_b32 s65, s65, 3\n"
    "s_lshl_b32 s64, s64, 2\n"
    "s_add_u32 s53, s64, s65\n"
    "s_lshl_b32 s65, s65, 1\n"
    "s_add_u32 s61, s54, s65\n"
    "s_cmp_ge_u32 s61, 7\n"
    "s_cselect_b32 s65, 7, 0\n"
    "s_sub_u32 s61, s61, s65\n"
    "s_mul_i32 s64, s53, 229376\n"
    "s_mul_i32 s65, s52, 7168\n"
    "s_add_u32 s64, s64, s65\n"
    "s_mov_b64 s[24:25], s[18:19]\n"
    "s_add_u32 s24, s24, s64\n"
    "s_addc_u32 s25, s25, 0\n"
    "s_and_b32 s25, s25, 0xffff\n"
    "s_mov_b32 s26, 229376\n"
    "s_mov_b32 s27, 0x20000\n"
    "s_mul_i32 s64, s52, 229376\n"
    "s_lshl_b32 s65, s54, 7\n"
    "s_add_u32 s64, s64, s65\n"
    "s_sub_u32 s65, 1835008, s64\n"
    "s_mov_b64 s[28:29], s[98:99]\n"
    "s_add_u32 s28, s28, s64\n"
    "s_addc_u32 s29, s29, 0\n"
    "s_and_b32 s29, s29, 0xffff\n"
    "s_mov_b32 s30, s65\n"
    "s_mov_b32 s31, 0x20000\n"
    "v_bfe_u32 v8, v3, 2, 3\n"
    "v_lshlrev_b32 v4, 10, v8\n"
    "v_lshrrev_b32 v8, 5, v3\n"
    "v_lshl_add_u32 v4, v8, 6, v4\n"
    "v_and_b32 v9, 3, v3\n"
    "v_bfe_u32 v8, v3, 4, 1\n"
    "v_lshlrev_b32 v8, 1, v8\n"
    "v_xor_b32 v9, v9, v8\n"
    "v_lshl_add_u32 v4, v9, 4, v4\n"
    "s_mul_i32 s55, s52, 20480\n"
    "s_mov_b32 s60, 0\n"
    "s_mov_b32 s63, s61\n"
    "s_lshl_b32 s60, s61, 15\n"
    "s_add_u32 m0, s55, 0\n"
    "s_add_u32 s57, s60, 0\n"
    "buffer_load_dwordx4 v4, s[28:31], s57 offen lds\n"
    "s_add_u32 m0, s55, 1024\n"
    "s_add_u32 s57, s60, 8192\n"
    "buffer_load_dwordx4 v4, s[28:31], s57 offen lds\n"
    "s_add_u32 m0, s55, 2048\n"
    "s_add_u32 s57, s60, 16384\n"
    "buffer_load_dwordx4 v4, s[28:31], s57 offen lds\n"
    "s_add_u32 m0, s55, 3072\n"
    "s_add_u32 s57, s60, 24576\n"
    "buffer_load_dwordx4 v4, s[28:31], s57 offen lds\n"
    "s_add_u32 s61, s61, 1\n"
    "s_cmp_eq_u32 s61, 7\n"
    "s_cselect_b32 s61, 0, s61\n"
    "s_lshl_b32 s60, s61, 15\n"
    "s_add_u32 m0, s55, 4096\n"
    "s_add_u32 s57, s60, 0\n"
    "buffer_load_dwordx4 v4, s[28:31], s57 offen lds\n"
    "s_add_u32 m0, s55, 5120\n"
    "s_add_u32 s57, s60, 8192\n"
    "buffer_load_dwordx4 v4, s[28:31], s57 offen lds\n"
    "s_add_u32 m0, s55, 6144\n"
    "s_add_u32 s57, s60, 16384\n"
    "buffer_load_dwordx4 v4, s[28:31], s57 offen lds\n"
    "s_add_u32 m0, s55, 7168\n"
    "s_add_u32 s57, s60, 24576\n"
    "buffer_load_dwordx4 v4, s[28:31], s57 offen lds\n"
    "s_add_u32 s61, s61, 1\n"
    "s_cmp_eq_u32 s61, 7\n"
    "s_cselect_b32 s61, 0, s61\n"
    "s_lshl_b32 s60, s61, 15\n"
    "s_add_u32 m0, s55, 8192\n"
    "s_add_u32 s57, s60, 0\n"
    "buffer_load_dwordx4 v4, s[28:31], s57 offen lds\n"
    "s_add_u32 m0, s55, 9216\n"
    "s_add_u32 s57, s60, 8192\n"
    "buffer_load_dwordx4 v4, s[28:31], s57 offen lds\n"
    "s_add_u32 m0, s55, 10240\n"
    "s_add_u32 s57, s60, 16384\n"
    "buffer_load_dwordx4 v4, s[28:31], s57 offen lds\n"
    "s_add_u32 m0, s55, 11264\n"
    "s_add_u32 s57, s60, 24576\n"
    "buffer_load_dwordx4 v4, s[28:31], s57 offen lds\n"
    "s_add_u32 s61, s61, 1\n"
    "s_cmp_eq_u32 s61, 7\n"
    "s_cselect_b32 s61, 0, s61\n"
    "s_lshl_b32 s60, s61, 15\n"
    "s_add_u32 m0, s55, 12288\n"
    "s_add_u32 s57, s60, 0\n"
    "buffer_load_dwordx4 v4, s[28:31], s57 offen lds\n"
    "s_add_u32 m0, s55, 13312\n"
    "s_add_u32 s57, s60, 8192\n"
    "buffer_load_dwordx4 v4, s[28:31], s57 offen lds\n"
    "s_add_u32 m0, s55, 14336\n"
    "s_add_u32 s57, s60, 16384\n"
    "buffer_load_dwordx4 v4, s[28:31], s57 offen lds\n"
    "s_add_u32 m0, s55, 15360\n"
    "s_add_u32 s57, s60, 24576\n"
    "buffer_load_dwordx4 v4, s[28:31], s57 offen lds\n"
    "s_add_u32 s61, s61, 1\n"
    "s_cmp_eq_u32 s61, 7\n"
    "s_cselect_b32 s61, 0, s61\n"
    "s_lshl_b32 s60, s61, 15\n"
    "s_add_u32 m0, s55, 16384\n"
    "s_add_u32 s57, s60, 0\n"
    "buffer_load_dwordx4 v4, s[28:31], s57 offen lds\n"
    "s_add_u32 m0, s55, 17408\n"
    "s_add_u32 s57, s60, 8192\n"
    "buffer_load_dwordx4 v4, s[28:31], s57 offen lds\n"
    "s_add_u32 m0, s55, 18432\n"
    "s_add_u32 s57, s60, 16384\n"
    "buffer_load_dwordx4 v4, s[28:31], s57 offen lds\n"
    "s_add_u32 m0, s55, 19456\n"
    "s_add_u32 s57, s60, 24576\n"
    "buffer_load_dwordx4 v4, s[28:31], s57 offen lds\n"
    "s_add_u32 s61, s61, 1\n"
    "s_cmp_eq_u32 s61, 7\n"
    "s_cselect_b32 s61, 0, s61\n"
    "v_lshrrev_b32 v8, 5, v3\n"
    "v_lshlrev_b32 v5, 10, v8\n"
    "v_bfe_u32 v9, v3, 2, 3\n"
    "v_lshl_add_u32 v5, v9, 6, v5\n"
    "v_and_b32 v9, 3, v3\n"
    "v_lshl_add_u32 v5, v9, 3, v5\n"
    "v_bfe_u32 v9, v3, 4, 1\n"
    "v_lshl_add_u32 v5, v9, 5, v5\n"
    "v_add_u32 v5, s55, v5\n"
    "v_xor_b32 v6, 32, v5\n"
    "s_mov_b32 s62, 0xffff0000\n"
    "s_lshr_b32 s64, s52, 1\n"
    "s_lshl_b32 s66, s53, 2\n"
    "s_add_u32 s66, s66, s64\n"
    "s_and_b32 s64, s52, 1\n"
    "s_lshl_b32 s67, s54, 1\n"
    "s_add_u32 s67, s67, s64\n"
    "v_and_b32 v7, 15, v3\n"
    "v_lshrrev_b32 v8, 4, v3\n"
    "v_lshlrev_b32 v11, 10, v7\n"
    "v_lshl_add_u32 v11, v8, 3, v11\n"
    "v_lshlrev_b32 v12, 4, v8\n"
    "v_lshlrev_b32 v13, 5, v3\n"
    "s_lshl_b32 s64, s67, 7\n"
    "s_mov_b64 s[36:37], s[96:97]\n"
    "s_add_u32 s36, s36, s64\n"
    "s_addc_u32 s37, s37, 0\n"
    "s_and_b32 s37, s37, 0xffff\n"
    "s_mov_b32 s38, 12288\n"
    "s_mov_b32 s39, 0x20000\n"
    "s_lshl_b32 s64, s66, 14\n"
    "s_lshl_b32 s65, s67, 6\n"
    "s_add_u32 s64, s64, s65\n"
    "s_mov_b64 s[40:41], s[20:21]\n"
    "s_add_u32 s40, s40, s64\n"
    "s_addc_u32 s41, s41, 0\n"
    "s_and_b32 s41, s41, 0xffff\n"
    "s_mov_b32 s42, 16384\n"
    "s_mov_b32 s43, 0x20000\n"
    "s_lshl_b32 s64, s66, 4\n"
    "s_add_u32 s64, s64, s67\n"
    "s_lshl_b32 s64, s64, 10\n"
    "s_lshl_b32 s64, s64, 1\n"
    "s_mov_b64 s[32:33], s[84:85]\n"
    "s_add_u32 s32, s32, s64\n"
    "s_addc_u32 s33, s33, 0\n"
    "s_and_b32 s33, s33, 0xffff\n"
    "s_mov_b32 s34, 2048\n"
    "s_mov_b32 s35, 0x20000\n"
    "s_lshr_b32 s64, s53, 2\n"
    "s_lshl_b32 s64, s64, 7\n"
    "s_mul_i32 s65, s52, 7\n"
    "s_lshr_b32 s65, s65, 1\n"
    "s_lshl_b32 s65, s65, 2\n"
    "s_add_u32 s64, s64, s65\n"
    "s_add_u32 s64, s64, 667648\n"
    "v_and_b32 v14, 3, v3\n"
    "v_lshlrev_b32 v14, 2, v14\n"
    "v_add_u32 v14, s64, v14\n"
    "s_mov_b32 s66, 200000\n"
    "L_wpa5_%=:\n"
    "global_load_dword v15, v14, s[96:97] offset:0 sc1\n"
    "s_waitcnt vmcnt(0)\n"
    "v_cmp_ne_u32 vcc, 1, v15\n"
    "s_sub_u32 s66, s66, 1\n"
    "s_cbranch_vccz L_wda5_%=\n"
    "s_sleep 2\n"
    "s_cmp_lg_u32 s66, 0\n"
    "s_cbranch_scc1 L_wpa5_%=\n"
    "L_wda5_%=:\n"
    "s_lshl_b32 s56, s63, 10\n"
    "s_add_u32 s58, s56, 0\n"
    "buffer_load_dwordx4 a[64:67], v2, s[24:27], s58 offen sc1\n"
    "s_add_u32 s58, s56, 57344\n"
    "buffer_load_dwordx4 a[68:71], v2, s[24:27], s58 offen sc1\n"
    "s_add_u32 s58, s56, 114688\n"
    "buffer_load_dwordx4 a[72:75], v2, s[24:27], s58 offen sc1\n"
    "s_add_u32 s58, s56, 172032\n"
    "buffer_load_dwordx4 a[76:79], v2, s[24:27], s58 offen sc1\n"
    "s_add_u32 s63, s63, 1\n"
    "s_cmp_eq_u32 s63, 7\n"
    "s_cselect_b32 s63, 0, s63\n"
    "s_lshl_b32 s56, s63, 10\n"
    "s_add_u32 s58, s56, 0\n"
    "buffer_load_dwordx4 a[80:83], v2, s[24:27], s58 offen sc1\n"
    "s_add_u32 s58, s56, 57344\n"
    "buffer_load_dwordx4 a[84:87], v2, s[24:27], s58 offen sc1\n"
    "s_add_u32 s58, s56, 114688\n"
    "buffer_load_dwordx4 a[88:91], v2, s[24:27], s58 offen sc1\n"
    "s_add_u32 s58, s56, 172032\n"
    "buffer_load_dwordx4 a[92:95], v2, s[24:27], s58 offen sc1\n"
    "s_add_u32 s63, s63, 1\n"
    "s_cmp_eq_u32 s63, 7\n"
    "s_cselect_b32 s63, 0, s63\n"
    "s_lshl_b32 s56, s63, 10\n"
    "s_add_u32 s58, s56, 0\n"
    "buffer_load_dwordx4 a[96:99], v2, s[24:27], s58 offen sc1\n"
    "s_add_u32 s58, s56, 57344\n"
    "buffer_load_dwordx4 a[100:103], v2, s[24:27], s58 offen sc1\n"
    "s_add_u32 s58, s56, 114688\n"
    "buffer_load_dwordx4 a[104:107], v2, s[24:27], s58 offen sc1\n"
    "s_add_u32 s58, s56, 172032\n"
    "buffer_load_dwordx4 a[108:111], v2, s[24:27], s58 offen sc1\n"
    "s_add_u32 s63, s63, 1\n"
    "s_cmp_eq_u32 s63, 7\n"
    "s_cselect_b32 s63, 0, s63\n"
    "s_lshl_b32 s56, s63, 10\n"
    "s_add_u32 s58, s56, 0\n"
    "buffer_load_dwordx4 a[112:115], v2, s[24:27], s58 offen sc1\n"
    "s_add_u32 s58, s56, 57344\n"
    "buffer_load_dwordx4 a[116:119], v2, s[24:27], s58 offen sc1\n"
    "s_add_u32 s58, s56, 114688\n"
    "buffer_load_dwordx4 a[120:123], v2, s[24:27], s58 offen sc1\n"
    "s_add_u32 s58, s56, 172032\n"
    "buffer_load_dwordx4 a[124:127], v2, s[24:27], s58 offen sc1\n"
    "s_add_u32 s63, s63, 1\n"
    "s_cmp_eq_u32 s63, 7\n"
    "s_cselect_b32 s63, 0, s63\n"
    "s_waitcnt vmcnt(12)\n"
    "ds_read_b64_tr_b16 v[16:17], v5 offset:0\n"
    "ds_read_b64_tr_b16 v[18:19], v5 offset:2048\n"
    "ds_read_b64_tr_b16 v[20:21], v6 offset:0\n"
    "ds_read_b64_tr_b16 v[22:23], v6 offset:2048\n"
    "ds_read_b64_tr_b16 v[24:25], v5 offset:512\n"
    "ds_read_b64_tr_b16 v[26:27], v5 offset:2560\n"
    "ds_read_b64_tr_b16 v[28:29], v6 offset:512\n"
    "ds_read_b64_tr_b16 v[30:31], v6 offset:2560\n"
    "s_waitcnt lgkmcnt(0)\n"
    "v_mfma_f32_16x16x32_bf16 a[0:3], v[16:19], a[64:67], 0\n"
    "s_lshl_b32 s60, s61, 15\n"
    "s_add_u32 m0, s55, 0\n"
    "s_add_u32 s57, s60, 0\n"
    "buffer_load_dwordx4 v4, s[28:31], s57 offen lds\n"
    "v_mfma_f32_16x16x32_bf16 a[4:7], v[20:23], a[64:67], 0\n"
    "s_waitcnt vmcnt(9)\n"
    "ds_read_b64_tr_b16 v[32:33], v5 offset:4096\n"
    "ds_read_b64_tr_b16 v[34:35], v5 offset:6144\n"
    "v_mfma_f32_16x16x32_bf16 a[8:11], v[24:27], a[64:67], 0\n"
    "ds_read_b64_tr_b16 v[36:37], v6 offset:4096\n"
    "ds_read_b64_tr_b16 v[38:39], v6 offset:6144\n"
    "v_mfma_f32_16x16x32_bf16 a[12:15], v[28:31], a[64:67], 0\n"
    "s_lshl_b32 s56, s63, 10\n"
    "s_add_u32 s58, s56, 0\n"
    "buffer_load_dwordx4 a[64:67], v2, s[24:27], s58 offen sc1\n"
    "v_mfma_f32_16x16x32_bf16 a[16:19], v[16:19], a[68:71], 0\n"
    "s_add_u32 m0, s55, 1024\n"
    "s_add_u32 s57, s60, 8192\n"
    "buffer_load_dwordx4 v4, s[28:31], s57 offen lds\n"
    "v_mfma_f32_16x16x32_bf16 a[20:23], v[20:23], a[68:71], 0\n"
    "ds_read_b64_tr_b16 v[40:41], v5 offset:4608\n"
    "ds_read_b64_tr_b16 v[42:43], v5 offset:6656\n"
    "v_mfma_f32_16x16x32_bf16 a[24:27], v[24:27], a[68:71], 0\n"
    "ds_read_b64_tr_b16 v[44:45], v6 offset:4608\n"
    "ds_read_b64_tr_b16 v[46:47], v6 offset:6656\n"
    "v_mfma_f32_16x16x32_bf16 a[28:31], v[28:31], a[68:71], 0\n"
    "s_add_u32 s58, s56, 57344\n"
    "buffer_load_dwordx4 a[68:71], v2, s[24:27], s58 offen sc1\n"
    "v_mfma_f32_16x16x32_bf16 a[32:35], v[16:19], a[72:75], 0\n"
    "s_add_u32 m0, s55, 2048\n"
    "s_add_u32 s57, s60, 16384\n"
    "buffer_load_dwordx4 v4, s[28:31], s57 offen lds\n"
    "v_mfma_f32_16x16x32_bf16 a[36:39], v[20:23], a[72:75], 0\n"
    "v_mfma_f32_16x16x32_bf16 a[40:43], v[24:27], a[72:75], 0\n"
    "v_mfma_f32_16x16x32_bf16 a[44:47], v[28:31], a[72:75], 0\n"
    "s_add_u32 s58, s56, 114688\n"
    "buffer_load_dwordx4 a[72:75], v2, s[24:27], s58 offen sc1\n"
    "v_mfma_f32_16x16x32_bf16 a[48:51], v[16:19], a[76:79], 0\n"
    "s_add_u32 m0, s55, 3072\n"
    "s_add_u32 s57, s60, 24576\n"
    "buffer_load_dwordx4 v4, s[28:31], s57 offen lds\n"
    "s_add_u32 s61, s61, 1\n"
    "s_cmp_eq_u32 s61, 7\n"
    "s_cselect_b32 s61, 0, s61\n"
    "v_mfma_f32_16x16x32_bf16 a[52:55], v[20:23], a[76:79], 0\n"
    "v_mfma_f32_16x16x32_bf16 a[56:59], v[24:27], a[76:79], 0\n"
    "v_mfma_f32_16x16x32_bf16 a[60:63], v[28:31], a[76:79], 0\n"
    "s_add_u32 s58, s56, 172032\n"
    "buffer_load_dwordx4 a[76:79], v2, s[24:27], s58 offen sc1\n"
    "s_add_u32 s63, s63, 1\n"
    "s_cmp_eq_u32 s63, 7\n"
    "s_cselect_b32 s63, 0, s63\n"
    "s_waitcnt lgkmcnt(0)\n"
    "v_mfma_f32_16x16x32_bf16 a[0:3], v[32:35], a[80:83], a[0:3]\n"
    "s_lshl_b32 s60, s61, 15\n"
    "s_add_u32 m0, s55, 4096\n"
    "s_add_u32 s57, s60, 0\n"
    "buffer_load_dwordx4 v4, s[28:31], s57 offen lds\n"
    "v_mfma_f32_16x16x32_bf16 a[4:7], v[36:39], a[80:83], a[4:7]\n"
    "s_waitcnt vmcnt(13)\n"
    "ds_read_b64_tr_b16 v[16:17], v5 offset:8192\n"
    "ds_read_b64_tr_b16 v[18:19], v5 offset:10240\n"
    "v_mfma_f32_16x16x32_bf16 a[8:11], v[40:43], a[80:83], a[8:11]\n"
    "ds_read_b64_tr_b16 v[20:21], v6 offset:8192\n"
    "ds_read_b64_tr_b16 v[22:23], v6 offset:10240\n"
    "v_mfma_f32_16x16x32_bf16 a[12:15], v[44:47], a[80:83], a[12:15]\n"
    "s_lshl_b32 s56, s63, 10\n"
    "s_add_u32 s58, s56, 0\n"
    "buffer_load_dwordx4 a[80:83], v2, s[24:27], s58 offen sc1\n"
    "v_mfma_f32_16x16x32_bf16 a[16:19], v[32:35], a[84:87], a[16:19]\n"
    "s_add_u32 m0, s55, 5120\n"
    "s_add_u32 s57, s60, 8192\n"
    "buffer_load_dwordx4 v4, s[28:31], s57 offen lds\n"
    "v_mfma_f32_16x16x32_bf16 a[20:23], v[36:39], a[84:87], a[20:23]\n"
    "ds_read_b64_tr_b16 v[24:25], v5 offset:8704\n"
    "ds_read_b64_tr_b16 v[26:27], v5 offset:10752\n"
    "v_mfma_f32_16x16x32_bf16 a[24:27], v[40:43], a[84:87], a[24:27]\n"
    "ds_read_b64_tr_b16 v[28:29], v6 offset:8704\n"
    "ds_read_b64_tr_b16 v[30:31], v6 offset:10752\n"
    "v_mfma_f32_16x16x32_bf16 a[28:31], v[44:47], a[84:87], a[28:31]\n"
    "s_add_u32 s58, s56, 57344\n"
    "buffer_load_dwordx4 a[84:87], v2, s[24:27], s58 offen sc1\n"
    "v_mfma_f32_16x16x32_bf16 a[32:35], v[32:35], a[88:91], a[32:35]\n"
    "s_add_u32 m0, s55, 6144\n"
    "s_add_u32 s57, s60, 16384\n"
    "buffer_load_dwordx4 v4, s[28:31], s57 offen lds\n"
    "v_mfma_f32_16x16x32_bf16 a[36:39], v[36:39], a[88:91], a[36:39]\n"
    "v_mfma_f32_16x16x32_bf16 a[40:43], v[40:43], a[88:91], a[40:43]\n"
    "v_mfma_f32_16x16x32_bf16 a[44:47], v[44:47], a[88:91], a[44:47]\n"
    "s_add_u32 s58, s56, 114688\n"
    "buffer_load_dwordx4 a[88:91], v2, s[24:27], s58 offen sc1\n"
    "v_mfma_f32_16x16x32_bf16 a[48:51], v[32:35], a[92:95], a[48:51]\n"
    "s_add_u32 m0, s55, 7168\n"
    "s_add_u32 s57, s60, 24576\n"
    "buffer_load_dwordx4 v4, s[28:31], s57 offen lds\n"
    "s_add_u32 s61, s61, 1\n"
    "s_cmp_eq_u32 s61, 7\n"
    "s_cselect_b32 s61, 0, s61\n"
    "v_mfma_f32_16x16x32_bf16 a[52:55], v[36:39], a[92:95], a[52:55]\n"
    "v_mfma_f32_16x16x32_bf16 a[56:59], v[40:43], a[92:95], a[56:59]\n"
    "v_mfma_f32_16x16x32_bf16 a[60:63], v[44:47], a[92:95], a[60:63]\n"
    "s_add_u32 s58, s56, 172032\n"
    "buffer_load_dwordx4 a[92:95], v2, s[24:27], s58 offen sc1\n"
    "s_add_u32 s63, s63, 1\n"
    "s_cmp_eq_u32 s63, 7\n"
    "s_cselect_b32 s63, 0, s63\n"
    "s_waitcnt lgkmcnt(0)\n"
    "v_mfma_f32_16x16x32_bf16 a[0:3], v[16:19], a[96:99], a[0:3]\n"
    "s_waitcnt vmcnt(16)\n"
    "ds_read_b64_tr_b16 v[32:33], v5 offset:12288\n"
    "ds_read_b64_tr_b16 v[34:35], v5 offset:14336\n"
    "v_mfma_f32_16x16x32_bf16 a[4:7], v[20:23], a[96:99], a[4:7]\n"
    "ds_read_b64_tr_b16 v[36:37], v6 offset:12288\n"
    "ds_read_b64_tr_b16 v[38:39], v6 offset:14336\n"
    "v_mfma_f32_16x16x32_bf16 a[8:11], v[24:27], a[96:99], a[8:11]\n"
    "ds_read_b64_tr_b16 v[40:41], v5 offset:12800\n"
    "ds_read_b64_tr_b16 v[42:43], v5 offset:14848\n"
    "v_mfma_f32_16x16x32_bf16 a[12:15], v[28:31], a[96:99], a[12:15]\n"
    "s_lshl_b32 s56, s63, 10\n"
    "s_add_u32 s58, s56, 0\n"
    "buffer_load_dwordx4 a[96:99], v2, s[24:27], s58 offen sc1\n"
    "v_mfma_f32_16x16x32_bf16 a[16:19], v[16:19], a[100:103], a[16:19]\n"
    "ds_read_b64_tr_b16 v[44:45], v6 offset:12800\n"
    "ds_read_b64_tr_b16 v[46:47], v6 offset:14848\n"
    "v_mfma_f32_16x16x32_bf16 a[20:23], v[20:23], a[100:103], a[20:23]\n"
    "v_mfma_f32_16x16x32_bf16 a[24:27], v[24:27], a[100:103], a[24:27]\n"
    "v_mfma_f32_16x16x32_bf16 a[28:31], v[28:31], a[100:103], a[28:31]\n"
    "s_add_u32 s58, s56, 57344\n"
    "buffer_load_dwordx4 a[100:103], v2, s[24:27], s58 offen sc1\n"
    "v_mfma_f32_16x16x32_bf16 a[32:35], v[16:19], a[104:107], a[32:35]\n"
    "v_mfma_f32_16x16x32_bf16 a[36:39], v[20:23], a[104:107], a[36:39]\n"
    "v_mfma_f32_16x16x32_bf16 a[40:43], v[24:27], a[104:107], a[40:43]\n"
    "v_mfma_f32_16x16x32_bf16 a[44:47], v[28:31], a[104:107], a[44:47]\n"
    "s_add_u32 s58, s56, 114688\n"
    "buffer_load_dwordx4 a[104:107], v2, s[24:27], s58 offen sc1\n"
    "v_mfma_f32_16x16x32_bf16 a[48:51], v[16:19], a[108:111], a[48:51]\n"
    "v_mfma_f32_16x16x32_bf16 a[52:55], v[20:23], a[108:111], a[52:55]\n"
    "v_mfma_f32_16x16x32_bf16 a[56:59], v[24:27], a[108:111], a[56:59]\n"
    "v_mfma_f32_16x16x32_bf16 a[60:63], v[28:31], a[108:111], a[60:63]\n"
    "s_add_u32 s58, s56, 172032\n"
    "buffer_load_dwordx4 a[108:111], v2, s[24:27], s58 offen sc1\n"
    "s_add_u32 s63, s63, 1\n"
    "s_cmp_eq_u32 s63, 7\n"
    "s_cselect_b32 s63, 0, s63\n"
    "s_waitcnt lgkmcnt(0)\n"
    "v_mfma_f32_16x16x32_bf16 a[0:3], v[32:35], a[112:115], a[0:3]\n"
    "s_waitcnt vmcnt(12)\n"
    "ds_read_b64_tr_b16 v[16:17], v5 offset:16384\n"
    "ds_read_b64_tr_b16 v[18:19], v5 offset:18432\n"
    "v_mfma_f32_16x16x32_bf16 a[4:7], v[36:39], a[112:115], a[4:7]\n"
    "ds_read_b64_tr_b16 v[20:21], v6 offset:16384\n"
    "ds_read_b64_tr_b16 v[22:23], v6 offset:18432\n"
    "v_mfma_f32_16x16x32_bf16 a[8:11], v[40:43], a[112:115], a[8:11]\n"
    "ds_read_b64_tr_b16 v[24:25], v5 offset:16896\n"
    "ds_read_b64_tr_b16 v[26:27], v5 offset:18944\n"
    "v_mfma_f32_16x16x32_bf16 a[12:15], v[44:47], a[112:115], a[12:15]\n"
    "ds_read_b64_tr_b16 v[28:29], v6 offset:16896\n"
    "ds_read_b64_tr_b16 v[30:31], v6 offset:18944\n"
    "v_mfma_f32_16x16x32_bf16 a[16:19], v[32:35], a[116:119], a[16:19]\n"
    "v_mfma_f32_16x16x32_bf16 a[20:23], v[36:39], a[116:119], a[20:23]\n"
    "v_mfma_f32_16x16x32_bf16 a[24:27], v[40:43], a[116:119], a[24:27]\n"
    "v_mfma_f32_16x16x32_bf16 a[28:31], v[44:47], a[116:119], a[28:31]\n"
    "v_mfma_f32_16x16x32_bf16 a[32:35], v[32:35], a[120:123], a[32:35]\n"
    "v_mfma_f32_16x16x32_bf16 a[36:39], v[36:39], a[120:123], a[36:39]\n"
    "v_mfma_f32_16x16x32_bf16 a[40:43], v[40:43], a[120:123], a[40:43]\n"
    "v_mfma_f32_16x16x32_bf16 a[44:47], v[44:47], a[120:123], a[44:47]\n"
    "v_mfma_f32_16x16x32_bf16 a[48:51], v[32:35], a[124:127], a[48:51]\n"
    "v_mfma_f32_16x16x32_bf16 a[52:55], v[36:39], a[124:127], a[52:55]\n"
    "v_mfma_f32_16x16x32_bf16 a[56:59], v[40:43], a[124:127], a[56:59]\n"
    "v_mfma_f32_16x16x32_bf16 a[60:63], v[44:47], a[124:127], a[60:63]\n"
    "buffer_load_dwordx4 v[92:95], v2, s[32:35], 0 offen offset:0 sc1\n"
    "buffer_load_dwordx4 v[96:99], v2, s[32:35], 0 offen offset:1024 sc1\n"
    "s_mov_b32 s64, 10240\n"
    "buffer_load_dwordx4 v[84:87], v12, s[36:39], s64 offen offset:0 sc1\n"
    "buffer_load_dwordx4 v[88:91], v12, s[36:39], s64 offen offset:64 sc1\n"
    "s_waitcnt lgkmcnt(0)\n"
    "v_mfma_f32_16x16x32_bf16 a[0:3], v[16:19], a[64:67], a[0:3]\n"
    "s_waitcnt vmcnt(8)\n"
    "ds_read_b64_tr_b16 v[32:33], v5 offset:0\n"
    "ds_read_b64_tr_b16 v[34:35], v5 offset:2048\n"
    "v_mfma_f32_16x16x32_bf16 a[4:7], v[20:23], a[64:67], a[4:7]\n"
    "ds_read_b64_tr_b16 v[36:37], v6 offset:0\n"
    "ds_read_b64_tr_b16 v[38:39], v6 offset:2048\n"
    "v_mfma_f32_16x16x32_bf16 a[8:11], v[24:27], a[64:67], a[8:11]\n"
    "ds_read_b64_tr_b16 v[40:41], v5 offset:512\n"
    "ds_read_b64_tr_b16 v[42:43], v5 offset:2560\n"
    "v_mfma_f32_16x16x32_bf16 a[12:15], v[28:31], a[64:67], a[12:15]\n"
    "ds_read_b64_tr_b16 v[44:45], v6 offset:512\n"
    "ds_read_b64_tr_b16 v[46:47], v6 offset:2560\n"
    "v_mfma_f32_16x16x32_bf16 a[16:19], v[16:19], a[68:71], a[16:19]\n"
    "v_mfma_f32_16x16x32_bf16 a[20:23], v[20:23], a[68:71], a[20:23]\n"
    "v_mfma_f32_16x16x32_bf16 a[24:27], v[24:27], a[68:71], a[24:27]\n"
    "v_mfma_f32_16x16x32_bf16 a[28:31], v[28:31], a[68:71], a[28:31]\n"
    "v_mfma_f32_16x16x32_bf16 a[32:35], v[16:19], a[72:75], a[32:35]\n"
    "v_mfma_f32_16x16x32_bf16 a[36:39], v[20:23], a[72:75], a[36:39]\n"
    "v_mfma_f32_16x16x32_bf16 a[40:43], v[24:27], a[72:75], a[40:43]\n"
    "v_mfma_f32_16x16x32_bf16 a[44:47], v[28:31], a[72:75], a[44:47]\n"
    "v_mfma_f32_16x16x32_bf16 a[48:51], v[16:19], a[76:79], a[48:51]\n"
    "v_mfma_f32_16x16x32_bf16 a[52:55], v[20:23], a[76:79], a[52:55]\n"
    "v_mfma_f32_16x16x32_bf16 a[56:59], v[24:27], a[76:79], a[56:59]\n"
    "v_mfma_f32_16x16x32_bf16 a[60:63], v[28:31], a[76:79], a[60:63]\n"
    "s_waitcnt lgkmcnt(0)\n"
    "v_mfma_f32_16x16x32_bf16 a[0:3], v[32:35], a[80:83], a[0:3]\n"
    "s_waitcnt vmcnt(4)\n"
    "ds_read_b64_tr_b16 v[16:17], v5 offset:4096\n"
    "ds_read_b64_tr_b16 v[18:19], v5 offset:6144\n"
    "v_mfma_f32_16x16x32_bf16 a[4:7], v[36:39], a[80:83], a[4:7]\n"
    "ds_read_b64_tr_b16 v[20:21], v6 offset:4096\n"
    "ds_read_b64_tr_b16 v[22:23], v6 offset:6144\n"
    "v_mfma_f32_16x16x32_bf16 a[8:11], v[40:43], a[80:83], a[8:11]\n"
    "ds_read_b64_tr_b16 v[24:25], v5 offset:4608\n"
    "ds_read_b64_tr_b16 v[26:27], v5 offset:6656\n"
    "v_mfma_f32_16x16x32_bf16 a[12:15], v[44:47], a[80:83], a[12:15]\n"
    "ds_read_b64_tr_b16 v[28:29], v6 offset:4608\n"
    "ds_read_b64_tr_b16 v[30:31], v6 offset:6656\n"
    "v_mfma_f32_16x16x32_bf16 a[16:19], v[32:35], a[84:87], a[16:19]\n"
    "v_mfma_f32_16x16x32_bf16 a[20:23], v[36:39], a[84:87], a[20:23]\n"
    "v_mfma_f32_16x16x32_bf16 a[24:27], v[40:43], a[84:87], a[24:27]\n"
    "v_mfma_f32_16x16x32_bf16 a[28:31], v[44:47], a[84:87], a[28:31]\n"
    "v_mfma_f32_16x16x32_bf16 a[32:35], v[32:35], a[88:91], a[32:35]\n"
    "v_mfma_f32_16x16x32_bf16 a[36:39], v[36:39], a[88:91], a[36:39]\n"
    "v_mfma_f32_16x16x32_bf16 a[40:43], v[40:43], a[88:91], a[40:43]\n"
    "v_mfma_f32_16x16x32_bf16 a[44:47], v[44:47], a[88:91], a[44:47]\n"
    "v_mfma_f32_16x16x32_bf16 a[48:51], v[32:35], a[92:95], a[48:51]\n"
    "v_mfma_f32_16x16x32_bf16 a[52:55], v[36:39], a[92:95], a[52:55]\n"
    "v_mfma_f32_16x16x32_bf16 a[56:59], v[40:43], a[92:95], a[56:59]\n"
    "v_mfma_f32_16x16x32_bf16 a[60:63], v[44:47], a[92:95], a[60:63]\n"
    "s_waitcnt lgkmcnt(0)\n"
    "v_mfma_f32_16x16x32_bf16 a[0:3], v[16:19], a[96:99], a[0:3]\n"
    "v_mfma_f32_16x16x32_bf16 a[4:7], v[20:23], a[96:99], a[4:7]\n"
    "v_mfma_f32_16x16x32_bf16 a[8:11], v[24:27], a[96:99], a[8:11]\n"
    "v_mfma_f32_16x16x32_bf16 a[12:15], v[28:31], a[96:99], a[12:15]\n"
    "v_mfma_f32_16x16x32_bf16 a[16:19], v[16:19], a[100:103], a[16:19]\n"
    "v_mfma_f32_16x16x32_bf16 a[20:23], v[20:23], a[100:103], a[20:23]\n"
    "v_mfma_f32_16x16x32_bf16 a[24:27], v[24:27], a[100:103], a[24:27]\n"
    "v_mfma_f32_16x16x32_bf16 a[28:31], v[28:31], a[100:103], a[28:31]\n"
    "v_mfma_f32_16x16x32_bf16 a[32:35], v[16:19], a[104:107], a[32:35]\n"
    "v_mfma_f32_16x16x32_bf16 a[36:39], v[20:23], a[104:107], a[36:39]\n"
    "v_mfma_f32_16x16x32_bf16 a[40:43], v[24:27], a[104:107], a[40:43]\n"
    "v_mfma_f32_16x16x32_bf16 a[44:47], v[28:31], a[104:107], a[44:47]\n"
    "v_mfma_f32_16x16x32_bf16 a[48:51], v[16:19], a[108:111], a[48:51]\n"
    "v_mfma_f32_16x16x32_bf16 a[52:55], v[20:23], a[108:111], a[52:55]\n"
    "v_mfma_f32_16x16x32_bf16 a[56:59], v[24:27], a[108:111], a[56:59]\n"
    "v_mfma_f32_16x16x32_bf16 a[60:63], v[28:31], a[108:111], a[60:63]\n"
    "s_lshl_b32 s65, s52, 14\n"
    "v_add_u32 v10, s65, v2\n"
    "s_lshl_b32 s64, s52, 11\n"
    "v_add_u32 v7, s64, v2\n"
    "v_add_u32 v8, 0x10000, v7\n"
    "s_nop 4\n"
    "s_barrier\n"
    "ds_write_b128 v10, a[0:3] offset:0\n"
    "ds_write_b128 v10, a[4:7] offset:1024\n"
    "ds_write_b128 v10, a[8:11] offset:2048\n"
    "ds_write_b128 v10, a[12:15] offset:3072\n"
    "ds_write_b128 v10, a[16:19] offset:4096\n"
    "ds_write_b128 v10, a[20:23] offset:5120\n"
    "ds_write_b128 v10, a[24:27] offset:6144\n"
    "ds_write_b128 v10, a[28:31] offset:7168\n"
    "ds_write_b128 v10, a[32:35] offset:8192\n"
    "ds_write_b128 v10, a[36:39] offset:9216\n"
    "ds_write_b128 v10, a[40:43] offset:10240\n"
    "ds_write_b128 v10, a[44:47] offset:11264\n"
    "ds_write_b128 v10, a[48:51] offset:12288\n"
    "ds_write_b128 v10, a[52:55] offset:13312\n"
    "ds_write_b128 v10, a[56:59] offset:14336\n"
    "ds_write_b128 v10, a[60:63] offset:15360\n"
    "s_waitcnt lgkmcnt(0)\n"
    "s_barrier\n"
    "ds_read_b128 v[16:19], v7 offset:0\n"
    "ds_read_b128 v[20:23], v7 offset:1024\n"
    "ds_read_b128 v[24:27], v7 offset:16384\n"
    "ds_read_b128 v[28:31], v7 offset:17408\n"
    "ds_read_b128 v[32:35], v7 offset:32768\n"
    "ds_read_b128 v[36:39], v7 offset:33792\n"
    "ds_read_b128 v[40:43], v7 offset:49152\n"
    "ds_read_b128 v[44:47], v7 offset:50176\n"
    "ds_read_b128 v[48:51], v8 offset:0\n"
    "ds_read_b128 v[52:55], v8 offset:1024\n"
    "ds_read_b128 v[56:59], v8 offset:16384\n"
    "ds_read_b128 v[60:63], v8 offset:17408\n"
    "ds_read_b128 v[64:67], v8 offset:32768\n"
    "ds_read_b128 v[68:71], v8 offset:33792\n"
    "ds_read_b128 v[72:75], v8 offset:49152\n"
    "ds_read_b128 v[76:79], v8 offset:50176\n"
    "s_waitcnt lgkmcnt(12)\n"
    "v_add_f32 v16, v16, v24\n"
    "v_add_f32 v17, v17, v25\n"
    "v_add_f32 v18, v18, v26\n"
    "v_add_f32 v19, v19, v27\n"
    "v_add_f32 v20, v20, v28\n"
    "v_add_f32 v21, v21, v29\n"
    "v_add_f32 v22, v22, v30\n"
    "v_add_f32 v23, v23, v31\n"
    "s_waitcnt lgkmcnt(10)\n"
    "v_add_f32 v16, v16, v32\n"
    "v_add_f32 v17, v17, v33\n"
    "v_add_f32 v18, v18, v34\n"
    "v_add_f32 v19, v19, v35\n"
    "v_add_f32 v20, v20, v36\n"
    "v_add_f32 v21, v21, v37\n"
    "v_add_f32 v22, v22, v38\n"
    "v_add_f32 v23, v23, v39\n"
    "s_waitcnt lgkmcnt(8)\n"
    "v_add_f32 v16, v16, v40\n"
    "v_add_f32 v17, v17, v41\n"
    "v_add_f32 v18, v18, v42\n"
    "v_add_f32 v19, v19, v43\n"
    "v_add_f32 v20, v20, v44\n"
    "v_add_f32 v21, v21, v45\n"
    "v_add_f32 v22, v22, v46\n"
    "v_add_f32 v23, v23, v47\n"
    "s_waitcnt lgkmcnt(6)\n"
    "v_add_f32 v16, v16, v48\n"
    "v_add_f32 v17, v17, v49\n"
    "v_add_f32 v18, v18, v50\n"
    "v_add_f32 v19, v19, v51\n"
    "v_add_f32 v20, v20, v52\n"
    "v_add_f32 v21, v21, v53\n"
    "v_add_f32 v22, v22, v54\n"
    "v_add_f32 v23, v23, v55\n"
    "s_waitcnt lgkmcnt(4)\n"
    "v_add_f32 v16, v16, v56\n"
    "v_add_f32 v17, v17, v57\n"
    "v_add_f32 v18, v18, v58\n"
    "v_add_f32 v19, v19, v59\n"
    "v_add_f32 v20, v20, v60\n"
    "v_add_f32 v21, v21, v61\n"
    "v_add_f32 v22, v22, v62\n"
    "v_add_f32 v23, v23, v63\n"
    "s_waitcnt lgkmcnt(2)\n"
    "v_add_f32 v16, v16, v64\n"
    "v_add_f32 v17, v17, v65\n"
    "v_add_f32 v18, v18, v66\n"
    "v_add_f32 v19, v19, v67\n"
    "v_add_f32 v20, v20, v68\n"
    "v_add_f32 v21, v21, v69\n"
    "v_add_f32 v22, v22, v70\n"
    "v_add_f32 v23, v23, v71\n"
    "s_waitcnt lgkmcnt(0)\n"
    "v_add_f32 v16, v16, v72\n"
    "v_add_f32 v17, v17, v73\n"
    "v_add_f32 v18, v18, v74\n"
    "v_add_f32 v19, v19, v75\n"
    "v_add_f32 v20, v20, v76\n"
    "v_add_f32 v21, v21, v77\n"
    "v_add_f32 v22, v22, v78\n"
    "v_add_f32 v23, v23, v79\n"
    "s_waitcnt vmcnt(0)\n"
    "v_fma_f32 v16, v16, v84, v92\n"
    "v_fma_f32 v17, v17, v85, v93\n"
    "v_fma_f32 v18, v18, v86, v94\n"
    "v_fma_f32 v19, v19, v87, v95\n"
    "v_fma_f32 v20, v20, v88, v96\n"
    "v_fma_f32 v21, v21, v89, v97\n"
    "v_fma_f32 v22, v22, v90, v98\n"
    "v_fma_f32 v23, v23, v91, v99\n"
    "v_cvt_pk_bf16_f32 v40, v16, v17\n"
    "v_cvt_pk_bf16_f32 v41, v18, v19\n"
    "v_cvt_pk_bf16_f32 v42, v20, v21\n"
    "v_cvt_pk_bf16_f32 v43, v22, v23\n"
    "buffer_store_dwordx2 v[40:41], v11, s[40:43], 0 offen offset:0\n"
    "buffer_store_dwordx2 v[42:43], v11, s[40:43], 0 offen offset:32\n"
    "s_waitcnt vmcnt(0)\n"
    "L_code_end:\n"
    :: "s"(__builtin_amdgcn_kernarg_segment_ptr()), "s"(blockIdx.x), "v"(threadIdx.x)
    : "v1", "v2", "v3", "v4", "v5", "v6", "v7", "v8", "v9", "v10", "v11", "v12", "v13", "v14", "v15", "v16", "v17", "v18", "v19", "v20", "v21", "v22", "v23", "v24", "v25", "v26", "v27", "v28", "v29", "v30", "v31", "v32", "v33", "v34", "v35", "v36", "v37", "v38", "v39", "v40", "v41", "v42", "v43", "v44", "v45", "v46", "v47", "v48", "v49", "v50", "v51", "v52", "v53", "v54", "v55", "v56", "v57", "v58", "v59", "v60", "v61", "v62", "v63", "v64", "v65", "v66", "v67", "v68", "v69", "v70", "v71", "v72", "v73", "v74", "v75", "v76", "v77", "v78", "v79", "v80", "v81", "v82", "v83", "v84", "v85", "v86", "v87", "v88", "v89", "v90", "v91", "v92", "v93", "v94", "v95", "v96", "v97", "v98", "v99", "v100", "v101", "v102", "v103", "v104", "v105", "v106", "v107", "v108", "v109", "v110", "v111", "v112", "v113", "v114", "v115", "v116", "v117", "v118", "v119", "v120", "v121", "v122", "v123", "v124", "v125", "v126", "v127", "a0", "a1", "a2", "a3", "a4", "a5", "a6", "a7", "a8", "a9", "a10", "a11", "a12", "a13", "a14", "a15", "a16", "a17", "a18", "a19", "a20", "a21", "a22", "a23", "a24", "a25", "a26", "a27", "a28", "a29", "a30", "a31", "a32", "a33", "a34", "a35", "a36", "a37", "a38", "a39", "a40", "a41", "a42", "a43", "a44", "a45", "a46", "a47", "a48", "a49", "a50", "a51", "a52", "a53", "a54", "a55", "a56", "a57", "a58", "a59", "a60", "a61", "a62", "a63", "a64", "a65", "a66", "a67", "a68", "a69", "a70", "a71", "a72", "a73", "a74", "a75", "a76", "a77", "a78", "a79", "a80", "a81", "a82", "a83", "a84", "a85", "a86", "a87", "a88", "a89", "a90", "a91", "a92", "a93", "a94", "a95", "a96", "a97", "a98", "a99", "a100", "a101", "a102", "a103", "a104", "a105", "a106", "a107", "a108", "a109", "a110", "a111", "a112", "a113", "a114", "a115", "a116", "a117", "a118", "a119", "a120", "a121", "a122", "a123", "a124", "a125", "a126", "a127", "s14", "s15", "s16", "s17", "s18", "s19", "s20", "s21", "s22", "s23", "s24", "s25", "s26", "s27", "s28", "s29", "s30", "s31", "s32", "s33", "s34", "s35", "s36", "s37", "s38", "s39", "s40", "s41", "s42", "s43", "s44", "s45", "s46", "s47", "s48", "s49", "s50", "s51", "s52", "s53", "s54", "s55", "s56", "s57", "s58", "s59", "s60", "s61", "s62", "s63", "s64", "s65", "s66", "s67", "s68", "s69", "s70", "s71", "s72", "s73", "s74", "s75", "s76", "s77", "s78", "s79", "s80", "s81", "s82", "s83", "s84", "s85", "s86", "s87", "s88", "s89", "s90", "s91", "s92", "s93", "s94", "s95", "s96", "s97", "s98", "s99", "vcc", "scc", "memory");
}

extern "C" void kernel_launch(void* const* d_in, const int* in_sizes, int n_in, void* d_out, int out_size, void* d_ws, size_t ws_size, hipStream_t stream) {
  (void)in_sizes; (void)n_in; (void)out_size; (void)ws_size;
  char* ws = (char*)d_ws;
  (void)hipFuncSetAttribute((const void*)kf_attn, hipFuncAttributeMaxDynamicSharedMemorySize, 163840);
  kf_attn<<<256, 512, 163840, stream>>>(d_in[1], d_in[3], d_in[4], d_in[2], ws, d_in[5], d_in[7], d_in[8], d_in[9], d_in[0], d_in[10], d_in[11], d_out);
}
```
